```python
import torch

def _softmax(x, axis=-1):
    return torch.softmax(x, dim=axis)

def _gelu(x):
    return torch.nn.functional.gelu(x, approximate='tanh')

def _silu(x):
    return torch.nn.functional.silu(x)

def _sigmoid(x):
    return torch.sigmoid(x)

def _layernorm(x, axis=-1, eps=1e-05):
    m = x.mean(dim=axis, keepdim=True)
    v = x.var(dim=axis, keepdim=True, unbiased=False)
    return (x - m) / torch.sqrt(v + eps)

def _scatter_add(tgt, idx, src, axis=0):
    return tgt.clone().index_add_(axis, idx, src)

def _segment_sum(src, seg, n):
    out = torch.zeros((n,) + src.shape[1:], dtype=src.dtype)
    return out.index_add_(0, seg, src)

def _pad(x, pw):
    flat = tuple((v for pair in reversed(pw) for v in pair))
    return torch.nn.functional.pad(x, flat)
kernel_name = 'standalone__video_video_swin__vision'

def setup_inputs():

    _w = lambda a: (lambda t: t.to(torch.bfloat16) if t.is_floating_point() else t)(torch.as_tensor(a))
    T = 4
    H = W = 224 // 8 * 8
    C, win = (96, 8)
    return {'x': _w((lambda *s: torch.randn(*s, dtype=torch.bfloat16) * 0.1)(1, T, H, W, C)), 'wqkv_t': _w((lambda *s: torch.randn(*s, dtype=torch.bfloat16) * 0.1)(C, 3 * C)), 'wo_t': _w((lambda *s: torch.randn(*s, dtype=torch.bfloat16) * 0.1)(C, C)), 'wqkv_s': _w((lambda *s: torch.randn(*s, dtype=torch.bfloat16) * 0.1)(C, 3 * C)), 'wo_s': _w((lambda *s: torch.randn(*s, dtype=torch.bfloat16) * 0.8)(C, C))}

def reference(x, wqkv_t, wo_t, wqkv_s, wo_s):
    B, T, H, W, C = x.shape
    win = 8
    scale = C ** (-0.5)
    h = _layernorm(x, axis=-1)
    ht = (lambda x, *d: x.permute(*d))(h, 0, 2, 3, 1, 4).reshape(B * H * W, T, C)
    qkv = ht @ wqkv_t
    q, k, v = (qkv[..., :C].clone(), qkv[..., C:2 * C].clone(), qkv[..., 2 * C:].clone())
    p = _softmax(torch.einsum('btc,bsc->bts', q, k) * scale, axis=-1)
    ot = torch.einsum('bts,bsc->btc', p, v) @ wo_t
    ot = (lambda x, *d: x.permute(*d))(ot.reshape(B, H, W, T, C), 0, 3, 1, 2, 4)
    x = x + ot
    h = _layernorm(x, axis=-1)
    nh, nw = (H // win, W // win)
    hs = h.reshape(B * T, nh, win, nw, win, C)
    hw = (lambda x, *d: x.permute(*d))(hs, 0, 1, 3, 2, 4, 5).reshape(B * T * nh * nw, win * win, C)
    qkv = hw @ wqkv_s
    q, k, v = (qkv[..., :C].clone(), qkv[..., C:2 * C].clone(), qkv[..., 2 * C:].clone())
    p = _softmax(torch.einsum('bqc,bkc->bqk', q, k) * scale, axis=-1)
    os = torch.einsum('bqk,bkc->bqc', p, v) @ wo_s
    os = (lambda x, *d: x.permute(*d))(os.reshape(B * T, nh, nw, win, win, C), 0, 1, 3, 2, 4, 5)
    os = os.reshape(B, T, H, W, C)
    return x + os
```

```cpp
#include <hip/hip_runtime.h>
#include <hip/hip_bf16.h>
#include <stdint.h>

#if defined(__HIP_DEVICE_COMPILE__)
#define NOPK __attribute__((target("no-packed-fp32-ops")))
#else
#define NOPK
#endif
#define DI __device__ __forceinline__ NOPK

typedef float f32x16 __attribute__((ext_vector_type(16)));
typedef float f32x4 __attribute__((ext_vector_type(4)));
typedef unsigned u32x4 __attribute__((ext_vector_type(4)));
typedef unsigned u32x2 __attribute__((ext_vector_type(2)));
typedef __bf16 bf16x8 __attribute__((ext_vector_type(8)));
typedef __bf16 bf16x2 __attribute__((ext_vector_type(2)));
typedef short s16x4 __attribute__((ext_vector_type(4)));
#define LAS __attribute__((address_space(3)))

constexpr int WF_OFF = 0;
constexpr int SLOT_OFF = 73728;
constexpr int SLOT_B = 9216;
constexpr int R_OFF = SLOT_OFF + 8 * SLOT_B;
constexpr int RP_OFF = R_OFF + 2048;
constexpr int CNT_OFF = RP_OFF + 1024;
constexpr int DUM_OFF = CNT_OFF + 128;
constexpr int LDS_BYTES = DUM_OFF + 9216;

constexpr unsigned RS_IN = 224u * 224u * 192u;
constexpr unsigned ROWB = 224u * 192u;
constexpr unsigned RS_OUT = 2u * ROWB;

DI f32x16 mfma32(u32x4 a, u32x4 b, f32x16 c) {
  return __builtin_amdgcn_mfma_f32_32x32x16_bf16(__builtin_bit_cast(bf16x8, a), __builtin_bit_cast(bf16x8, b), c, 0, 0, 0);
}
DI f32x4 mfma4(unsigned a0, unsigned a1, unsigned b0, unsigned b1, f32x4 c) {
  u32x2 a = {a0, a1}, b = {b0, b1};
  return __builtin_amdgcn_mfma_f32_4x4x4bf16_1k(__builtin_bit_cast(s16x4, a), __builtin_bit_cast(s16x4, b), c, 0, 0, 0);
}
DI unsigned pk(float lo, float hi) {
  bf16x2 p = {(__bf16)lo, (__bf16)hi};
  return __builtin_bit_cast(unsigned, p);
}
#define PACKS(x, s) u32x4{pk((x)[8 * (s)], (x)[8 * (s) + 1]), pk((x)[8 * (s) + 2], (x)[8 * (s) + 3]), \
                          pk((x)[8 * (s) + 4], (x)[8 * (s) + 5]), pk((x)[8 * (s) + 6], (x)[8 * (s) + 7])}
DI float fbits(unsigned u) { return __builtin_bit_cast(float, u); }
DI unsigned ubits(float f) { return __builtin_bit_cast(unsigned, f); }
DI float merge_add(float a, float b) {
  auto r = __builtin_amdgcn_permlane32_swap(ubits(a), ubits(b), false, false);
  return fbits(r[0]) + fbits(r[1]);
}
DI float xhalf_add(float a) { return merge_add(a, a); }
DI float xhalf_max(float a) {
  auto r = __builtin_amdgcn_permlane32_swap(ubits(a), ubits(a), false, false);
  return __builtin_fmaxf(fbits(r[0]), fbits(r[1]));
}
template <int I> DI float quad_bcast(float v) {
  return fbits((unsigned)__builtin_amdgcn_mov_dpp((int)ubits(v), I * 0x55, 0xf, 0xf, true));
}
DI u32x4 lds_ld(const char* smem, unsigned off) { return *(const u32x4*)(smem + off); }
DI void lds_st(char* smem, unsigned off, u32x4 v) { *(u32x4*)(smem + off) = v; }

DI void dma16(unsigned lds_dst, const char* sbase, unsigned voff) {
  unsigned keep;
  asm volatile("s_mov_b32 %0, m0\n\ts_mov_b32 m0, %1\n\ts_nop 0\n\tglobal_load_lds_dwordx4 %2, %3\n\ts_mov_b32 m0, %0"
               : "=&s"(keep) : "s"(lds_dst), "v"(voff), "s"(sbase) : "memory");
}
DI void wait_vm0() { asm volatile("s_waitcnt vmcnt(0)" ::: "memory"); }
DI void wait_lgkm0() { asm volatile("s_waitcnt lgkmcnt(0)" ::: "memory"); }
DI void wg_barrier() { asm volatile("s_waitcnt lgkmcnt(0)\n\ts_barrier" ::: "memory"); }

DI void grp_arrive(unsigned lds_addr) {
  unsigned a, o;
  asm volatile("v_mov_b32 %0, %2\n\tv_mov_b32 %1, 1\n\ts_waitcnt lgkmcnt(0)\n\ts_mov_b64 exec, 1\n\tds_add_u32 %0, %1\n\ts_mov_b64 exec, -1"
               : "=&v"(a), "=&v"(o) : "s"(lds_addr) : "memory");
}
DI void grp_wait(unsigned lds_addr, unsigned target) {
  unsigned a, v, sv;
  asm volatile("v_mov_b32 %0, %3\n"
               "1:\n\tds_read_b32 %1, %0\n\ts_waitcnt lgkmcnt(0)\n\tv_readfirstlane_b32 %2, %1\n\ts_nop 1\n\ts_cmp_ge_u32 %2, %4\n\t"
               "s_cbranch_scc1 2f\n\ts_sleep 1\n\ts_branch 1b\n2:"
               : "=&v"(a), "=&v"(v), "=&s"(sv) : "s"(lds_addr), "s"(target) : "memory", "scc");
}
DI void grp_wait_peeked(unsigned lds_addr, unsigned target, unsigned peeked) {
  unsigned a, v, sv;
  asm volatile("v_readfirstlane_b32 %2, %5\n\tv_mov_b32 %0, %3\n\ts_nop 0\n\ts_cmp_ge_u32 %2, %4\n\ts_cbranch_scc1 2f\n"
               "1:\n\tds_read_b32 %1, %0\n\ts_waitcnt lgkmcnt(0)\n\tv_readfirstlane_b32 %2, %1\n\ts_nop 1\n\ts_cmp_ge_u32 %2, %4\n\t"
               "s_cbranch_scc1 2f\n\ts_sleep 1\n\ts_branch 1b\n2:"
               : "=&v"(a), "=&v"(v), "=&s"(sv) : "s"(lds_addr), "s"(target), "v"(peeked) : "memory", "scc");
}
template <int PA, int PB> DI void set_prio(int grp) {
  asm volatile("s_cmp_eq_u32 %0, 0\n\ts_cbranch_scc1 1f\n\ts_setprio %2\n\ts_branch 2f\n1:\n\ts_setprio %1\n2:"
               :: "s"(grp), "i"(PA), "i"(PB) : "scc");
}

#define STAMP0(i)
#define STAMPL(i)
#define STAMP_DECL
#define STAMP_OUT
DI int swap23(int n) { return (n & 0x13) | ((n & 4) << 1) | ((n & 8) >> 1); }

#define SB() __builtin_amdgcn_sched_barrier(0)
#define MEMB() asm volatile("" ::: "memory")
#define UNROLL _Pragma("unroll")
#define PMODE 1
#define PRIO_DENSE() set_prio<3, 2>(grp)
#define PRIO_PURE() set_prio<1, 0>(grp)

__global__ __launch_bounds__(512, 2) NOPK void swin_kernel(const char* __restrict__ x, const char* __restrict__ wqkv_t,
                                                           const char* __restrict__ wo_t, const char* __restrict__ wqkv_s,
                                                           const char* __restrict__ wo_s, char* __restrict__ out, unsigned long long* dbg) {
  __shared__ __attribute__((aligned(16))) char smem[LDS_BYTES];
  const unsigned lds0 = (unsigned)(size_t)smem;
  const int lane = threadIdx.x & 63;
  const int wave = __builtin_amdgcn_readfirstlane(threadIdx.x >> 6);
  const int grp = wave >> 2, w = wave & 3;
  const int col = lane & 31, h = lane >> 5, p = col >> 2, t = col & 3;
  const unsigned l16 = (unsigned)lane * 16u;

  STAMP_DECL
  STAMP0(0);
  const unsigned m1in = lane < 32 ? l16 + 1024u : ((l16 - 512u) ^ 16u) + RS_IN;
  const unsigned m1out = lane < 32 ? l16 + 1024u : ((l16 - 512u) ^ 16u) + RS_OUT;
#define POFF(i, lb, m1) ((i) == 0 ? (lb) : (i) == 1 ? (m1) : (i) == 2 ? ((lb) ^ 16u) : (i) == 3 ? ((lb) ^ 32u) : (i) == 4 ? ((m1) ^ 32u) : ((lb) ^ 48u))
#define PUNI(i, RS) ((i) == 0 ? 0u : (i) == 1 ? 0u : (i) == 2 ? (RS) + 512u : (i) == 3 ? 2u * (RS) : (i) == 4 ? 2u * (RS) : 3u * (RS) + 512u)
#define OPAQUE(v) asm volatile("" : "+v"(v))
  const __amdgpu_buffer_rsrc_t xrs = __builtin_amdgcn_make_buffer_rsrc((void*)x, 0, 38535168, 0x00020000);
  int win = blockIdx.x + 256 * grp;
  u32x4 xf[2][6];
  unsigned xb0;
  { int wy = win / 28, wx = win - wy * 28; xb0 = (unsigned)((wy * 8 + 2 * w) * 224 + wx * 8) * 192u; }
#define XLOAD(n) xf[(n) / 6][(n) % 6] = __builtin_amdgcn_raw_buffer_load_b128(xrs, POFF((n) % 6, l16, m1in), xb0 + ((n) / 6) * ROWB + PUNI((n) % 6, RS_IN), 0)

  if (threadIdx.x < 32) *(unsigned*)(smem + CNT_OFF + threadIdx.x * 4) = 0u;

  {
    unsigned qo[3], qf[3], vv[3];
    UNROLL for (int m = 0; m < 3; ++m) {
      int idx = m * 64 + lane, row = idx / 24, qq = idx - row * 24;
      int c = qq ^ (row >> 1) ^ ((w & 1) << 2);
      qo[m] = (unsigned)(row * 576 + c * 16);
      qf[m] = (unsigned)(row * 576 + (c ^ 4) * 16);
      int row2 = idx / 12, q2 = idx - row2 * 12;
      vv[m] = w < 2 ? (unsigned)(row2 * 576 + 384 + (q2 ^ (row2 >> 2)) * 16) : (unsigned)(m * 1024) + l16;
    }
    const unsigned vstep = w < 2 ? 16 * 576 : 3072;
    const unsigned vsrc0 = w < 2 ? w * 48 * 576 : (w - 2) * 9216;
    const unsigned vdst0 = w < 2 ? WF_OFF + 18432 + w * 9216 : WF_OFF + (w - 2) * 9216;
#define QPIECES(g) UNROLL for (int i = 0; i < 9; ++i) dma16(lds0 + SLOT_OFF + (g) * 36864 + w * 9216 + i * 1024, ((g) ? wqkv_s : wqkv_t) + w * 24 * 576 + (i / 3) * 8 * 576, (i / 3 == 1) ? qf[i % 3] : qo[i % 3])
#define VPIECES(g) UNROLL for (int i = 0; i < 9; ++i) dma16(lds0 + vdst0 + (g) * 36864 + i * 1024, (w < 2 ? ((g) ? wqkv_s : wqkv_t) : ((g) ? wo_s : wo_t)) + vsrc0 + (i / 3) * vstep, vv[i % 3])
    if (!grp) {
      QPIECES(0);
      wg_barrier();
      UNROLL for (int n = 0; n < 12; ++n) XLOAD(n);
      asm volatile("s_waitcnt vmcnt(12)" ::: "memory");
      grp_arrive(lds0 + CNT_OFF + 8);
    } else {
      VPIECES(0);
      wg_barrier();
      wait_vm0();
      grp_arrive(lds0 + CNT_OFF + 8);
      QPIECES(1);
      VPIECES(1);
      wait_vm0();
      grp_arrive(lds0 + CNT_OFF + 32 + 8);
      UNROLL for (int n = 0; n < 12; ++n) XLOAD(n);
      MEMB();
    }
#undef QPIECES
#undef VPIECES
  }
  const unsigned slot = SLOT_OFF + wave * SLOT_B;
  const unsigned cbase = CNT_OFF + grp * 32;
  const unsigned cntA = cbase, cntB = cbase + 4;
  const unsigned QK = SLOT_OFF + grp * 36864;
  const unsigned RA = WF_OFF + grp * 36864, RB = RA + 18432;
  if (!grp) __builtin_amdgcn_s_setprio(1);
  grp_wait(lds0 + cbase + 8, grp ? 4 : 8);
  STAMP0(1);

  const float inv96 = 1.0f / 96.0f;
  {
    const int pn = swap23(col);
    const int role = (w + 2 * grp) & 3;
    const unsigned rp = RP_OFF + grp * 512;
    if (role < 3) {
      unsigned qb[4];
      UNROLL for (int e = 0; e < 4; ++e) qb[e] = QK + (unsigned)(pn * 384 + (((2 * e) | h) ^ ((pn >> 1) & 7)) * 16);
#define QKF(rt, c2) lds_ld(smem, qb[(c2) & 3] + (rt) * 12288 + ((c2) >> 2) * 128)
      const unsigned ro = (unsigned)role * 12288u;
#define QKR(c2) lds_ld(smem, qb[(c2) & 3] + ro + ((c2) >> 2) * 128)
      u32x4 bb[6], af[2][6];
      UNROLL for (int kk = 0; kk < 6; ++kk) bb[kk] = QKR(kk);
      UNROLL for (int kk = 0; kk < 6; ++kk) af[0][kk] = QKF(0, 6 + kk);
      f32x16 d2 = {};
      UNROLL for (int ct = 0; ct < 3; ++ct) {
        UNROLL for (int kk = 0; kk < 6; ++kk) af[(ct + 1) & 1][kk] = ct < 2 ? QKF(ct + 1, 6 + kk) : QKF(0, kk);
        SB();
        UNROLL for (int kk = 0; kk < 6; ++kk) d2 = mfma32(af[ct & 1][kk], bb[kk], d2);
        SB();
      }
      UNROLL for (int kk = 0; kk < 6; ++kk) bb[kk] = QKR(6 + kk);
      f32x16 DA[3];
      UNROLL for (int rt = 0; rt < 3; ++rt) {
        if (rt < 2) { UNROLL for (int kk = 0; kk < 6; ++kk) af[rt & 1][kk] = QKF(rt + 1, kk); }
        SB();
        f32x16 d = {};
        UNROLL for (int kk = 0; kk < 6; ++kk) d = mfma32(af[(rt + 1) & 1][kk], bb[kk], d);
        DA[rt] = d;
        if (rt == 0) {
          float rs = 0.f;
          UNROLL for (int i = 0; i < 16; ++i) rs += d2[i];
          rs = xhalf_add(rs);
          if (h == 0) *(float*)(smem + rp + (32 * role + pn) * 4) = rs * inv96;
        }
        if (rt == 1) grp_arrive(lds0 + cbase + 12);
        SB();
      }
      float csA = 0.f;
      UNROLL for (int rt = 0; rt < 3; ++rt)
        UNROLL for (int i = 0; i < 16; ++i) csA += DA[rt][i];
      csA = xhalf_add(csA);
      grp_wait(lds0 + cbase + 12, 4);
      const float fA = 0.10206207261596575f * 1.4426950408889634f;
      float tot = 0.f;
      UNROLL for (int rt = 0; rt < 3; ++rt)
        UNROLL for (int g = 0; g < 4; ++g) {
          int c = 32 * rt + 16 * (g >> 1) + 8 * h + 4 * (g & 1);
          f32x4 a = *(const f32x4*)(smem + rp + c * 4);
          UNROLL for (int i = 0; i < 4; ++i) { DA[rt][4 * g + i] -= a[i]; tot += a[i]; }
        }
      tot = xhalf_add(tot);
      const float cadd = fA * inv96 * (tot - csA);
      UNROLL for (int rt = 0; rt < 3; ++rt) {
        UNROLL for (int i = 0; i < 16; ++i) DA[rt][i] = __builtin_fmaf(DA[rt][i], fA, cadd);
        lds_st(smem, RA + (role * 6 + 2 * rt) * 1024 + l16, PACKS(DA[rt], 0));
        lds_st(smem, RA + (role * 6 + 2 * rt + 1) * 1024 + l16, PACKS(DA[rt], 1));
      }
      grp_arrive(lds0 + cbase + 16);
#undef QKF
#undef QKR
    } else {
      u32x4 bfr[3][6];
      {
        int b = (lane >> 4) & 1, q = (lane & 15) >> 2, pp = lane & 3;
        const unsigned tb = RA + (unsigned)((8 * h + q) * 192 + (16 * b + 8 * (pp & 1) + 4 * (pp >> 1)) * 2);
        UNROLL for (int ct = 0; ct < 3; ++ct)
          UNROLL for (int kk = 0; kk < 6; ++kk) {
            s16x4 lo = __builtin_amdgcn_ds_read_tr16_b64_v4i16((LAS s16x4*)(smem + tb + kk * 3072 + ct * 64));
            s16x4 hi = __builtin_amdgcn_ds_read_tr16_b64_v4i16((LAS s16x4*)(smem + tb + kk * 3072 + 768 + ct * 64));
            u32x2 l2 = __builtin_bit_cast(u32x2, lo), h2 = __builtin_bit_cast(u32x2, hi);
            bfr[ct][kk] = u32x4{l2[0], l2[1], h2[0], h2[1]};
          }
      }
      grp_arrive(lds0 + cbase + 12);
      unsigned vb[2];
      UNROLL for (int e = 0; e < 2; ++e) vb[e] = RB + (unsigned)(pn * 192 + (((2 * e) | h) ^ ((pn >> 2) & 3)) * 16);
#define VF(rt, kk) lds_ld(smem, vb[(kk) & 1] + (rt) * 6144 + ((kk) >> 1) * 64)
      u32x4 PB[3][6];
      u32x4 av[6];
      UNROLL for (int kk = 0; kk < 6; ++kk) av[kk] = VF(0, kk);
      UNROLL for (int ct = 0; ct < 3; ++ct) {
        f32x16 DB[3];
        float cs = 0.f;
        UNROLL for (int rt = 0; rt < 3; ++rt) {
          const int n = 3 * ct + rt;
          f32x16 d = {};
          UNROLL for (int kk = 0; kk < 6; ++kk) {
            d = mfma32(av[kk], bfr[ct][kk], d);
            if (n < 8) av[kk] = VF((rt + 1) % 3, kk);
            SB();
          }
          DB[rt] = d;
          if (rt > 0) { UNROLL for (int i = 0; i < 16; ++i) cs += DB[rt - 1][i]; }
          SB();
        }
        UNROLL for (int i = 0; i < 16; ++i) cs += DB[2][i];
        const float cm = xhalf_add(cs) * inv96;
        UNROLL for (int rt = 0; rt < 3; ++rt) {
          UNROLL for (int i = 0; i < 16; ++i) DB[rt][i] -= cm;
          PB[ct][2 * rt] = PACKS(DB[rt], 0); PB[ct][2 * rt + 1] = PACKS(DB[rt], 1);
        }
      }
      MEMB();
      UNROLL for (int ct = 0; ct < 3; ++ct)
        UNROLL for (int ks = 0; ks < 6; ++ks) lds_st(smem, RB + (ct * 6 + ks) * 1024 + l16, PB[ct][ks]);
      grp_arrive(lds0 + cbase + 16);
      grp_wait(lds0 + cbase + 12, 4);
#undef VF
    }
  }
  STAMP0(2);

  const unsigned xa_e = slot + (unsigned)((t * 96 + 12 * p + (h ^ t)) * 16);
  const unsigned xa_o = slot + (unsigned)((t * 96 + 12 * p + ((2 + h) ^ t)) * 16);
  const unsigned t1adj = t < 2 ? 6144u : (unsigned)-3072;
  const unsigned sl16 = slot + l16;
#define XA(ks) ((((ks) & 1) ? xa_o : xa_e) + 64 * ((ks) >> 1))
  const bool own = (t == w);
  const unsigned exo = (unsigned)(h * 384 + p * 48 + ((t ^ w) - 1) * 16);
  const unsigned ex_w = own ? (unsigned)(DUM_OFF + h * 128 + ((3 * (p & 6) + 6 + (p & 1)) & 7) * 16)
                            : SLOT_OFF + (grp * 4 + t) * SLOT_B + exo;
  const unsigned ex_r = slot + exo;
  const unsigned long long notown = ~(0x1111111111111111ull << w);
  const unsigned rbase = R_OFF + grp * 1024;
  unsigned wS = l16 + 36864u;
  asm volatile("" : "+v"(wS));
#define WLD(i) lds_ld(smem, ((i) < 36 ? l16 + (i) * 1024 : wS + ((i) - 36) * 1024))

  const unsigned ONES = 0x3F803F80u;
  const unsigned id0 = t == 0 ? 0x00003F80u : (t == 1 ? 0x3F800000u : 0u);
  const unsigned id1 = t == 2 ? 0x00003F80u : (t == 3 ? 0x3F800000u : 0u);
  unsigned msk[4];
  UNROLL for (int g = 0; g < 4; ++g) msk[g] = ((col >> 3) == g && h == (p & 1)) ? 0xffffffffu : 0u;
  const f32x4 z4 = {0.f, 0.f, 0.f, 0.f};
  const f32x16 zz = {};

  {
    UNROLL for (int i = 0; i < 6; ++i) lds_st(smem, sl16 + i * 1024, xf[0][i]);
    UNROLL for (int i = 0; i < 3; ++i) lds_st(smem, sl16 + 6144 + i * 1024, xf[1][i]);
    MEMB();
    UNROLL for (int ks = 0; ks < 6; ++ks) xf[0][ks] = lds_ld(smem, XA(ks));
    MEMB();
    UNROLL for (int i = 0; i < 3; ++i) lds_st(smem, sl16 + i * 1024, xf[1][3 + i]);
    MEMB();
    UNROLL for (int ks = 0; ks < 6; ++ks) xf[1][ks] = lds_ld(smem, t1adj + XA(ks));
    MEMB();
  }
  grp_wait(lds0 + CNT_OFF + 16, 4);

  u32x4 wb[4];
  wb[0] = WLD(0); wb[1] = WLD(1); wb[2] = WLD(2);
  u32x4 x1[2][6];
  UNROLL for (int a = 0; a < 2; ++a) UNROLL for (int i = 0; i < 6; ++i) x1[a][i] = wb[0];
  unsigned iter = 1;

#define T1_STAGE(OUTP, ob)                                                                                       \
  {                                                                                                              \
    f32x4 sm0 = z4, sm1 = z4, g0 = z4, g1 = z4;                                                                  \
    UNROLL for (int ks = 0; ks < 6; ++ks) {                                                                      \
      if (ks < 4) {                                                                                              \
        if (ks == 0) { UNROLL for (int k2 = 0; k2 < 6; ++k2) lds_st(smem, XA(k2), x1[0][k2]); MEMB(); }          \
        if (ks == 1) { UNROLL for (int k2 = 0; k2 < 6; ++k2) x1[0][k2] = lds_ld(smem, sl16 + k2 * 1024); MEMB(); } \
        if (ks == 2) { UNROLL for (int k2 = 0; k2 < 6; ++k2) lds_st(smem, XA(k2), x1[1][k2]); MEMB(); }          \
        if (ks == 3) { UNROLL for (int k2 = 0; k2 < 6; ++k2) x1[1][k2] = lds_ld(smem, sl16 + k2 * 1024); MEMB(); } \
      }                                                                                                          \
      UNROLL for (int c = 0; c < 2; ++c) {                                                                       \
        sm0 = mfma4(ONES, ONES, xf[0][ks][2 * c], xf[0][ks][2 * c + 1], sm0);                                    \
        sm1 = mfma4(ONES, ONES, xf[1][ks][2 * c], xf[1][ks][2 * c + 1], sm1);                                    \
        g0 = mfma4(xf[0][ks][2 * c], xf[0][ks][2 * c + 1], xf[0][ks][2 * c], xf[0][ks][2 * c + 1], g0);          \
        g1 = mfma4(xf[1][ks][2 * c], xf[1][ks][2 * c + 1], xf[1][ks][2 * c], xf[1][ks][2 * c + 1], g1);          \
      }                                                                                                          \
      SB();                                                                                                      \
    }                                                                                                            \
    UNROLL for (int i = 0; i < 18; ++i) {                                                                        \
      const int tt = i / 6, ks = i % 6;                                                                          \
      wb[(i + 3) & 3] = WLD(i + 3);                                                                              \
      acc[tt & 1][0] = mfma32(wb[i & 3], xf[0][ks], ks == 0 ? zz : acc[tt & 1][0]);                              \
      acc[tt & 1][1] = mfma32(wb[i & 3], xf[1][ks], ks == 0 ? zz : acc[tt & 1][1]);                              \
      if (tt > 0 && ks >= 1 && ks <= 4) {                                                                        \
        const int a = (ks - 1) >> 1, s = (ks - 1) & 1;                                                           \
        uf[a][2 * (tt - 1) + s] = PACKS(acc[(tt - 1) & 1][a], s);                                                \
      }                                                                                                          \
      if ((i % 3) != 2) {                                                                                        \
        const int n_ = 2 * (i / 3) + (i % 3);                                                                    \
        unsigned lb_ = l16, m1_ = m1out; OPAQUE(lb_); OPAQUE(m1_);                                               \
        __builtin_amdgcn_raw_buffer_store_b128(x1[n_ / 6][n_ % 6],                                               \
            __builtin_amdgcn_make_buffer_rsrc(out + (size_t)((ob) + (n_ / 6) * ROWB + PUNI(n_ % 6, RS_OUT)), 0, (OUTP) ? 0x40000 : 0, 0x00020000), \
            POFF(n_ % 6, lb_, m1_), 0, 0);                                                                       \
      }                                                                                                          \
      if (i == 2) grp_arrive(lds0 + cntB);                                                                       \
      if (i == 1) {                                                                                              \
        float q0a = (t & 1) ? g0[1] : g0[0], q0b = (t & 1) ? g0[3] : g0[2];                                      \
        float q1a = (t & 1) ? g1[1] : g1[0], q1b = (t & 1) ? g1[3] : g1[2];                                      \
        float q0 = (t & 2) ? q0b : q0a, q1 = (t & 2) ? q1b : q1a;                                                \
        float mean = merge_add(sm0[0], sm1[0]) * inv96;                                                          \
        float var = merge_add(q0, q1) * inv96 - mean * mean;                                                     \
        var = __builtin_fmaxf(var, 0.f);                                                                         \
        rr = __builtin_amdgcn_rsqf(var + 1e-5f);                                                                 \
      }                                                                                                          \
      SB();                                                                                                      \
    }                                                                                                            \
  }

  unsigned ob = 0;
  for (;;) {
    STAMPL(3);
    float rr;
    u32x4 uf[2][6];
    f32x16 acc[2][2];
    T1_STAGE((iter > 1), ob);
    STAMPL(4);
    PRIO_DENSE();
    u32x4 pt[2][2];
    float sum0 = 0.f, sum1 = 0.f, sq0 = 0.f, sq1 = 0.f;
    {
      f32x4 s0, s1, s0b, s1b;
      f32x16 vac[2][2];
      f32x16 oac[2];
      u32x4 vpk[2][2];
      float rs0, rs1, rs2, rs3, y0, y1, y2, y3, e0, e1, e2, e3;
      unsigned cbv;
#define O_STEP(tt)                                                                                               \
  UNROLL for (int a = 0; a < 2; ++a) {                                                                           \
    f32x16 o;                                                                                                    \
    UNROLL for (int g = 0; g < 4; ++g) {                                                                         \
      f32x4 r4 = mfma4(id0, id1, xf[a][2 * (tt) + (g >> 1)][2 * (g & 1)], xf[a][2 * (tt) + (g >> 1)][2 * (g & 1) + 1], z4); \
      UNROLL for (int e = 0; e < 4; ++e) o[4 * g + e] = r4[e];                                                   \
    }                                                                                                            \
    oac[a] = o;                                                                                                  \
  }                                                                                                              \
  oac[0] = mfma32(vpk[0][0], pt[0][0], oac[0]);                                                                  \
  oac[0] = mfma32(vpk[0][1], pt[0][1], oac[0]);                                                                  \
  oac[1] = mfma32(vpk[1][0], pt[1][0], oac[1]);                                                                  \
  oac[1] = mfma32(vpk[1][1], pt[1][1], oac[1]);
#define S2_STEP(tt, a, s)                                                                                        \
  UNROLL for (int i_ = 8 * (s); i_ < 8 * (s) + 8; ++i_) {                                                        \
    if ((a) == 0) { sum0 += oac[0][i_]; sq0 = __builtin_fmaf(oac[0][i_], oac[0][i_], sq0); }                     \
    else          { sum1 += oac[1][i_]; sq1 = __builtin_fmaf(oac[1][i_], oac[1][i_], sq1); }                     \
  }                                                                                                              \
  x1[a][2 * (tt) + (s)] = PACKS(oac[a], s);                                                                      \
  lds_st(smem, ex_w + ((a) * 6 + 2 * (tt) + (s)) * 768, x1[a][2 * (tt) + (s)]);
      UNROLL for (int j = 0; j < 18; ++j) {
        const int tt = j / 6, ks = j % 6;
        if (j < 15) wb[(j + 18 + 3) & 3] = WLD(j + 18 + 3);
        vac[tt & 1][0] = mfma32(xf[0][ks], wb[(j + 18) & 3], ks == 0 ? zz : vac[tt & 1][0]);
        vac[tt & 1][1] = mfma32(xf[1][ks], wb[(j + 18) & 3], ks == 0 ? zz : vac[tt & 1][1]);
        if (j == 0) { uf[0][4] = PACKS(acc[0][0], 0); uf[0][5] = PACKS(acc[0][0], 1); }
        if (j == 4) { MEMB(); cbv = *(const unsigned*)(smem + cntB); }
        if (j == 1) {
          uf[1][4] = PACKS(acc[0][1], 0); uf[1][5] = PACKS(acc[0][1], 1);
          SB();
          s0 = z4; s1 = z4; s0b = z4; s1b = z4;
          UNROLL for (int k2 = 0; k2 < 6; ++k2) {
            s0 = mfma4(xf[0][k2][0], xf[0][k2][1], uf[0][k2][0], uf[0][k2][1], s0);
            s1 = mfma4(xf[1][k2][0], xf[1][k2][1], uf[1][k2][0], uf[1][k2][1], s1);
            s0b = mfma4(xf[0][k2][2], xf[0][k2][3], uf[0][k2][2], uf[0][k2][3], s0b);
            s1b = mfma4(xf[1][k2][2], xf[1][k2][3], uf[1][k2][2], uf[1][k2][3], s1b);
          }
          rs0 = quad_bcast<0>(rr); rs1 = quad_bcast<1>(rr); rs2 = quad_bcast<2>(rr); rs3 = quad_bcast<3>(rr);
        }
        if (j == 2) {
          y0 = merge_add(s0[0] + s0b[0], s1[0] + s1b[0]) * rs0;
          y1 = merge_add(s0[1] + s0b[1], s1[1] + s1b[1]) * rs1;
        }
        if (j == 3) {
          y2 = merge_add(s0[2] + s0b[2], s1[2] + s1b[2]) * rs2;
          y3 = merge_add(s0[3] + s0b[3], s1[3] + s1b[3]) * rs3;
        }
        if (j == 4) {
          float mx = __builtin_fmaxf(__builtin_fmaxf(y0, y1), __builtin_fmaxf(y2, y3));
          float cc = -rr * mx;
          e0 = __builtin_amdgcn_exp2f(__builtin_fmaf(y0, rr, cc));
          e1 = __builtin_amdgcn_exp2f(__builtin_fmaf(y1, rr, cc));
          e2 = __builtin_amdgcn_exp2f(__builtin_fmaf(y2, rr, cc));
          e3 = __builtin_amdgcn_exp2f(__builtin_fmaf(y3, rr, cc));
        }
        if (j == 5) {
          float inv = __builtin_amdgcn_rcpf((e0 + e1) + (e2 + e3));
          unsigned p01 = pk(e0 * rs0 * inv, e1 * rs1 * inv);
          unsigned p23 = pk(e2 * rs2 * inv, e3 * rs3 * inv);
          auto a = __builtin_amdgcn_permlane32_swap(p01, p01, false, false);
          auto b = __builtin_amdgcn_permlane32_swap(p23, p23, false, false);
          UNROLL for (int ti = 0; ti < 2; ++ti)
            UNROLL for (int s = 0; s < 2; ++s)
              pt[ti][s] = u32x4{a[ti] & msk[2 * s], b[ti] & msk[2 * s], a[ti] & msk[2 * s + 1], b[ti] & msk[2 * s + 1]};
        }
        if (tt > 0) {
          if (ks == 0) { vpk[0][0] = PACKS(vac[(tt - 1) & 1][0], 0); vpk[0][1] = PACKS(vac[(tt - 1) & 1][0], 1); }
          if (ks == 1) {
            vpk[1][0] = PACKS(vac[(tt - 1) & 1][1], 0); vpk[1][1] = PACKS(vac[(tt - 1) & 1][1], 1);
            SB();
            O_STEP(tt - 1);
            if (tt == 1) {
              grp_wait_peeked(lds0 + cntB, 4 * iter, cbv);
            }
          }
          if (ks >= 2) { S2_STEP(tt - 1, (ks - 2) >> 1, (ks - 2) & 1); }
        }
        SB();
      }
      vpk[0][0] = PACKS(vac[0][0], 0); vpk[0][1] = PACKS(vac[0][0], 1);
      vpk[1][0] = PACKS(vac[0][1], 0); vpk[1][1] = PACKS(vac[0][1], 1);
      O_STEP(2);
      S2_STEP(2, 0, 0); S2_STEP(2, 0, 1); S2_STEP(2, 1, 0); S2_STEP(2, 1, 1);
#undef O_STEP
#undef S2_STEP
    }
    {
      float mean = merge_add(sum0, sum1) * inv96;
      float var = merge_add(sq0, sq1) * inv96 - mean * mean;
      var = __builtin_fmaxf(var, 0.f);
      float r2 = __builtin_amdgcn_rsqf(var + 1e-5f);
      *(float*)(smem + rbase + t * 256 + (h * 32 + 4 * p + w) * 4) = r2;
    }
    STAMPL(5);
    grp_arrive(lds0 + cntA);
    PRIO_PURE();
    grp_wait(lds0 + CNT_OFF + 32 + 16, 4);
    wb[0] = WLD(36); wb[1] = WLD(37); wb[2] = WLD(38);
    grp_wait(lds0 + cntA, 4 * iter);
    asm volatile("s_mov_b64 exec, %13\n\t"
                 "ds_read_b128 %0, %12\n\tds_read_b128 %1, %12 offset:768\n\tds_read_b128 %2, %12 offset:1536\n\t"
                 "ds_read_b128 %3, %12 offset:2304\n\tds_read_b128 %4, %12 offset:3072\n\tds_read_b128 %5, %12 offset:3840\n\t"
                 "ds_read_b128 %6, %12 offset:4608\n\tds_read_b128 %7, %12 offset:5376\n\tds_read_b128 %8, %12 offset:6144\n\t"
                 "ds_read_b128 %9, %12 offset:6912\n\tds_read_b128 %10, %12 offset:7680\n\tds_read_b128 %11, %12 offset:8448\n\t"
                 "s_mov_b64 exec, -1\n\ts_waitcnt lgkmcnt(0)"
                 : "+v"(x1[0][0]), "+v"(x1[0][1]), "+v"(x1[0][2]), "+v"(x1[0][3]), "+v"(x1[0][4]), "+v"(x1[0][5]),
                   "+v"(x1[1][0]), "+v"(x1[1][1]), "+v"(x1[1][2]), "+v"(x1[1][3]), "+v"(x1[1][4]), "+v"(x1[1][5])
                 : "v"(lds0 + ex_r), "s"(notown) : "memory");
    float rq[2];
    rq[0] = *(const float*)(smem + rbase + w * 256 + col * 4);
    rq[1] = *(const float*)(smem + rbase + w * 256 + (32 + col) * 4);
    const unsigned rka = rbase + w * 256 + h * 16;
#define RKLD(G) (*(const f32x4*)(smem + rka + (G) * 32))
    f32x4 rkb[2];
    STAMPL(6);
    const int nwin = win + 512;
    const bool more = nwin < 784;
    unsigned xo;
    { int nw = more ? nwin : win; int wy = nw / 28, wx = nw - wy * 28; xo = (unsigned)((wy * 8 + 2 * w) * 224 + wx * 8) * 192u; }
    const char* xb = x + xo;
    u32x4 pre[3];
    SB();
#define SW(k) ((k) < 18 ? 36 + (k) : ((k) < 54 ? 18 + (k) : (k) - 54))
    u32x4 pf[2][4];
    u32x4 vp[3][4];
    f32x16 sa[2][2];
    u32x4 uq[6];
    f32x16 ua[2];
    float mxv, ccv, l0, l1, invv;
#define U_STEP(q, i)                                                                                             \
  {                                                                                                              \
    const int tt_ = (i) / 6, ks_ = (i) % 6, k_ = 18 * (q) + (i);                                                 \
    wb[(k_ + 3) & 3] = WLD(SW(k_ + 3));                                                                          \
    ua[tt_ & 1] = mfma32(wb[k_ & 3], x1[q][ks_], ks_ == 0 ? zz : ua[tt_ & 1]);                                   \
    if (tt_ > 0 && (ks_ == 2 || ks_ == 3)) uq[2 * (tt_ - 1) + (ks_ - 2)] = PACKS(ua[(tt_ - 1) & 1], ks_ - 2);    \
  }
#define S_STEP(q, ks_)                                                                                           \
  {                                                                                                              \
    sa[q][0] = mfma32(x1[0][ks_], uq[ks_], (ks_) == 0 ? zz : sa[q][0]);                                          \
    sa[q][1] = mfma32(x1[1][ks_], uq[ks_], (ks_) == 0 ? zz : sa[q][1]);                                          \
    if ((ks_) == 1) uq[4] = PACKS(ua[0], 0);                                                                     \
    if ((ks_) == 2) uq[5] = PACKS(ua[0], 1);                                                                     \
  }
#define P1(q, G)                                                                                                 \
  {                                                                                                              \
    rkb[((G) + 1) & 1] = RKLD(((G) + 1) & 7);                                                                    \
    UNROLL for (int e = 0; e < 4; ++e) sa[q][(G) >> 2][4 * ((G) & 3) + e] *= rkb[(G) & 1][e];                    \
    const float m0_ = __builtin_fmaxf(sa[q][(G) >> 2][4 * ((G) & 3)], sa[q][(G) >> 2][4 * ((G) & 3) + 1]);       \
    mxv = (G) == 0 ? m0_ : __builtin_fmaxf(mxv, m0_);                                                            \
    mxv = __builtin_fmaxf(__builtin_fmaxf(mxv, sa[q][(G) >> 2][4 * ((G) & 3) + 2]), sa[q][(G) >> 2][4 * ((G) & 3) + 3]); \
  }
#define X1(q) { mxv = xhalf_max(mxv); ccv = -rq[q] * mxv; l0 = 0.f; l1 = 0.f; }
#define P2H(q, H)                                                                                                \
  {                                                                                                              \
    const int b_ = (H) >> 3, i_ = 2 * ((H) & 7), G_ = (H) >> 1;                                                  \
    if (((H) & 1) == 0) { MEMB(); rkb[(G_ + 1) & 1] = RKLD((G_ + 1) & 7); }                                      \
    const float ea = __builtin_amdgcn_exp2f(__builtin_fmaf(sa[q][b_][i_], rq[q], ccv));                          \
    const float eb = __builtin_amdgcn_exp2f(__builtin_fmaf(sa[q][b_][i_ + 1], rq[q], ccv));                      \
    l0 += ea; l1 += eb;                                                                                          \
    sa[q][b_][i_] = ea * rkb[G_ & 1][2 * ((H) & 1)]; sa[q][b_][i_ + 1] = eb * rkb[G_ & 1][2 * ((H) & 1) + 1];    \
  }
#define X2(q) { invv = __builtin_amdgcn_rcpf(xhalf_add(l0 + l1)); }
#define P3(q, F)                                                                                                 \
  {                                                                                                              \
    UNROLL for (int e = 0; e < 8; ++e) sa[q][(F) >> 1][8 * ((F) & 1) + e] *= invv;                               \
    pf[q][F] = PACKS(sa[q][(F) >> 1], (F) & 1);                                                                  \
  }
    UNROLL for (int i = 0; i < 18; ++i) {
      U_STEP(0, i);
      if (i >= 6) {
        unsigned lb_ = l16, m1_ = m1in; OPAQUE(lb_); OPAQUE(m1_);
        if (i < 12) dma16(lds0 + slot + (i - 6) * 1024, xb + PUNI(i - 6, RS_IN), POFF(i - 6, lb_, m1_));
        else if (i < 15) dma16(lds0 + slot + 6144 + (i - 12) * 1024, xb + ROWB + PUNI(i - 12, RS_IN), POFF(i - 12, lb_, m1_));
        else pre[i - 15] = __builtin_amdgcn_raw_buffer_load_b128(xrs, POFF(i - 12, lb_, m1_), xo + ROWB + PUNI(i - 12, RS_IN), 0);
      }
      SB();
    }
    UNROLL for (int ks = 0; ks < 6; ++ks) { S_STEP(0, ks); if (ks == 4) rkb[0] = RKLD(0); SB(); }
    STAMPL(7);
    PRIO_DENSE();
    UNROLL for (int i = 0; i < 18; ++i) {
      U_STEP(1, i);
      if (i >= 1 && i <= 4) { P1(0, 2 * (i - 1)); P1(0, 2 * (i - 1) + 1); }
      if (i == 5) X1(0);
      if (i >= 6) P2H(0, i - 6);
      SB();
    }
    UNROLL for (int ks = 0; ks < 6; ++ks) {
      S_STEP(1, ks);
      if (ks < 2) { P2H(0, 12 + 2 * ks); P2H(0, 13 + 2 * ks); }
      if (ks == 2) X2(0);
      if (ks == 3) { P3(0, 0); }
      if (ks == 4) { P3(0, 1); P3(0, 2); }
      if (ks == 5) { P3(0, 3); }
      SB();
    }
    STAMPL(8);
    {
      f32x16 v0, v1;
      UNROLL for (int j = 0; j < 18; ++j) {
        const int tt = j / 6, ks = j % 6, k = 36 + j;
        wb[(k + 3) & 3] = WLD(SW(k + 3));
        if (ks == 0 && tt > 0) {
          vp[tt - 1][0] = PACKS(v0, 0); vp[tt - 1][1] = PACKS(v0, 1);
          v0 = mfma32(x1[0][ks], wb[k & 3], zz);
          SB();
          vp[tt - 1][2] = PACKS(v1, 0); vp[tt - 1][3] = PACKS(v1, 1);
          v1 = mfma32(x1[1][ks], wb[k & 3], zz);
        } else {
          v0 = mfma32(x1[0][ks], wb[k & 3], ks == 0 ? zz : v0);
          v1 = mfma32(x1[1][ks], wb[k & 3], ks == 0 ? zz : v1);
        }
        if (j == 1) { P1(1, 0); P1(1, 1); P1(1, 2); }
        if (j == 2) { P1(1, 3); P1(1, 4); P1(1, 5); }
        if (j == 3) { P1(1, 6); P1(1, 7); X1(1); }
        if (j == 4) { P2H(1, 0); P2H(1, 1); }
        if (j == 5) { P2H(1, 2); P2H(1, 3); }
        if (j == 7) { P2H(1, 4); }
        if (j == 8) { P2H(1, 5); }
        if (j == 9) { P2H(1, 6); P2H(1, 7); }
        if (j == 10) { P2H(1, 8); P2H(1, 9); }
        if (j == 11) { P2H(1, 10); P2H(1, 11); }
        if (j == 13) { P2H(1, 12); P2H(1, 13); }
        if (j == 14) { P2H(1, 14); P2H(1, 15); }
        if (j == 15) { X2(1); }
        if (j == 16) { P3(1, 0); }
        if (j == 17) { P3(1, 1); P3(1, 2); }
        SB();
      }
      vp[2][0] = PACKS(v0, 0); vp[2][1] = PACKS(v0, 1);
      vp[2][2] = PACKS(v1, 0); vp[2][3] = PACKS(v1, 1);
    }
    STAMPL(9);
    PRIO_PURE();
    {
      f32x16 oc[2];
      UNROLL for (int n = 0; n < 6; ++n) {
        const int q = n / 3, tt = n % 3;
        f32x16 o;
        UNROLL for (int g = 0; g < 4; ++g) {
          f32x4 r4 = mfma4(id0, id1, x1[q][2 * tt + (g >> 1)][2 * (g & 1)], x1[q][2 * tt + (g >> 1)][2 * (g & 1) + 1], z4);
          UNROLL for (int e = 0; e < 4; ++e) o[4 * g + e] = r4[e];
        }
        o = mfma32(vp[tt][0], pf[q][0], o);
        o = mfma32(vp[tt][1], pf[q][1], o);
        if (n == 0) { P3(1, 3); }
        if (n > 0) {
          const int qp = (n - 1) / 3, tp = (n - 1) % 3;
          x1[qp][2 * tp] = PACKS(oc[(n - 1) & 1], 0);
          x1[qp][2 * tp + 1] = PACKS(oc[(n - 1) & 1], 1);
        }
        o = mfma32(vp[tt][2], pf[q][2], o);
        o = mfma32(vp[tt][3], pf[q][3], o);
        oc[n & 1] = o;
        SB();
      }
      { u32x4 t0 = wb[2], t1 = wb[3], t2 = wb[0]; wb[0] = t0; wb[1] = t1; wb[2] = t2; }
      STAMPL(10);
      wait_vm0();
      UNROLL for (int ks = 0; ks < 6; ++ks) xf[0][ks] = lds_ld(smem, XA(ks));
      MEMB();
      UNROLL for (int i = 0; i < 3; ++i) lds_st(smem, sl16 + i * 1024, pre[i]);
      MEMB();
      UNROLL for (int ks = 0; ks < 6; ++ks) xf[1][ks] = lds_ld(smem, t1adj + XA(ks));
      MEMB();
      x1[1][4] = PACKS(oc[1], 0);
      x1[1][5] = PACKS(oc[1], 1);
    }
    STAMPL(11);
    { int wy = win / 28, wx = win - wy * 28; ob = (unsigned)w * RS_IN + (unsigned)((wy * 8) * 224 + wx * 8) * 192u; }
    if (!more) {
      UNROLL for (int a = 0; a < 2; ++a) {
        UNROLL for (int ks = 0; ks < 6; ++ks) lds_st(smem, XA(ks), x1[a][ks]);
        MEMB();
        UNROLL for (int i = 0; i < 6; ++i) x1[a][i] = lds_ld(smem, sl16 + i * 1024);
        MEMB();
      }
      UNROLL for (int a = 0; a < 2; ++a)
        UNROLL for (int i = 0; i < 6; ++i) *(u32x4*)(out + (size_t)(ob + a * ROWB + PUNI(i, RS_OUT)) + POFF(i, l16, m1out)) = x1[a][i];
      STAMPL(12);
      break;
    }
    STAMPL(12);
    win = nwin;
    ++iter;
  }
  STAMP_OUT;
}

extern "C" void kernel_launch(void* const* d_in, const int* in_sizes, int n_in,
                              void* d_out, int out_size, void* d_ws, size_t ws_size,
                              hipStream_t stream) {
  swin_kernel<<<256, 512, 0, stream>>>((const char*)d_in[0], (const char*)d_in[1], (const char*)d_in[2],
                                       (const char*)d_in[3], (const char*)d_in[4], (char*)d_out, (unsigned long long*)d_ws);
}
```

```cpp
#define CODEPF
#define CODEPF_COND wave == 7
#include <hip/hip_runtime.h>
#include <hip/hip_bf16.h>
#include <stdint.h>

#if defined(__HIP_DEVICE_COMPILE__)
#define NOPK __attribute__((target("no-packed-fp32-ops")))
#else
#define NOPK
#endif
#define DI __device__ __forceinline__ NOPK

typedef float f32x16 __attribute__((ext_vector_type(16)));
typedef float f32x4 __attribute__((ext_vector_type(4)));
typedef unsigned u32x4 __attribute__((ext_vector_type(4)));
typedef unsigned u32x2 __attribute__((ext_vector_type(2)));
typedef __bf16 bf16x8 __attribute__((ext_vector_type(8)));
typedef __bf16 bf16x2 __attribute__((ext_vector_type(2)));
typedef short s16x4 __attribute__((ext_vector_type(4)));
#define LAS __attribute__((address_space(3)))

constexpr int WF_OFF = 0;
constexpr int SLOT_OFF = 73728;
constexpr int SLOT_B = 9216;
constexpr int R_OFF = SLOT_OFF + 8 * SLOT_B;
constexpr int RP_OFF = R_OFF + 2048;
constexpr int CNT_OFF = RP_OFF + 1024;
constexpr int DUM_OFF = CNT_OFF + 128;
constexpr int LDS_BYTES = DUM_OFF + 9216;

constexpr unsigned RS_IN = 224u * 224u * 192u;
constexpr unsigned ROWB = 224u * 192u;
constexpr unsigned RS_OUT = 2u * ROWB;

DI f32x16 mfma32(u32x4 a, u32x4 b, f32x16 c) {
  return __builtin_amdgcn_mfma_f32_32x32x16_bf16(__builtin_bit_cast(bf16x8, a), __builtin_bit_cast(bf16x8, b), c, 0, 0, 0);
}
DI f32x4 mfma4(unsigned a0, unsigned a1, unsigned b0, unsigned b1, f32x4 c) {
  u32x2 a = {a0, a1}, b = {b0, b1};
  return __builtin_amdgcn_mfma_f32_4x4x4bf16_1k(__builtin_bit_cast(s16x4, a), __builtin_bit_cast(s16x4, b), c, 0, 0, 0);
}
DI unsigned pk(float lo, float hi) {
  bf16x2 p = {(__bf16)lo, (__bf16)hi};
  return __builtin_bit_cast(unsigned, p);
}
#define PACKS(x, s) u32x4{pk((x)[8 * (s)], (x)[8 * (s) + 1]), pk((x)[8 * (s) + 2], (x)[8 * (s) + 3]), \
                          pk((x)[8 * (s) + 4], (x)[8 * (s) + 5]), pk((x)[8 * (s) + 6], (x)[8 * (s) + 7])}
DI float fbits(unsigned u) { return __builtin_bit_cast(float, u); }
DI unsigned ubits(float f) { return __builtin_bit_cast(unsigned, f); }
DI float merge_add(float a, float b) {
  auto r = __builtin_amdgcn_permlane32_swap(ubits(a), ubits(b), false, false);
  return fbits(r[0]) + fbits(r[1]);
}
DI float xhalf_add(float a) { return merge_add(a, a); }
DI float xhalf_max(float a) {
  auto r = __builtin_amdgcn_permlane32_swap(ubits(a), ubits(a), false, false);
  return __builtin_fmaxf(fbits(r[0]), fbits(r[1]));
}
template <int I> DI float quad_bcast(float v) {
  return fbits((unsigned)__builtin_amdgcn_mov_dpp((int)ubits(v), I * 0x55, 0xf, 0xf, true));
}
DI u32x4 lds_ld(const char* smem, unsigned off) { return *(const u32x4*)(smem + off); }
DI void lds_st(char* smem, unsigned off, u32x4 v) { *(u32x4*)(smem + off) = v; }

DI void dma16(unsigned lds_dst, const char* sbase, unsigned voff) {
  unsigned keep;
  asm volatile("s_mov_b32 %0, m0\n\ts_mov_b32 m0, %1\n\ts_nop 0\n\tglobal_load_lds_dwordx4 %2, %3\n\ts_mov_b32 m0, %0"
               : "=&s"(keep) : "s"(lds_dst), "v"(voff), "s"(sbase) : "memory");
}
DI void wait_vm0() { asm volatile("s_waitcnt vmcnt(0)" ::: "memory"); }
DI void wait_lgkm0() { asm volatile("s_waitcnt lgkmcnt(0)" ::: "memory"); }
DI void wg_barrier() { asm volatile("s_waitcnt lgkmcnt(0)\n\ts_barrier" ::: "memory"); }

DI void grp_arrive(unsigned lds_addr) {
  unsigned a, o;
  asm volatile("v_mov_b32 %0, %2\n\tv_mov_b32 %1, 1\n\ts_waitcnt lgkmcnt(0)\n\ts_mov_b64 exec, 1\n\tds_add_u32 %0, %1\n\ts_mov_b64 exec, -1"
               : "=&v"(a), "=&v"(o) : "s"(lds_addr) : "memory");
}
DI void grp_wait(unsigned lds_addr, unsigned target) {
  unsigned a, v, sv;
  asm volatile("v_mov_b32 %0, %3\n"
               "1:\n\tds_read_b32 %1, %0\n\ts_waitcnt lgkmcnt(0)\n\tv_readfirstlane_b32 %2, %1\n\ts_nop 1\n\ts_cmp_ge_u32 %2, %4\n\t"
               "s_cbranch_scc1 2f\n\ts_sleep 1\n\ts_branch 1b\n2:"
               : "=&v"(a), "=&v"(v), "=&s"(sv) : "s"(lds_addr), "s"(target) : "memory", "scc");
}
DI void grp_wait_peeked(unsigned lds_addr, unsigned target, unsigned peeked) {
  unsigned a, v, sv;
  asm volatile("v_readfirstlane_b32 %2, %5\n\tv_mov_b32 %0, %3\n\ts_nop 0\n\ts_cmp_ge_u32 %2, %4\n\ts_cbranch_scc1 2f\n"
               "1:\n\tds_read_b32 %1, %0\n\ts_waitcnt lgkmcnt(0)\n\tv_readfirstlane_b32 %2, %1\n\ts_nop 1\n\ts_cmp_ge_u32 %2, %4\n\t"
               "s_cbranch_scc1 2f\n\ts_sleep 1\n\ts_branch 1b\n2:"
               : "=&v"(a), "=&v"(v), "=&s"(sv) : "s"(lds_addr), "s"(target), "v"(peeked) : "memory", "scc");
}
template <int PA, int PB> DI void set_prio(int grp) {
  asm volatile("s_cmp_eq_u32 %0, 0\n\ts_cbranch_scc1 1f\n\ts_setprio %2\n\ts_branch 2f\n1:\n\ts_setprio %1\n2:"
               :: "s"(grp), "i"(PA), "i"(PB) : "scc");
}

#define STAMP0(i)
#define STAMPL(i)
#define STAMP_DECL
#define STAMP_OUT
DI int swap23(int n) { return (n & 0x13) | ((n & 4) << 1) | ((n & 8) >> 1); }

#define SB() __builtin_amdgcn_sched_barrier(0)
#define MEMB() asm volatile("" ::: "memory")
#define UNROLL _Pragma("unroll")
#define PMODE 5
#define PRIO_DENSE() set_prio<3, 2>(pgrp)
#define PRIO_PURE() set_prio<1, 0>(pgrp)

__global__ __launch_bounds__(512, 2) NOPK void swin_kernel(const char* __restrict__ x, const char* __restrict__ wqkv_t,
                                                           const char* __restrict__ wo_t, const char* __restrict__ wqkv_s,
                                                           const char* __restrict__ wo_s, char* __restrict__ out, unsigned long long* dbg) {
  __shared__ __attribute__((aligned(16))) char smem[LDS_BYTES];
  const unsigned lds0 = (unsigned)(size_t)smem;
  const int lane = threadIdx.x & 63;
  const int wave = __builtin_amdgcn_readfirstlane(threadIdx.x >> 6);
  const int grp = wave >> 2, w = wave & 3;
  const int col = lane & 31, h = lane >> 5, p = col >> 2, t = col & 3;
  const unsigned l16 = (unsigned)lane * 16u;
  const int pgrp = __builtin_amdgcn_readfirstlane(blockIdx.x < 16 ? grp ^ 1 : grp);

  STAMP_DECL
  STAMP0(0);
  {
    unsigned long long pc; unsigned len;
    asm volatile("s_getpc_b64 %0\n.Lswin_pf_start:\n\ts_mov_b32 %1, .Lswin_pf_end-.Lswin_pf_start" : "=s"(pc), "=s"(len));
    if (CODEPF_COND) {
      UNROLL for (int i = 0; i < 8; ++i) {
        unsigned o = (unsigned)lane * 64u + i * 4096u;
        o = o < len ? o : 0u;
        unsigned keep;
        asm volatile("s_mov_b32 %0, m0\n\ts_mov_b32 m0, %1\n\ts_nop 0\n\tglobal_load_lds_dword %2, %3\n\ts_mov_b32 m0, %0"
                     : "=&s"(keep) : "s"(lds0 + DUM_OFF + 1024), "v"(o), "s"(pc) : "memory");
      }
    }
  }
  const unsigned m1in = lane < 32 ? l16 + 1024u : ((l16 - 512u) ^ 16u) + RS_IN;
  const unsigned m1out = lane < 32 ? l16 + 1024u : ((l16 - 512u) ^ 16u) + RS_OUT;
#define POFF(i, lb, m1) ((i) == 0 ? (lb) : (i) == 1 ? (m1) : (i) == 2 ? ((lb) ^ 16u) : (i) == 3 ? ((lb) ^ 32u) : (i) == 4 ? ((m1) ^ 32u) : ((lb) ^ 48u))
#define PUNI(i, RS) ((i) == 0 ? 0u : (i) == 1 ? 0u : (i) == 2 ? (RS) + 512u : (i) == 3 ? 2u * (RS) : (i) == 4 ? 2u * (RS) : 3u * (RS) + 512u)
#define OPAQUE(v) asm volatile("" : "+v"(v))
  const __amdgpu_buffer_rsrc_t xrs = __builtin_amdgcn_make_buffer_rsrc((void*)x, 0, 38535168, 0x00020000);
  int win = blockIdx.x + 256 * grp;
  u32x4 xf[2][6];
  unsigned xb0;
  { int wy = win / 28, wx = win - wy * 28; xb0 = (unsigned)((wy * 8 + 2 * w) * 224 + wx * 8) * 192u; }
#define XLOAD(n) xf[(n) / 6][(n) % 6] = __builtin_amdgcn_raw_buffer_load_b128(xrs, POFF((n) % 6, l16, m1in), xb0 + ((n) / 6) * ROWB + PUNI((n) % 6, RS_IN), 0)

  if (threadIdx.x < 32) *(unsigned*)(smem + CNT_OFF + threadIdx.x * 4) = 0u;
#define CN(g, o) (lds0 + CNT_OFF + (g) * 64 + (o))
  const unsigned slot = SLOT_OFF + wave * SLOT_B;
  const unsigned cntA = CNT_OFF + grp * 64 + 32, cntB = cntA + 4;
  const float inv96 = 1.0f / 96.0f;

  const unsigned xa_e = slot + (unsigned)((t * 96 + 12 * p + (h ^ t)) * 16);
  const unsigned xa_o = slot + (unsigned)((t * 96 + 12 * p + ((2 + h) ^ t)) * 16);
  const unsigned t1adj = t < 2 ? 6144u : (unsigned)-3072;
  const unsigned sl16 = slot + l16;
#define XA(ks) ((((ks) & 1) ? xa_o : xa_e) + 64 * ((ks) >> 1))

  {
    constexpr unsigned QK = SLOT_OFF + 36864, RWO = WF_OFF + 36864, RWV = RWO + 18432;
    unsigned qo[3], qf[3], vv[3];
    UNROLL for (int m = 0; m < 3; ++m) {
      int idx = m * 64 + lane, row = idx / 24, qq = idx - row * 24;
      int c = qq ^ (row >> 1) ^ ((w & 1) << 2);
      qo[m] = (unsigned)(row * 576 + c * 16);
      qf[m] = (unsigned)(row * 576 + (c ^ 4) * 16);
      int row2 = idx / 12, q2 = idx - row2 * 12;
      vv[m] = w < 2 ? (unsigned)(row2 * 576 + 384 + (q2 ^ (row2 >> 2)) * 16) : (unsigned)(m * 1024) + l16;
    }
    const unsigned vstep = w < 2 ? 16 * 576 : 3072;
    const unsigned vsrc0 = w < 2 ? w * 48 * 576 : (w - 2) * 9216;
    const unsigned vdst0 = w < 2 ? RWV + w * 9216 : RWO + (w - 2) * 9216;
#define QPIECES(g) UNROLL for (int i = 0; i < 9; ++i) dma16(lds0 + QK + w * 9216 + i * 1024, ((g) ? wqkv_s : wqkv_t) + w * 24 * 576 + (i / 3) * 8 * 576, (i / 3 == 1) ? qf[i % 3] : qo[i % 3])
#define VPIECES(g) UNROLL for (int i = 0; i < 9; ++i) dma16(lds0 + vdst0 + i * 1024, (w < 2 ? ((g) ? wqkv_s : wqkv_t) : ((g) ? wo_s : wo_t)) + vsrc0 + (i / 3) * vstep, vv[i % 3])
    if (!grp) { QPIECES(0); } else { VPIECES(0); }
    wg_barrier();
    if (!grp) {
      const char* xb = x + xb0;
      UNROLL for (int i = 0; i < 6; ++i) dma16(lds0 + slot + i * 1024, xb + PUNI(i, RS_IN), POFF(i, l16, m1in));
      UNROLL for (int i = 0; i < 3; ++i) dma16(lds0 + slot + 6144 + i * 1024, xb + ROWB + PUNI(i, RS_IN), POFF(i, l16, m1in));
      UNROLL for (int n = 9; n < 12; ++n) XLOAD(n);
      MEMB();
      __builtin_amdgcn_s_setprio(1);
    }
    const int pn = swap23(col);
    unsigned qb[4], vb[2];
    UNROLL for (int e = 0; e < 4; ++e) qb[e] = QK + (unsigned)(pn * 384 + (((2 * e) | h) ^ ((pn >> 1) & 7)) * 16);
    UNROLL for (int e = 0; e < 2; ++e) vb[e] = RWV + (unsigned)(pn * 192 + (((2 * e) | h) ^ ((pn >> 2) & 3)) * 16);
    const unsigned tb = RWO + (unsigned)((8 * h + ((lane & 15) >> 2)) * 192 + (16 * ((lane >> 4) & 1) + 8 * (lane & 1) + 4 * ((lane >> 1) & 1)) * 2 + w * 64);
    const unsigned ro = (unsigned)w * 12288u;
    UNROLL for (int e = 0; e < 4; ++e) OPAQUE(qb[e]);
    if (!grp) { asm volatile("s_waitcnt vmcnt(12)" ::: "memory"); grp_arrive(CN(0, 0)); }
    else      { wait_vm0(); grp_arrive(CN(0, 4)); }
    STAMP0(1);
#define QKF(rt, c2) lds_ld(smem, qb[(c2) & 3] + (rt) * 12288 + ((c2) >> 2) * 128)
#define QKR(c2) lds_ld(smem, qb[(c2) & 3] + ro + ((c2) >> 2) * 128)
#define VF(rt, kk) lds_ld(smem, vb[(kk) & 1] + (rt) * 6144 + ((kk) >> 1) * 64)

    for (int g = 0; g <= grp; ++g) {
      const unsigned RA = WF_OFF + g * 36864, RB = RA + 18432;
      const unsigned rp = RP_OFF + g * 512;
      if (g == 1) {
        grp_wait(CN(0, 16), 6);
        grp_wait(CN(0, 12), 3);
        QPIECES(1);
        VPIECES(1);
        asm volatile("s_waitcnt vmcnt(9)" ::: "memory");
        grp_arrive(CN(1, 0));
        wait_vm0();
        grp_arrive(CN(1, 4));
        UNROLL for (int n = 0; n < 12; ++n) XLOAD(n);
        MEMB();
      }
      if (w < 3) {
        grp_wait(CN(g, 0), 4);
        if (grp) {
          {
            u32x4 bb[6], af[3][6];
            UNROLL for (int kk = 0; kk < 6; ++kk) { bb[kk] = QKR(kk); af[0][kk] = QKF(0, 6 + kk); }
            UNROLL for (int ct = 1; ct < 3; ++ct)
              UNROLL for (int kk = 0; kk < 6; ++kk) af[ct][kk] = QKF(ct, 6 + kk);
            SB();
            f32x16 d2 = {};
            UNROLL for (int ct = 0; ct < 3; ++ct)
              UNROLL for (int kk = 0; kk < 6; ++kk) d2 = mfma32(af[ct][kk], bb[kk], d2);
            float r0 = (d2[0] + d2[1]) + (d2[2] + d2[3]), r1 = (d2[4] + d2[5]) + (d2[6] + d2[7]);
            float r2_ = (d2[8] + d2[9]) + (d2[10] + d2[11]), r3 = (d2[12] + d2[13]) + (d2[14] + d2[15]);
            float rs = xhalf_add((r0 + r1) + (r2_ + r3));
            *(float*)(smem + rp + (32 * w + pn) * 4) = rs * -inv96;
            grp_arrive(CN(g, 8));
            grp_arrive(CN(g, 16));
          }
          grp_wait(CN(g, 4), 4);
          u32x4 bfr[6], av[3][6];
          UNROLL for (int kk = 0; kk < 6; ++kk) {
            s16x4 lo = __builtin_amdgcn_ds_read_tr16_b64_v4i16((LAS s16x4*)(smem + tb + kk * 3072));
            s16x4 hi = __builtin_amdgcn_ds_read_tr16_b64_v4i16((LAS s16x4*)(smem + tb + kk * 3072 + 768));
            u32x2 l2 = __builtin_bit_cast(u32x2, lo), h2 = __builtin_bit_cast(u32x2, hi);
            bfr[kk] = u32x4{l2[0], l2[1], h2[0], h2[1]};
            av[0][kk] = VF(0, kk);
          }
          UNROLL for (int rt = 1; rt < 3; ++rt)
            UNROLL for (int kk = 0; kk < 6; ++kk) av[rt][kk] = VF(rt, kk);
          SB();
          f32x16 DB[3];
          float c0 = 0.f, c1 = 0.f;
          UNROLL for (int rt = 0; rt < 3; ++rt) {
            f32x16 d = {};
            UNROLL for (int kk = 0; kk < 6; ++kk) d = mfma32(av[rt][kk], bfr[kk], d);
            DB[rt] = d;
            if (rt == 1) grp_arrive(CN(g, 12));
            if (rt > 0) { UNROLL for (int i = 0; i < 16; i += 2) { c0 += DB[rt - 1][i]; c1 += DB[rt - 1][i + 1]; } }
            SB();
          }
          UNROLL for (int i = 0; i < 16; i += 2) { c0 += DB[2][i]; c1 += DB[2][i + 1]; }
          const float cm = xhalf_add(c0 + c1) * inv96;
          grp_wait(CN(g, 12), 3 * g);
          UNROLL for (int rt = 0; rt < 3; ++rt) {
            UNROLL for (int i = 0; i < 16; ++i) DB[rt][i] -= cm;
            lds_st(smem, RB + (w * 6 + 2 * rt) * 1024 + l16, PACKS(DB[rt], 0));
            lds_st(smem, RB + (w * 6 + 2 * rt + 1) * 1024 + l16, PACKS(DB[rt], 1));
          }
          grp_arrive(CN(g, 24));
        }
        if (g == grp) {
          u32x4 bb[6], af[3][6];
          UNROLL for (int kk = 0; kk < 6; ++kk) { bb[kk] = QKR(6 + kk); af[0][kk] = QKF(0, kk); }
          UNROLL for (int rt = 1; rt < 3; ++rt)
            UNROLL for (int kk = 0; kk < 6; ++kk) af[rt][kk] = QKF(rt, kk);
          SB();
          f32x16 DA[3];
          float c0 = 0.f, c1 = 0.f;
          UNROLL for (int rt = 0; rt < 3; ++rt) {
            f32x16 d = {};
            UNROLL for (int kk = 0; kk < 6; ++kk) d = mfma32(af[rt][kk], bb[kk], d);
            DA[rt] = d;
            if (rt == 1) grp_arrive(CN(g, 16));
            if (rt > 0) { UNROLL for (int i = 0; i < 16; i += 2) { c0 += DA[rt - 1][i]; c1 += DA[rt - 1][i + 1]; } }
            SB();
          }
          UNROLL for (int i = 0; i < 16; i += 2) { c0 += DA[2][i]; c1 += DA[2][i + 1]; }
          grp_wait(CN(g, 8), 3);
          f32x4 rm[12];
          UNROLL for (int n = 0; n < 12; ++n) rm[n] = *(const f32x4*)(smem + rp + (32 * (n >> 2) + 16 * ((n >> 1) & 1) + 8 * h + 4 * (n & 1)) * 4);
          UNROLL for (int n = 0; n < 12; ++n) {
            c0 += rm[n][0] + rm[n][2]; c1 += rm[n][1] + rm[n][3];
            UNROLL for (int i = 0; i < 4; ++i) DA[n >> 2][4 * (n & 3) + i] += rm[n][i];
          }
          const float fA = 0.10206207261596575f * 1.4426950408889634f;
          const float cadd = xhalf_add(c0 + c1) * (-fA * inv96);
          UNROLL for (int rt = 0; rt < 3; ++rt) {
            UNROLL for (int i = 0; i < 16; ++i) DA[rt][i] = __builtin_fmaf(DA[rt][i], fA, cadd);
            lds_st(smem, RA + (w * 6 + 2 * rt) * 1024 + l16, PACKS(DA[rt], 0));
            lds_st(smem, RA + (w * 6 + 2 * rt + 1) * 1024 + l16, PACKS(DA[rt], 1));
          }
          grp_arrive(CN(g, 20));
        }
      }
    }
#undef QKF
#undef QKR
#undef VF
#undef QPIECES
#undef VPIECES
  }
  STAMP0(2);
  if (grp) {
    grp_wait(CN(1, 16), 6);
    UNROLL for (int i = 0; i < 6; ++i) lds_st(smem, sl16 + i * 1024, xf[0][i]);
    UNROLL for (int i = 0; i < 3; ++i) lds_st(smem, sl16 + 6144 + i * 1024, xf[1][i]);
    MEMB();
  } else {
    wait_vm0();
  }
  UNROLL for (int ks = 0; ks < 6; ++ks) xf[0][ks] = lds_ld(smem, XA(ks));
  MEMB();
  UNROLL for (int i = 0; i < 3; ++i) lds_st(smem, sl16 + i * 1024, xf[1][3 + i]);
  MEMB();
  UNROLL for (int ks = 0; ks < 6; ++ks) xf[1][ks] = lds_ld(smem, t1adj + XA(ks));
  MEMB();

  const bool own = (t == w);
  const unsigned exo = (unsigned)(h * 384 + p * 48 + ((t ^ w) - 1) * 16);
  const unsigned ex_w = own ? (unsigned)(DUM_OFF + h * 128 + ((3 * (p & 6) + 6 + (p & 1)) & 7) * 16)
                            : SLOT_OFF + (grp * 4 + t) * SLOT_B + exo;
  const unsigned ex_r = slot + exo;
  const unsigned long long notown = ~(0x1111111111111111ull << w);
  const unsigned rbase = R_OFF + grp * 1024;
  unsigned wS = l16 + 36864u;
  asm volatile("" : "+v"(wS));
#define WLD(i) lds_ld(smem, ((i) < 36 ? l16 + (i) * 1024 : wS + ((i) - 36) * 1024))

  const unsigned ONES = 0x3F803F80u;
  const unsigned id0 = t == 0 ? 0x00003F80u : (t == 1 ? 0x3F800000u : 0u);
  const unsigned id1 = t == 2 ? 0x00003F80u : (t == 3 ? 0x3F800000u : 0u);
  unsigned msk[4];
  UNROLL for (int g = 0; g < 4; ++g) msk[g] = ((col >> 3) == g && h == (p & 1)) ? 0xffffffffu : 0u;
  const f32x4 z4 = {0.f, 0.f, 0.f, 0.f};
  const f32x16 zz = {};

  grp_wait(CN(0, 20), 3);

  u32x4 wb[4];
  wb[0] = WLD(0); wb[1] = WLD(1); wb[2] = WLD(2);
  u32x4 x1[2][6];
  UNROLL for (int a = 0; a < 2; ++a) UNROLL for (int i = 0; i < 6; ++i) x1[a][i] = wb[0];
  unsigned iter = 1;

#define T1_STAGE(OUTP, ob)                                                                                       \
  {                                                                                                              \
    f32x4 sm0 = z4, sm1 = z4, g0 = z4, g1 = z4;                                                                  \
    unsigned b1v;                                                                                                \
    UNROLL for (int ks = 0; ks < 6; ++ks) {                                                                      \
      if (ks < 4) {                                                                                              \
        if (ks == 0) { UNROLL for (int k2 = 0; k2 < 6; ++k2) lds_st(smem, XA(k2), x1[0][k2]); MEMB(); }          \
        if (ks == 1) { UNROLL for (int k2 = 0; k2 < 6; ++k2) x1[0][k2] = lds_ld(smem, sl16 + k2 * 1024); MEMB(); } \
        if (ks == 2) { UNROLL for (int k2 = 0; k2 < 6; ++k2) lds_st(smem, XA(k2), x1[1][k2]); MEMB(); }          \
        if (ks == 3) { UNROLL for (int k2 = 0; k2 < 6; ++k2) x1[1][k2] = lds_ld(smem, sl16 + k2 * 1024); MEMB(); } \
      }                                                                                                          \
      UNROLL for (int c = 0; c < 2; ++c) {                                                                       \
        sm0 = mfma4(ONES, ONES, xf[0][ks][2 * c], xf[0][ks][2 * c + 1], sm0);                                    \
        sm1 = mfma4(ONES, ONES, xf[1][ks][2 * c], xf[1][ks][2 * c + 1], sm1);                                    \
        g0 = mfma4(xf[0][ks][2 * c], xf[0][ks][2 * c + 1], xf[0][ks][2 * c], xf[0][ks][2 * c + 1], g0);          \
        g1 = mfma4(xf[1][ks][2 * c], xf[1][ks][2 * c + 1], xf[1][ks][2 * c], xf[1][ks][2 * c + 1], g1);          \
      }                                                                                                          \
      SB();                                                                                                      \
    }                                                                                                            \
    UNROLL for (int i = 0; i < 18; ++i) {                                                                        \
      const int tt = i / 6, ks = i % 6;                                                                          \
      wb[(i + 3) & 3] = WLD(i + 3);                                                                              \
      acc[tt & 1][0] = mfma32(wb[i & 3], xf[0][ks], ks == 0 ? zz : acc[tt & 1][0]);                              \
      acc[tt & 1][1] = mfma32(wb[i & 3], xf[1][ks], ks == 0 ? zz : acc[tt & 1][1]);                              \
      if (tt > 0 && ks >= 1 && ks <= 4) {                                                                        \
        const int a = (ks - 1) >> 1, s = (ks - 1) & 1;                                                           \
        uf[a][2 * (tt - 1) + s] = PACKS(acc[(tt - 1) & 1][a], s);                                                \
      }                                                                                                          \
      if ((i % 3) != 2) {                                                                                        \
        const int n_ = 2 * (i / 3) + (i % 3);                                                                    \
        unsigned lb_ = l16, m1_ = m1out; OPAQUE(lb_); OPAQUE(m1_);                                               \
        __builtin_amdgcn_raw_buffer_store_b128(x1[n_ / 6][n_ % 6],                                               \
            __builtin_amdgcn_make_buffer_rsrc(out + (size_t)((ob) + (n_ / 6) * ROWB + PUNI(n_ % 6, RS_OUT)), 0, (OUTP) ? 0x40000 : 0, 0x00020000), \
            POFF(n_ % 6, lb_, m1_), 0, 0);                                                                       \
      }                                                                                                          \
      if (i == 2) grp_arrive(lds0 + cntB);                                                                       \
      if (i == 11) { MEMB(); b1v = *(const unsigned*)(smem + CNT_OFF + 24); }                                    \
      if (i == 14) grp_wait_peeked(CN(0, 24), 3, b1v);                                                           \
      if (i == 1) {                                                                                              \
        float q0a = (t & 1) ? g0[1] : g0[0], q0b = (t & 1) ? g0[3] : g0[2];                                      \
        float q1a = (t & 1) ? g1[1] : g1[0], q1b = (t & 1) ? g1[3] : g1[2];                                      \
        float q0 = (t & 2) ? q0b : q0a, q1 = (t & 2) ? q1b : q1a;                                                \
        float mean = merge_add(sm0[0], sm1[0]) * inv96;                                                          \
        float var = merge_add(q0, q1) * inv96 - mean * mean;                                                     \
        var = __builtin_fmaxf(var, 0.f);                                                                         \
        rr = __builtin_amdgcn_rsqf(var + 1e-5f);                                                                 \
      }                                                                                                          \
      SB();                                                                                                      \
    }                                                                                                            \
  }

  unsigned ob = 0;
  for (;;) {
    STAMPL(3);
    float rr;
    u32x4 uf[2][6];
    f32x16 acc[2][2];
    T1_STAGE((iter > 1), ob);
    STAMPL(4);
    PRIO_DENSE();
    u32x4 pt[2][2];
    float sum0 = 0.f, sum1 = 0.f, sq0 = 0.f, sq1 = 0.f;
    {
      f32x4 s0, s1, s0b, s1b;
      f32x16 vac[2][2];
      f32x16 oac[2];
      u32x4 vpk[2][2];
      float rs0, rs1, rs2, rs3, y0, y1, y2, y3, e0, e1, e2, e3;
      unsigned cbv;
#define O_STEP(tt)                                                                                               \
  UNROLL for (int a = 0; a < 2; ++a) {                                                                           \
    f32x16 o;                                                                                                    \
    UNROLL for (int g = 0; g < 4; ++g) {                                                                         \
      f32x4 r4 = mfma4(id0, id1, xf[a][2 * (tt) + (g >> 1)][2 * (g & 1)], xf[a][2 * (tt) + (g >> 1)][2 * (g & 1) + 1], z4); \
      UNROLL for (int e = 0; e < 4; ++e) o[4 * g + e] = r4[e];                                                   \
    }                                                                                                            \
    oac[a] = o;                                                                                                  \
  }                                                                                                              \
  oac[0] = mfma32(vpk[0][0], pt[0][0], oac[0]);                                                                  \
  oac[0] = mfma32(vpk[0][1], pt[0][1], oac[0]);                                                                  \
  oac[1] = mfma32(vpk[1][0], pt[1][0], oac[1]);                                                                  \
  oac[1] = mfma32(vpk[1][1], pt[1][1], oac[1]);
#define S2_STEP(tt, a, s)                                                                                        \
  UNROLL for (int i_ = 8 * (s); i_ < 8 * (s) + 8; ++i_) {                                                        \
    if ((a) == 0) { sum0 += oac[0][i_]; sq0 = __builtin_fmaf(oac[0][i_], oac[0][i_], sq0); }                     \
    else          { sum1 += oac[1][i_]; sq1 = __builtin_fmaf(oac[1][i_], oac[1][i_], sq1); }                     \
  }                                                                                                              \
  x1[a][2 * (tt) + (s)] = PACKS(oac[a], s);                                                                      \
  lds_st(smem, ex_w + ((a) * 6 + 2 * (tt) + (s)) * 768, x1[a][2 * (tt) + (s)]);
      UNROLL for (int j = 0; j < 18; ++j) {
        const int tt = j / 6, ks = j % 6;
        if (j < 15) wb[(j + 18 + 3) & 3] = WLD(j + 18 + 3);
        vac[tt & 1][0] = mfma32(xf[0][ks], wb[(j + 18) & 3], ks == 0 ? zz : vac[tt & 1][0]);
        vac[tt & 1][1] = mfma32(xf[1][ks], wb[(j + 18) & 3], ks == 0 ? zz : vac[tt & 1][1]);
        if (j == 0) { uf[0][4] = PACKS(acc[0][0], 0); uf[0][5] = PACKS(acc[0][0], 1); }
        if (j == 4) { MEMB(); cbv = *(const unsigned*)(smem + cntB); }
        if (j == 1) {
          uf[1][4] = PACKS(acc[0][1], 0); uf[1][5] = PACKS(acc[0][1], 1);
          SB();
          s0 = z4; s1 = z4; s0b = z4; s1b = z4;
          UNROLL for (int k2 = 0; k2 < 6; ++k2) {
            s0 = mfma4(xf[0][k2][0], xf[0][k2][1], uf[0][k2][0], uf[0][k2][1], s0);
            s1 = mfma4(xf[1][k2][0], xf[1][k2][1], uf[1][k2][0], uf[1][k2][1], s1);
            s0b = mfma4(xf[0][k2][2], xf[0][k2][3], uf[0][k2][2], uf[0][k2][3], s0b);
            s1b = mfma4(xf[1][k2][2], xf[1][k2][3], uf[1][k2][2], uf[1][k2][3], s1b);
          }
          rs0 = quad_bcast<0>(rr); rs1 = quad_bcast<1>(rr); rs2 = quad_bcast<2>(rr); rs3 = quad_bcast<3>(rr);
        }
        if (j == 2) {
          y0 = merge_add(s0[0] + s0b[0], s1[0] + s1b[0]) * rs0;
          y1 = merge_add(s0[1] + s0b[1], s1[1] + s1b[1]) * rs1;
        }
        if (j == 3) {
          y2 = merge_add(s0[2] + s0b[2], s1[2] + s1b[2]) * rs2;
          y3 = merge_add(s0[3] + s0b[3], s1[3] + s1b[3]) * rs3;
        }
        if (j == 4) {
          float mx = __builtin_fmaxf(__builtin_fmaxf(y0, y1), __builtin_fmaxf(y2, y3));
          float cc = -rr * mx;
          e0 = __builtin_amdgcn_exp2f(__builtin_fmaf(y0, rr, cc));
          e1 = __builtin_amdgcn_exp2f(__builtin_fmaf(y1, rr, cc));
          e2 = __builtin_amdgcn_exp2f(__builtin_fmaf(y2, rr, cc));
          e3 = __builtin_amdgcn_exp2f(__builtin_fmaf(y3, rr, cc));
        }
        if (j == 5) {
          float inv = __builtin_amdgcn_rcpf((e0 + e1) + (e2 + e3));
          unsigned p01 = pk(e0 * rs0 * inv, e1 * rs1 * inv);
          unsigned p23 = pk(e2 * rs2 * inv, e3 * rs3 * inv);
          auto a = __builtin_amdgcn_permlane32_swap(p01, p01, false, false);
          auto b = __builtin_amdgcn_permlane32_swap(p23, p23, false, false);
          UNROLL for (int ti = 0; ti < 2; ++ti)
            UNROLL for (int s = 0; s < 2; ++s)
              pt[ti][s] = u32x4{a[ti] & msk[2 * s], b[ti] & msk[2 * s], a[ti] & msk[2 * s + 1], b[ti] & msk[2 * s + 1]};
        }
        if (tt > 0) {
          if (ks == 0) { vpk[0][0] = PACKS(vac[(tt - 1) & 1][0], 0); vpk[0][1] = PACKS(vac[(tt - 1) & 1][0], 1); }
          if (ks == 1) {
            vpk[1][0] = PACKS(vac[(tt - 1) & 1][1], 0); vpk[1][1] = PACKS(vac[(tt - 1) & 1][1], 1);
            SB();
            O_STEP(tt - 1);
            if (tt == 1) {
              grp_wait_peeked(lds0 + cntB, 4 * iter, cbv);
            }
          }
          if (ks >= 2) { S2_STEP(tt - 1, (ks - 2) >> 1, (ks - 2) & 1); }
        }
        SB();
      }
      vpk[0][0] = PACKS(vac[0][0], 0); vpk[0][1] = PACKS(vac[0][0], 1);
      vpk[1][0] = PACKS(vac[0][1], 0); vpk[1][1] = PACKS(vac[0][1], 1);
      O_STEP(2);
      S2_STEP(2, 0, 0); S2_STEP(2, 0, 1); S2_STEP(2, 1, 0); S2_STEP(2, 1, 1);
#undef O_STEP
#undef S2_STEP
    }
    {
      float mean = merge_add(sum0, sum1) * inv96;
      float var = merge_add(sq0, sq1) * inv96 - mean * mean;
      var = __builtin_fmaxf(var, 0.f);
      float r2 = __builtin_amdgcn_rsqf(var + 1e-5f);
      *(float*)(smem + rbase + t * 256 + (h * 32 + 4 * p + w) * 4) = r2;
    }
    STAMPL(5);
    grp_arrive(lds0 + cntA);
    PRIO_PURE();
    grp_wait(CN(1, 20), 3);
    wb[0] = WLD(36); wb[1] = WLD(37); wb[2] = WLD(38);
    grp_wait(lds0 + cntA, 4 * iter);
    asm volatile("s_mov_b64 exec, %13\n\t"
                 "ds_read_b128 %0, %12\n\tds_read_b128 %1, %12 offset:768\n\tds_read_b128 %2, %12 offset:1536\n\t"
                 "ds_read_b128 %3, %12 offset:2304\n\tds_read_b128 %4, %12 offset:3072\n\tds_read_b128 %5, %12 offset:3840\n\t"
                 "ds_read_b128 %6, %12 offset:4608\n\tds_read_b128 %7, %12 offset:5376\n\tds_read_b128 %8, %12 offset:6144\n\t"
                 "ds_read_b128 %9, %12 offset:6912\n\tds_read_b128 %10, %12 offset:7680\n\tds_read_b128 %11, %12 offset:8448\n\t"
                 "s_mov_b64 exec, -1\n\ts_waitcnt lgkmcnt(0)"
                 : "+v"(x1[0][0]), "+v"(x1[0][1]), "+v"(x1[0][2]), "+v"(x1[0][3]), "+v"(x1[0][4]), "+v"(x1[0][5]),
                   "+v"(x1[1][0]), "+v"(x1[1][1]), "+v"(x1[1][2]), "+v"(x1[1][3]), "+v"(x1[1][4]), "+v"(x1[1][5])
                 : "v"(lds0 + ex_r), "s"(notown) : "memory");
    float rq[2];
    rq[0] = *(const float*)(smem + rbase + w * 256 + col * 4);
    rq[1] = *(const float*)(smem + rbase + w * 256 + (32 + col) * 4);
    const unsigned rka = rbase + w * 256 + h * 16;
#define RKLD(G) (*(const f32x4*)(smem + rka + (G) * 32))
    f32x4 rkb[2];
    STAMPL(6);
    const int nwin = win + 512;
    const bool more = nwin < 784;
    unsigned xo;
    { int nw = more ? nwin : win; int wy = nw / 28, wx = nw - wy * 28; xo = (unsigned)((wy * 8 + 2 * w) * 224 + wx * 8) * 192u; }
    const char* xb = x + xo;
    u32x4 pre[3];
    SB();
#define SW(k) ((k) < 18 ? 36 + (k) : ((k) < 54 ? 18 + (k) : (k) - 54))
    u32x4 pf[2][4];
    u32x4 vp[3][4];
    f32x16 sa[2][2];
    u32x4 uq[6];
    f32x16 ua[2];
    float mxv, ccv, l0, l1, invv;
#define U_STEP(q, i)                                                                                             \
  {                                                                                                              \
    const int tt_ = (i) / 6, ks_ = (i) % 6, k_ = 18 * (q) + (i);                                                 \
    wb[(k_ + 3) & 3] = WLD(SW(k_ + 3));                                                                          \
    ua[tt_ & 1] = mfma32(wb[k_ & 3], x1[q][ks_], ks_ == 0 ? zz : ua[tt_ & 1]);                                   \
    if (tt_ > 0 && (ks_ == 2 || ks_ == 3)) uq[2 * (tt_ - 1) + (ks_ - 2)] = PACKS(ua[(tt_ - 1) & 1], ks_ - 2);    \
  }
#define S_STEP(q, ks_)                                                                                           \
  {                                                                                                              \
    sa[q][0] = mfma32(x1[0][ks_], uq[ks_], (ks_) == 0 ? zz : sa[q][0]);                                          \
    sa[q][1] = mfma32(x1[1][ks_], uq[ks_], (ks_) == 0 ? zz : sa[q][1]);                                          \
    if ((ks_) == 1) uq[4] = PACKS(ua[0], 0);                                                                     \
    if ((ks_) == 2) uq[5] = PACKS(ua[0], 1);                                                                     \
  }
#define P1(q, G)                                                                                                 \
  {                                                                                                              \
    rkb[((G) + 1) & 1] = RKLD(((G) + 1) & 7);                                                                    \
    UNROLL for (int e = 0; e < 4; ++e) sa[q][(G) >> 2][4 * ((G) & 3) + e] *= rkb[(G) & 1][e];                    \
    const float m0_ = __builtin_fmaxf(sa[q][(G) >> 2][4 * ((G) & 3)], sa[q][(G) >> 2][4 * ((G) & 3) + 1]);       \
    mxv = (G) == 0 ? m0_ : __builtin_fmaxf(mxv, m0_);                                                            \
    mxv = __builtin_fmaxf(__builtin_fmaxf(mxv, sa[q][(G) >> 2][4 * ((G) & 3) + 2]), sa[q][(G) >> 2][4 * ((G) & 3) + 3]); \
  }
#define X1(q) { mxv = xhalf_max(mxv); ccv = -rq[q] * mxv; l0 = 0.f; l1 = 0.f; }
#define P2H(q, H)                                                                                                \
  {                                                                                                              \
    const int b_ = (H) >> 3, i_ = 2 * ((H) & 7), G_ = (H) >> 1;                                                  \
    if (((H) & 1) == 0) { MEMB(); rkb[(G_ + 1) & 1] = RKLD((G_ + 1) & 7); }                                      \
    const float ea = __builtin_amdgcn_exp2f(__builtin_fmaf(sa[q][b_][i_], rq[q], ccv));                          \
    const float eb = __builtin_amdgcn_exp2f(__builtin_fmaf(sa[q][b_][i_ + 1], rq[q], ccv));                      \
    l0 += ea; l1 += eb;                                                                                          \
    sa[q][b_][i_] = ea * rkb[G_ & 1][2 * ((H) & 1)]; sa[q][b_][i_ + 1] = eb * rkb[G_ & 1][2 * ((H) & 1) + 1];    \
  }
#define X2(q) { invv = __builtin_amdgcn_rcpf(xhalf_add(l0 + l1)); }
#define P3(q, F)                                                                                                 \
  {                                                                                                              \
    UNROLL for (int e = 0; e < 8; ++e) sa[q][(F) >> 1][8 * ((F) & 1) + e] *= invv;                               \
    pf[q][F] = PACKS(sa[q][(F) >> 1], (F) & 1);                                                                  \
  }
    UNROLL for (int i = 0; i < 18; ++i) {
      U_STEP(0, i);
      if (i >= 6) {
        unsigned lb_ = l16, m1_ = m1in; OPAQUE(lb_); OPAQUE(m1_);
        if (i < 12) dma16(lds0 + slot + (i - 6) * 1024, xb + PUNI(i - 6, RS_IN), POFF(i - 6, lb_, m1_));
        else if (i < 15) dma16(lds0 + slot + 6144 + (i - 12) * 1024, xb + ROWB + PUNI(i - 12, RS_IN), POFF(i - 12, lb_, m1_));
        else pre[i - 15] = __builtin_amdgcn_raw_buffer_load_b128(xrs, POFF(i - 12, lb_, m1_), xo + ROWB + PUNI(i - 12, RS_IN), 0);
      }
      SB();
    }
    UNROLL for (int ks = 0; ks < 6; ++ks) { S_STEP(0, ks); if (ks == 4) rkb[0] = RKLD(0); SB(); }
    STAMPL(7);
    PRIO_DENSE();
    UNROLL for (int i = 0; i < 18; ++i) {
      U_STEP(1, i);
      if (i >= 1 && i <= 4) { P1(0, 2 * (i - 1)); P1(0, 2 * (i - 1) + 1); }
      if (i == 5) X1(0);
      if (i >= 6) P2H(0, i - 6);
      SB();
    }
    UNROLL for (int ks = 0; ks < 6; ++ks) {
      S_STEP(1, ks);
      if (ks < 2) { P2H(0, 12 + 2 * ks); P2H(0, 13 + 2 * ks); }
      if (ks == 2) X2(0);
      if (ks == 3) { P3(0, 0); }
      if (ks == 4) { P3(0, 1); P3(0, 2); }
      if (ks == 5) { P3(0, 3); }
      SB();
    }
    STAMPL(8);
    {
      f32x16 v0, v1;
      UNROLL for (int j = 0; j < 18; ++j) {
        const int tt = j / 6, ks = j % 6, k = 36 + j;
        wb[(k + 3) & 3] = WLD(SW(k + 3));
        if (ks == 0 && tt > 0) {
          vp[tt - 1][0] = PACKS(v0, 0); vp[tt - 1][1] = PACKS(v0, 1);
          v0 = mfma32(x1[0][ks], wb[k & 3], zz);
          SB();
          vp[tt - 1][2] = PACKS(v1, 0); vp[tt - 1][3] = PACKS(v1, 1);
          v1 = mfma32(x1[1][ks], wb[k & 3], zz);
        } else {
          v0 = mfma32(x1[0][ks], wb[k & 3], ks == 0 ? zz : v0);
          v1 = mfma32(x1[1][ks], wb[k & 3], ks == 0 ? zz : v1);
        }
        if (j == 1) { P1(1, 0); P1(1, 1); P1(1, 2); }
        if (j == 2) { P1(1, 3); P1(1, 4); P1(1, 5); }
        if (j == 3) { P1(1, 6); P1(1, 7); X1(1); }
        if (j == 4) { P2H(1, 0); P2H(1, 1); }
        if (j == 5) { P2H(1, 2); P2H(1, 3); }
        if (j == 7) { P2H(1, 4); }
        if (j == 8) { P2H(1, 5); }
        if (j == 9) { P2H(1, 6); P2H(1, 7); }
        if (j == 10) { P2H(1, 8); P2H(1, 9); }
        if (j == 11) { P2H(1, 10); P2H(1, 11); }
        if (j == 13) { P2H(1, 12); P2H(1, 13); }
        if (j == 14) { P2H(1, 14); P2H(1, 15); }
        if (j == 15) { X2(1); }
        if (j == 16) { P3(1, 0); }
        if (j == 17) { P3(1, 1); P3(1, 2); }
        SB();
      }
      vp[2][0] = PACKS(v0, 0); vp[2][1] = PACKS(v0, 1);
      vp[2][2] = PACKS(v1, 0); vp[2][3] = PACKS(v1, 1);
    }
    STAMPL(9);
    PRIO_PURE();
    {
      f32x16 oc[2];
      UNROLL for (int n = 0; n < 6; ++n) {
        const int q = n / 3, tt = n % 3;
        f32x16 o;
        UNROLL for (int g = 0; g < 4; ++g) {
          f32x4 r4 = mfma4(id0, id1, x1[q][2 * tt + (g >> 1)][2 * (g & 1)], x1[q][2 * tt + (g >> 1)][2 * (g & 1) + 1], z4);
          UNROLL for (int e = 0; e < 4; ++e) o[4 * g + e] = r4[e];
        }
        o = mfma32(vp[tt][0], pf[q][0], o);
        o = mfma32(vp[tt][1], pf[q][1], o);
        if (n == 0) { P3(1, 3); }
        if (n > 0) {
          const int qp = (n - 1) / 3, tp = (n - 1) % 3;
          x1[qp][2 * tp] = PACKS(oc[(n - 1) & 1], 0);
          x1[qp][2 * tp + 1] = PACKS(oc[(n - 1) & 1], 1);
        }
        o = mfma32(vp[tt][2], pf[q][2], o);
        o = mfma32(vp[tt][3], pf[q][3], o);
        oc[n & 1] = o;
        SB();
      }
      { u32x4 t0 = wb[2], t1 = wb[3], t2 = wb[0]; wb[0] = t0; wb[1] = t1; wb[2] = t2; }
      STAMPL(10);
      wait_vm0();
      UNROLL for (int ks = 0; ks < 6; ++ks) xf[0][ks] = lds_ld(smem, XA(ks));
      MEMB();
      UNROLL for (int i = 0; i < 3; ++i) lds_st(smem, sl16 + i * 1024, pre[i]);
      MEMB();
      UNROLL for (int ks = 0; ks < 6; ++ks) xf[1][ks] = lds_ld(smem, t1adj + XA(ks));
      MEMB();
      x1[1][4] = PACKS(oc[1], 0);
      x1[1][5] = PACKS(oc[1], 1);
    }
    STAMPL(11);
    { int wy = win / 28, wx = win - wy * 28; ob = (unsigned)w * RS_IN + (unsigned)((wy * 8) * 224 + wx * 8) * 192u; }
    if (!more) {
      UNROLL for (int a = 0; a < 2; ++a) {
        UNROLL for (int ks = 0; ks < 6; ++ks) lds_st(smem, XA(ks), x1[a][ks]);
        MEMB();
        UNROLL for (int i = 0; i < 6; ++i) x1[a][i] = lds_ld(smem, sl16 + i * 1024);
        MEMB();
      }
      UNROLL for (int a = 0; a < 2; ++a)
        UNROLL for (int i = 0; i < 6; ++i) *(u32x4*)(out + (size_t)(ob + a * ROWB + PUNI(i, RS_OUT)) + POFF(i, l16, m1out)) = x1[a][i];
      STAMPL(12);
      break;
    }
    STAMPL(12);
    win = nwin;
    ++iter;
  }
  STAMP_OUT;
  asm volatile(".Lswin_pf_end:");
}

extern "C" void kernel_launch(void* const* d_in, const int* in_sizes, int n_in,
                              void* d_out, int out_size, void* d_ws, size_t ws_size,
                              hipStream_t stream) {
  swin_kernel<<<256, 512, 0, stream>>>((const char*)d_in[0], (const char*)d_in[1], (const char*)d_in[2],
                                       (const char*)d_in[3], (const char*)d_in[4], (char*)d_out, (unsigned long long*)d_ws);
}
```

```cpp
#define XROLE 2
#define XB_EARLY 0
#define CODEPF
#define CODEPF_COND wave == 7
#include <hip/hip_runtime.h>
#include <hip/hip_bf16.h>
#include <stdint.h>

#if defined(__HIP_DEVICE_COMPILE__)
#define NOPK __attribute__((target("no-packed-fp32-ops")))
#else
#define NOPK
#endif
#define DI __device__ __forceinline__ NOPK

typedef float f32x16 __attribute__((ext_vector_type(16)));
typedef float f32x4 __attribute__((ext_vector_type(4)));
typedef unsigned u32x4 __attribute__((ext_vector_type(4)));
typedef unsigned u32x2 __attribute__((ext_vector_type(2)));
typedef __bf16 bf16x8 __attribute__((ext_vector_type(8)));
typedef __bf16 bf16x2 __attribute__((ext_vector_type(2)));
typedef short s16x4 __attribute__((ext_vector_type(4)));
#define LAS __attribute__((address_space(3)))

constexpr int WF_OFF = 0;
constexpr int SLOT_OFF = 73728;
constexpr int SLOT_B = 9216;
constexpr int R_OFF = SLOT_OFF + 8 * SLOT_B;
constexpr int RP_OFF = R_OFF + 2048;
constexpr int CNT_OFF = RP_OFF + 1024;
constexpr int DUM_OFF = CNT_OFF + 128;
constexpr int LDS_BYTES = DUM_OFF + 9216;

constexpr unsigned RS_IN = 224u * 224u * 192u;
constexpr unsigned ROWB = 224u * 192u;
constexpr unsigned RS_OUT = 2u * ROWB;

DI f32x16 mfma32(u32x4 a, u32x4 b, f32x16 c) {
  return __builtin_amdgcn_mfma_f32_32x32x16_bf16(__builtin_bit_cast(bf16x8, a), __builtin_bit_cast(bf16x8, b), c, 0, 0, 0);
}
DI f32x4 mfma4(unsigned a0, unsigned a1, unsigned b0, unsigned b1, f32x4 c) {
  u32x2 a = {a0, a1}, b = {b0, b1};
  return __builtin_amdgcn_mfma_f32_4x4x4bf16_1k(__builtin_bit_cast(s16x4, a), __builtin_bit_cast(s16x4, b), c, 0, 0, 0);
}
DI unsigned pk(float lo, float hi) {
  bf16x2 p = {(__bf16)lo, (__bf16)hi};
  return __builtin_bit_cast(unsigned, p);
}
#define PACKS(x, s) u32x4{pk((x)[8 * (s)], (x)[8 * (s) + 1]), pk((x)[8 * (s) + 2], (x)[8 * (s) + 3]), \
                          pk((x)[8 * (s) + 4], (x)[8 * (s) + 5]), pk((x)[8 * (s) + 6], (x)[8 * (s) + 7])}
DI float fbits(unsigned u) { return __builtin_bit_cast(float, u); }
DI unsigned ubits(float f) { return __builtin_bit_cast(unsigned, f); }
DI float merge_add(float a, float b) {
  auto r = __builtin_amdgcn_permlane32_swap(ubits(a), ubits(b), false, false);
  return fbits(r[0]) + fbits(r[1]);
}
DI float xhalf_add(float a) { return merge_add(a, a); }
DI float xhalf_max(float a) {
  auto r = __builtin_amdgcn_permlane32_swap(ubits(a), ubits(a), false, false);
  return __builtin_fmaxf(fbits(r[0]), fbits(r[1]));
}
template <int I> DI float quad_bcast(float v) {
  return fbits((unsigned)__builtin_amdgcn_mov_dpp((int)ubits(v), I * 0x55, 0xf, 0xf, true));
}
DI u32x4 lds_ld(const char* smem, unsigned off) { return *(const u32x4*)(smem + off); }
DI void lds_st(char* smem, unsigned off, u32x4 v) { *(u32x4*)(smem + off) = v; }

DI void dma16(unsigned lds_dst, const char* sbase, unsigned voff) {
  unsigned keep;
  asm volatile("s_mov_b32 %0, m0\n\ts_mov_b32 m0, %1\n\ts_nop 0\n\tglobal_load_lds_dwordx4 %2, %3\n\ts_mov_b32 m0, %0"
               : "=&s"(keep) : "s"(lds_dst), "v"(voff), "s"(sbase) : "memory");
}
DI void dma16nt(unsigned lds_dst, const char* sbase, unsigned voff) {
  unsigned keep;
  asm volatile("s_mov_b32 %0, m0\n\ts_mov_b32 m0, %1\n\ts_nop 0\n\tglobal_load_lds_dwordx4 %2, %3 nt\n\ts_mov_b32 m0, %0"
               : "=&s"(keep) : "s"(lds_dst), "v"(voff), "s"(sbase) : "memory");
}
DI void wait_vm0() { asm volatile("s_waitcnt vmcnt(0)" ::: "memory"); }
DI void wait_lgkm0() { asm volatile("s_waitcnt lgkmcnt(0)" ::: "memory"); }
DI void wg_barrier() { asm volatile("s_waitcnt lgkmcnt(0)\n\ts_barrier" ::: "memory"); }

DI void grp_arrive(unsigned lds_addr) {
  unsigned a, o;
  asm volatile("v_mov_b32 %0, %2\n\tv_mov_b32 %1, 1\n\ts_waitcnt lgkmcnt(0)\n\ts_mov_b64 exec, 1\n\tds_add_u32 %0, %1\n\ts_mov_b64 exec, -1"
               : "=&v"(a), "=&v"(o) : "s"(lds_addr) : "memory");
}
DI void grp_wait(unsigned lds_addr, unsigned target) {
  unsigned a, v, sv;
  asm volatile("v_mov_b32 %0, %3\n"
               "1:\n\tds_read_b32 %1, %0\n\ts_waitcnt lgkmcnt(0)\n\tv_readfirstlane_b32 %2, %1\n\ts_nop 1\n\ts_cmp_ge_u32 %2, %4\n\t"
               "s_cbranch_scc1 2f\n\ts_sleep 1\n\ts_branch 1b\n2:"
               : "=&v"(a), "=&v"(v), "=&s"(sv) : "s"(lds_addr), "s"(target) : "memory", "scc");
}
DI void grp_wait_peeked(unsigned lds_addr, unsigned target, unsigned peeked) {
  unsigned a, v, sv;
  asm volatile("v_readfirstlane_b32 %2, %5\n\tv_mov_b32 %0, %3\n\ts_nop 0\n\ts_cmp_ge_u32 %2, %4\n\ts_cbranch_scc1 2f\n"
               "1:\n\tds_read_b32 %1, %0\n\ts_waitcnt lgkmcnt(0)\n\tv_readfirstlane_b32 %2, %1\n\ts_nop 1\n\ts_cmp_ge_u32 %2, %4\n\t"
               "s_cbranch_scc1 2f\n\ts_sleep 1\n\ts_branch 1b\n2:"
               : "=&v"(a), "=&v"(v), "=&s"(sv) : "s"(lds_addr), "s"(target), "v"(peeked) : "memory", "scc");
}
template <int PA, int PB> DI void set_prio(int grp) {
  asm volatile("s_cmp_eq_u32 %0, 0\n\ts_cbranch_scc1 1f\n\ts_setprio %2\n\ts_branch 2f\n1:\n\ts_setprio %1\n2:"
               :: "s"(grp), "i"(PA), "i"(PB) : "scc");
}

#define STAMP0(i)
#define STAMPL(i)
#define STAMP_DECL
#define STAMP_OUT
DI int swap23(int n) { return (n & 0x13) | ((n & 4) << 1) | ((n & 8) >> 1); }

#define SB() __builtin_amdgcn_sched_barrier(0)
#define MEMB() asm volatile("" ::: "memory")
#define UNROLL _Pragma("unroll")
#define PMODE 5
#define PRIO_DENSE() set_prio<3, 2>(pgrp)
#define PRIO_PURE() set_prio<1, 0>(pgrp)

__global__ __launch_bounds__(512, 2) NOPK void swin_kernel(const char* __restrict__ x, const char* __restrict__ wqkv_t,
                                                           const char* __restrict__ wo_t, const char* __restrict__ wqkv_s,
                                                           const char* __restrict__ wo_s, char* __restrict__ out, unsigned long long* dbg) {
  __shared__ __attribute__((aligned(16))) char smem[LDS_BYTES];
  const unsigned lds0 = (unsigned)(size_t)smem;
  const int lane = threadIdx.x & 63;
  const int wave = __builtin_amdgcn_readfirstlane(threadIdx.x >> 6);
  const int grp = wave >> 2, w = wave & 3;
  const int col = lane & 31, h = lane >> 5, p = col >> 2, t = col & 3;
  const unsigned l16 = (unsigned)lane * 16u;
  const int pgrp = __builtin_amdgcn_readfirstlane(blockIdx.x < 16 ? grp ^ 1 : grp);

  STAMP_DECL
  STAMP0(0);
  {
    unsigned long long pc; unsigned len;
    asm volatile("s_getpc_b64 %0\n.Lswin_pf_start:\n\ts_mov_b32 %1, .Lswin_pf_end-.Lswin_pf_start" : "=s"(pc), "=s"(len));
    if (CODEPF_COND) {
      UNROLL for (int i = 0; i < 8; ++i) {
        unsigned o = (unsigned)lane * 64u + i * 4096u;
        o = o < len ? o : 0u;
        unsigned keep;
        asm volatile("s_mov_b32 %0, m0\n\ts_mov_b32 m0, %1\n\ts_nop 0\n\tglobal_load_lds_dword %2, %3\n\ts_mov_b32 m0, %0"
                     : "=&s"(keep) : "s"(lds0 + DUM_OFF + 1024), "v"(o), "s"(pc) : "memory");
      }
    }
  }
  const unsigned m1in = lane < 32 ? l16 + 1024u : ((l16 - 512u) ^ 16u) + RS_IN;
  const unsigned m1out = lane < 32 ? l16 + 1024u : ((l16 - 512u) ^ 16u) + RS_OUT;
#define POFF(i, lb, m1) ((i) == 0 ? (lb) : (i) == 1 ? (m1) : (i) == 2 ? ((lb) ^ 16u) : (i) == 3 ? ((lb) ^ 32u) : (i) == 4 ? ((m1) ^ 32u) : ((lb) ^ 48u))
#define PUNI(i, RS) ((i) == 0 ? 0u : (i) == 1 ? 0u : (i) == 2 ? (RS) + 512u : (i) == 3 ? 2u * (RS) : (i) == 4 ? 2u * (RS) : 3u * (RS) + 512u)
#define OPAQUE(v) asm volatile("" : "+v"(v))
  const __amdgpu_buffer_rsrc_t xrs = __builtin_amdgcn_make_buffer_rsrc((void*)x, 0, 38535168, 0x00020000);
  int win = blockIdx.x + 256 * grp;
  u32x4 xf[2][6];
  unsigned xb0;
  { int wy = win / 28, wx = win - wy * 28; xb0 = (unsigned)((wy * 8 + 2 * w) * 224 + wx * 8) * 192u; }
#define XLOAD(n) xf[(n) / 6][(n) % 6] = __builtin_amdgcn_raw_buffer_load_b128(xrs, POFF((n) % 6, l16, m1in), xb0 + ((n) / 6) * ROWB + PUNI((n) % 6, RS_IN), 2)

  if (threadIdx.x < 32) *(unsigned*)(smem + CNT_OFF + threadIdx.x * 4) = 0u;
#define CN(g, o) (lds0 + CNT_OFF + (g) * 64 + (o))
  const unsigned slot = SLOT_OFF + wave * SLOT_B;
  const unsigned cntA = CNT_OFF + grp * 64 + 32, cntB = cntA + 4;
  const float inv96 = 1.0f / 96.0f;

  const unsigned xa_e = slot + (unsigned)((t * 96 + 12 * p + (h ^ t)) * 16);
  const unsigned xa_o = slot + (unsigned)((t * 96 + 12 * p + ((2 + h) ^ t)) * 16);
  const unsigned t1adj = t < 2 ? 6144u : (unsigned)-3072;
  const unsigned sl16 = slot + l16;
#define XA(ks) ((((ks) & 1) ? xa_o : xa_e) + 64 * ((ks) >> 1))

  {
    const unsigned QK = SLOT_OFF + grp * 36864, RA = WF_OFF + grp * 36864, RB = RA + 18432;
    const unsigned rp = RP_OFF + grp * 512;
    const unsigned cn = lds0 + CNT_OFF + grp * 64;
    const char* wq = grp ? wqkv_s : wqkv_t;
    const char* wo = grp ? wo_s : wo_t;
    {
      unsigned qo[3], qf[3], vv[3];
      UNROLL for (int m = 0; m < 3; ++m) {
        int idx = m * 64 + lane, row = idx / 24, qq = idx - row * 24;
        int c = qq ^ (row >> 1) ^ ((w & 1) << 2);
        qo[m] = (unsigned)(row * 576 + c * 16);
        qf[m] = (unsigned)(row * 576 + (c ^ 4) * 16);
        int row2 = idx / 12, q2 = idx - row2 * 12;
        vv[m] = w < 2 ? (unsigned)(row2 * 576 + 384 + (q2 ^ (row2 >> 2)) * 16) : (unsigned)(m * 1024) + l16;
      }
      const unsigned vstep = w < 2 ? 16 * 576 : 3072;
      const char* vsrc = w < 2 ? wq + w * 48 * 576 : wo + (w - 2) * 9216;
      const unsigned vdst0 = w < 2 ? RB + w * 9216 : RA + (w - 2) * 9216;
      UNROLL for (int i = 0; i < 9; ++i) dma16(lds0 + QK + w * 9216 + i * 1024, wq + w * 24 * 576 + (i / 3) * 8 * 576, (i / 3 == 1) ? qf[i % 3] : qo[i % 3]);
      UNROLL for (int i = 0; i < 9; ++i) dma16(lds0 + vdst0 + i * 1024, vsrc + (i / 3) * vstep, vv[i % 3]);
    }
    const int role = (w + XROLE * grp) & 3;
    const int pn = swap23(col);
    unsigned qb[4], vb[2];
    UNROLL for (int e = 0; e < 4; ++e) qb[e] = QK + (unsigned)(pn * 384 + (((2 * e) | h) ^ ((pn >> 1) & 7)) * 16);
    UNROLL for (int e = 0; e < 2; ++e) vb[e] = RB + (unsigned)(pn * 192 + (((2 * e) | h) ^ ((pn >> 2) & 3)) * 16);
    const unsigned tb = RA + (unsigned)((8 * h + ((lane & 15) >> 2)) * 192 + (16 * ((lane >> 4) & 1) + 8 * (lane & 1) + 4 * ((lane >> 1) & 1)) * 2 + role * 64);
    const unsigned ro = (unsigned)role * 12288u;
    UNROLL for (int e = 0; e < 4; ++e) OPAQUE(qb[e]);
    wg_barrier();
    if (!grp) {
      UNROLL for (int n = 0; n < 12; ++n) XLOAD(n);
      asm volatile("s_waitcnt vmcnt(21)" ::: "memory");
      grp_arrive(cn + 0);
      asm volatile("s_waitcnt vmcnt(12)" ::: "memory");
      grp_arrive(cn + 4);
    } else {
      asm volatile("s_waitcnt vmcnt(9)" ::: "memory");
      grp_arrive(cn + 0);
      wait_vm0();
      grp_arrive(cn + 4);
    }
    STAMP0(1);
#define QKF(rt, c2) lds_ld(smem, qb[(c2) & 3] + (rt) * 12288 + ((c2) >> 2) * 128)
#define QKR(c2) lds_ld(smem, qb[(c2) & 3] + ro + ((c2) >> 2) * 128)
#define VF(rt, kk) lds_ld(smem, vb[(kk) & 1] + (rt) * 6144 + ((kk) >> 1) * 64)
    grp_wait(cn + 0, 4);
    if (role == 3) {
      __builtin_amdgcn_s_setprio(2);
      u32x4 af[3][6], bb[2][6];
      UNROLL for (int kk = 0; kk < 6; ++kk) { bb[0][kk] = QKF(0, kk); af[0][kk] = QKF(0, 6 + kk); }
      UNROLL for (int ct = 1; ct < 3; ++ct)
        UNROLL for (int kk = 0; kk < 6; ++kk) af[ct][kk] = QKF(ct, 6 + kk);
      f32x16 d2[2];
      UNROLL for (int rt = 0; rt < 4; ++rt) {
        if (rt < 2) { UNROLL for (int kk = 0; kk < 6; ++kk) bb[(rt + 1) & 1][kk] = QKF(rt + 1, kk); }
        SB();
        if (rt < 3) {
          UNROLL for (int ct = 0; ct < 3; ++ct)
            UNROLL for (int kk = 0; kk < 6; ++kk) d2[rt & 1] = mfma32(af[ct][kk], bb[rt & 1][kk], (ct | kk) ? d2[rt & 1] : f32x16{});
        }
        if (rt > 0) {
          const f32x16& d = d2[(rt - 1) & 1];
          float r0 = (d[0] + d[1]) + (d[2] + d[3]), r1 = (d[4] + d[5]) + (d[6] + d[7]);
          float r2_ = (d[8] + d[9]) + (d[10] + d[11]), r3 = (d[12] + d[13]) + (d[14] + d[15]);
          float rs = xhalf_add((r0 + r1) + (r2_ + r3));
          *(float*)(smem + rp + (32 * (rt - 1) + pn) * 4) = rs * -inv96;
        }
        SB();
      }
      grp_arrive(cn + 8);
      grp_arrive(cn + 16);
      __builtin_amdgcn_s_setprio(0);
    } else {
      f32x16 DA[3];
      float a0 = 0.f, a1 = 0.f;
      {
        u32x4 bb[6], af[3][6];
        UNROLL for (int kk = 0; kk < 6; ++kk) { bb[kk] = QKR(6 + kk); af[0][kk] = QKF(0, kk); }
        UNROLL for (int rt = 1; rt < 3; ++rt)
          UNROLL for (int kk = 0; kk < 6; ++kk) af[rt][kk] = QKF(rt, kk);
        SB();
        UNROLL for (int rt = 0; rt < 3; ++rt) {
          f32x16 d = {};
          UNROLL for (int kk = 0; kk < 6; ++kk) d = mfma32(af[rt][kk], bb[kk], d);
          DA[rt] = d;
          if (rt == 1) grp_arrive(cn + 16);
          if (rt > 0) { UNROLL for (int i = 0; i < 16; i += 2) { a0 += DA[rt - 1][i]; a1 += DA[rt - 1][i + 1]; } }
          SB();
        }
      }
      {
        grp_wait(cn + 4, 4);
        u32x4 bfr[6], av[2][6];
        UNROLL for (int kk = 0; kk < 6; ++kk) {
          s16x4 lo = __builtin_amdgcn_ds_read_tr16_b64_v4i16((LAS s16x4*)(smem + tb + kk * 3072));
          s16x4 hi = __builtin_amdgcn_ds_read_tr16_b64_v4i16((LAS s16x4*)(smem + tb + kk * 3072 + 768));
          u32x2 l2 = __builtin_bit_cast(u32x2, lo), h2 = __builtin_bit_cast(u32x2, hi);
          bfr[kk] = u32x4{l2[0], l2[1], h2[0], h2[1]};
          av[0][kk] = VF(0, kk);
        }
        UNROLL for (int kk = 0; kk < 6; ++kk) av[1][kk] = VF(1, kk);
        UNROLL for (int i = 0; i < 16; i += 2) { a0 += DA[2][i]; a1 += DA[2][i + 1]; }
        SB();
        f32x16 DB[3];
        float c0 = 0.f, c1 = 0.f;
        UNROLL for (int rt = 0; rt < 3; ++rt) {
          f32x16 d = {};
          UNROLL for (int kk = 0; kk < 6; ++kk) {
            d = mfma32(av[rt & 1][kk], bfr[kk], d);
            if (rt == 0) av[0][kk] = VF(2, kk);
          }
          DB[rt] = d;
          if (rt == 1) grp_arrive(cn + 12);
          if (rt > 0) { UNROLL for (int i = 0; i < 16; i += 2) { c0 += DB[rt - 1][i]; c1 += DB[rt - 1][i + 1]; } }
          SB();
        }
        UNROLL for (int i = 0; i < 16; i += 2) { c0 += DB[2][i]; c1 += DB[2][i + 1]; }
        const float cm = xhalf_add(c0 + c1) * inv96;
        grp_wait(cn + 12, 3);
        UNROLL for (int rt = 0; rt < 3; ++rt) {
          UNROLL for (int i = 0; i < 16; ++i) DB[rt][i] -= cm;
          lds_st(smem, RB + (role * 6 + 2 * rt) * 1024 + l16, PACKS(DB[rt], 0));
          lds_st(smem, RB + (role * 6 + 2 * rt + 1) * 1024 + l16, PACKS(DB[rt], 1));
        }
      }
      grp_wait(cn + 8, 1);
      f32x4 rm[12];
      UNROLL for (int n = 0; n < 12; ++n) rm[n] = *(const f32x4*)(smem + rp + (32 * (n >> 2) + 16 * ((n >> 1) & 1) + 8 * h + 4 * (n & 1)) * 4);
      UNROLL for (int n = 0; n < 12; ++n) {
        a0 += rm[n][0] + rm[n][2]; a1 += rm[n][1] + rm[n][3];
        UNROLL for (int i = 0; i < 4; ++i) DA[n >> 2][4 * (n & 3) + i] += rm[n][i];
      }
      const float fA = 0.10206207261596575f * 1.4426950408889634f;
      const float cadd = xhalf_add(a0 + a1) * (-fA * inv96);
      UNROLL for (int rt = 0; rt < 3; ++rt) {
        UNROLL for (int i = 0; i < 16; ++i) DA[rt][i] = __builtin_fmaf(DA[rt][i], fA, cadd);
        lds_st(smem, RA + (role * 6 + 2 * rt) * 1024 + l16, PACKS(DA[rt], 0));
        lds_st(smem, RA + (role * 6 + 2 * rt + 1) * 1024 + l16, PACKS(DA[rt], 1));
      }
      grp_arrive(cn + 20);
    }
#undef QKF
#undef QKR
#undef VF
    STAMP0(2);
    if (grp) { UNROLL for (int n = 0; n < 12; ++n) XLOAD(n); MEMB(); }
    grp_wait(cn + 16, 4);
  }
  UNROLL for (int i = 0; i < 6; ++i) lds_st(smem, sl16 + i * 1024, xf[0][i]);
  UNROLL for (int i = 0; i < 3; ++i) lds_st(smem, sl16 + 6144 + i * 1024, xf[1][i]);
  MEMB();
  UNROLL for (int ks = 0; ks < 6; ++ks) xf[0][ks] = lds_ld(smem, XA(ks));
  MEMB();
  UNROLL for (int i = 0; i < 3; ++i) lds_st(smem, sl16 + i * 1024, xf[1][3 + i]);
  MEMB();
  UNROLL for (int ks = 0; ks < 6; ++ks) xf[1][ks] = lds_ld(smem, t1adj + XA(ks));
  MEMB();

  const bool own = (t == w);
  const unsigned exo = (unsigned)(h * 384 + p * 48 + ((t ^ w) - 1) * 16);
  const unsigned ex_w = own ? (unsigned)(DUM_OFF + h * 128 + ((3 * (p & 6) + 6 + (p & 1)) & 7) * 16)
                            : SLOT_OFF + (grp * 4 + t) * SLOT_B + exo;
  const unsigned ex_r = slot + exo;
  const unsigned long long notown = ~(0x1111111111111111ull << w);
  const unsigned rbase = R_OFF + grp * 1024;
  unsigned wS = l16 + 36864u;
  asm volatile("" : "+v"(wS));
#define WLD(i) lds_ld(smem, ((i) < 36 ? l16 + (i) * 1024 : wS + ((i) - 36) * 1024))

  const unsigned ONES = 0x3F803F80u;
  const unsigned id0 = t == 0 ? 0x00003F80u : (t == 1 ? 0x3F800000u : 0u);
  const unsigned id1 = t == 2 ? 0x00003F80u : (t == 3 ? 0x3F800000u : 0u);
  unsigned msk[4];
  UNROLL for (int g = 0; g < 4; ++g) msk[g] = ((col >> 3) == g && h == (p & 1)) ? 0xffffffffu : 0u;
  const f32x4 z4 = {0.f, 0.f, 0.f, 0.f};
  const f32x16 zz = {};

  grp_wait(CN(0, 20), 3);

  u32x4 wb[4];
  wb[0] = WLD(0); wb[1] = WLD(1); wb[2] = WLD(2);
  u32x4 x1[2][6];
  UNROLL for (int a = 0; a < 2; ++a) UNROLL for (int i = 0; i < 6; ++i) x1[a][i] = wb[0];
  unsigned iter = 1;

#define T1_STAGE(OUTP, ob)                                                                                       \
  {                                                                                                              \
    f32x4 sm0 = z4, sm1 = z4, g0 = z4, g1 = z4;                                                                  \
    UNROLL for (int ks = 0; ks < 6; ++ks) {                                                                      \
      if (ks < 4) {                                                                                              \
        if (ks == 0) { UNROLL for (int k2 = 0; k2 < 6; ++k2) lds_st(smem, XA(k2), x1[0][k2]); MEMB(); }          \
        if (ks == 1) { UNROLL for (int k2 = 0; k2 < 6; ++k2) x1[0][k2] = lds_ld(smem, sl16 + k2 * 1024); MEMB(); } \
        if (ks == 2) { UNROLL for (int k2 = 0; k2 < 6; ++k2) lds_st(smem, XA(k2), x1[1][k2]); MEMB(); }          \
        if (ks == 3) { UNROLL for (int k2 = 0; k2 < 6; ++k2) x1[1][k2] = lds_ld(smem, sl16 + k2 * 1024); MEMB(); } \
      }                                                                                                          \
      UNROLL for (int c = 0; c < 2; ++c) {                                                                       \
        sm0 = mfma4(ONES, ONES, xf[0][ks][2 * c], xf[0][ks][2 * c + 1], sm0);                                    \
        sm1 = mfma4(ONES, ONES, xf[1][ks][2 * c], xf[1][ks][2 * c + 1], sm1);                                    \
        g0 = mfma4(xf[0][ks][2 * c], xf[0][ks][2 * c + 1], xf[0][ks][2 * c], xf[0][ks][2 * c + 1], g0);          \
        g1 = mfma4(xf[1][ks][2 * c], xf[1][ks][2 * c + 1], xf[1][ks][2 * c], xf[1][ks][2 * c + 1], g1);          \
      }                                                                                                          \
      SB();                                                                                                      \
    }                                                                                                            \
    UNROLL for (int i = 0; i < 18; ++i) {                                                                        \
      const int tt = i / 6, ks = i % 6;                                                                          \
      wb[(i + 3) & 3] = WLD(i + 3);                                                                              \
      acc[tt & 1][0] = mfma32(wb[i & 3], xf[0][ks], ks == 0 ? zz : acc[tt & 1][0]);                              \
      acc[tt & 1][1] = mfma32(wb[i & 3], xf[1][ks], ks == 0 ? zz : acc[tt & 1][1]);                              \
      if (tt > 0 && ks >= 1 && ks <= 4) {                                                                        \
        const int a = (ks - 1) >> 1, s = (ks - 1) & 1;                                                           \
        uf[a][2 * (tt - 1) + s] = PACKS(acc[(tt - 1) & 1][a], s);                                                \
      }                                                                                                          \
      if ((i % 3) != 2) {                                                                                        \
        const int n_ = 2 * (i / 3) + (i % 3);                                                                    \
        unsigned lb_ = l16, m1_ = m1out; OPAQUE(lb_); OPAQUE(m1_);                                               \
        __builtin_amdgcn_raw_buffer_store_b128(x1[n_ / 6][n_ % 6],                                               \
            __builtin_amdgcn_make_buffer_rsrc(out + (size_t)((ob) + (n_ / 6) * ROWB + PUNI(n_ % 6, RS_OUT)), 0, (OUTP) ? 0x40000 : 0, 0x00020000), \
            POFF(n_ % 6, lb_, m1_), 0, 2);                                                                       \
      }                                                                                                          \
      if (i == 2) grp_arrive(lds0 + cntB);                                                                       \
      if (i == 1) {                                                                                              \
        float q0a = (t & 1) ? g0[1] : g0[0], q0b = (t & 1) ? g0[3] : g0[2];                                      \
        float q1a = (t & 1) ? g1[1] : g1[0], q1b = (t & 1) ? g1[3] : g1[2];                                      \
        float q0 = (t & 2) ? q0b : q0a, q1 = (t & 2) ? q1b : q1a;                                                \
        float mean = merge_add(sm0[0], sm1[0]) * inv96;                                                          \
        float var = merge_add(q0, q1) * inv96 - mean * mean;                                                     \
        var = __builtin_fmaxf(var, 0.f);                                                                         \
        rr = __builtin_amdgcn_rsqf(var + 1e-5f);                                                                 \
      }                                                                                                          \
      SB();                                                                                                      \
    }                                                                                                            \
  }

  unsigned ob = 0;
  for (;;) {
    STAMPL(3);
    float rr;
    u32x4 uf[2][6];
    f32x16 acc[2][2];
    T1_STAGE((iter > 1), ob);
    STAMPL(4);
    PRIO_DENSE();
    u32x4 pt[2][2];
    float sum0 = 0.f, sum1 = 0.f, sq0 = 0.f, sq1 = 0.f;
    {
      f32x4 s0, s1, s0b, s1b;
      f32x16 vac[2][2];
      f32x16 oac[2];
      u32x4 vpk[2][2];
      float rs0, rs1, rs2, rs3, y0, y1, y2, y3, e0, e1, e2, e3;
      unsigned cbv;
#define O_STEP(tt)                                                                                               \
  UNROLL for (int a = 0; a < 2; ++a) {                                                                           \
    f32x16 o;                                                                                                    \
    UNROLL for (int g = 0; g < 4; ++g) {                                                                         \
      f32x4 r4 = mfma4(id0, id1, xf[a][2 * (tt) + (g >> 1)][2 * (g & 1)], xf[a][2 * (tt) + (g >> 1)][2 * (g & 1) + 1], z4); \
      UNROLL for (int e = 0; e < 4; ++e) o[4 * g + e] = r4[e];                                                   \
    }                                                                                                            \
    oac[a] = o;                                                                                                  \
  }                                                                                                              \
  oac[0] = mfma32(vpk[0][0], pt[0][0], oac[0]);                                                                  \
  oac[0] = mfma32(vpk[0][1], pt[0][1], oac[0]);                                                                  \
  oac[1] = mfma32(vpk[1][0], pt[1][0], oac[1]);                                                                  \
  oac[1] = mfma32(vpk[1][1], pt[1][1], oac[1]);
#define S2_STEP(tt, a, s)                                                                                        \
  UNROLL for (int i_ = 8 * (s); i_ < 8 * (s) + 8; ++i_) {                                                        \
    if ((a) == 0) { sum0 += oac[0][i_]; sq0 = __builtin_fmaf(oac[0][i_], oac[0][i_], sq0); }                     \
    else          { sum1 += oac[1][i_]; sq1 = __builtin_fmaf(oac[1][i_], oac[1][i_], sq1); }                     \
  }                                                                                                              \
  x1[a][2 * (tt) + (s)] = PACKS(oac[a], s);                                                                      \
  lds_st(smem, ex_w + ((a) * 6 + 2 * (tt) + (s)) * 768, x1[a][2 * (tt) + (s)]);
      UNROLL for (int j = 0; j < 18; ++j) {
        const int tt = j / 6, ks = j % 6;
        if (j < 15) wb[(j + 18 + 3) & 3] = WLD(j + 18 + 3);
        vac[tt & 1][0] = mfma32(xf[0][ks], wb[(j + 18) & 3], ks == 0 ? zz : vac[tt & 1][0]);
        vac[tt & 1][1] = mfma32(xf[1][ks], wb[(j + 18) & 3], ks == 0 ? zz : vac[tt & 1][1]);
        if (j == 0) { uf[0][4] = PACKS(acc[0][0], 0); uf[0][5] = PACKS(acc[0][0], 1); }
        if (j == 4) { MEMB(); cbv = *(const unsigned*)(smem + cntB); }
        if (j == 1) {
          uf[1][4] = PACKS(acc[0][1], 0); uf[1][5] = PACKS(acc[0][1], 1);
          SB();
          s0 = z4; s1 = z4; s0b = z4; s1b = z4;
          UNROLL for (int k2 = 0; k2 < 6; ++k2) {
            s0 = mfma4(xf[0][k2][0], xf[0][k2][1], uf[0][k2][0], uf[0][k2][1], s0);
            s1 = mfma4(xf[1][k2][0], xf[1][k2][1], uf[1][k2][0], uf[1][k2][1], s1);
            s0b = mfma4(xf[0][k2][2], xf[0][k2][3], uf[0][k2][2], uf[0][k2][3], s0b);
            s1b = mfma4(xf[1][k2][2], xf[1][k2][3], uf[1][k2][2], uf[1][k2][3], s1b);
          }
          rs0 = quad_bcast<0>(rr); rs1 = quad_bcast<1>(rr); rs2 = quad_bcast<2>(rr); rs3 = quad_bcast<3>(rr);
        }
        if (j == 2) {
          y0 = merge_add(s0[0] + s0b[0], s1[0] + s1b[0]) * rs0;
          y1 = merge_add(s0[1] + s0b[1], s1[1] + s1b[1]) * rs1;
        }
        if (j == 3) {
          y2 = merge_add(s0[2] + s0b[2], s1[2] + s1b[2]) * rs2;
          y3 = merge_add(s0[3] + s0b[3], s1[3] + s1b[3]) * rs3;
        }
        if (j == 4) {
          float mx = __builtin_fmaxf(__builtin_fmaxf(y0, y1), __builtin_fmaxf(y2, y3));
          float cc = -rr * mx;
          e0 = __builtin_amdgcn_exp2f(__builtin_fmaf(y0, rr, cc));
          e1 = __builtin_amdgcn_exp2f(__builtin_fmaf(y1, rr, cc));
          e2 = __builtin_amdgcn_exp2f(__builtin_fmaf(y2, rr, cc));
          e3 = __builtin_amdgcn_exp2f(__builtin_fmaf(y3, rr, cc));
        }
        if (j == 5) {
          float inv = __builtin_amdgcn_rcpf((e0 + e1) + (e2 + e3));
          unsigned p01 = pk(e0 * rs0 * inv, e1 * rs1 * inv);
          unsigned p23 = pk(e2 * rs2 * inv, e3 * rs3 * inv);
          auto a = __builtin_amdgcn_permlane32_swap(p01, p01, false, false);
          auto b = __builtin_amdgcn_permlane32_swap(p23, p23, false, false);
          UNROLL for (int ti = 0; ti < 2; ++ti)
            UNROLL for (int s = 0; s < 2; ++s)
              pt[ti][s] = u32x4{a[ti] & msk[2 * s], b[ti] & msk[2 * s], a[ti] & msk[2 * s + 1], b[ti] & msk[2 * s + 1]};
        }
        if (tt > 0) {
          if (ks == 0) { vpk[0][0] = PACKS(vac[(tt - 1) & 1][0], 0); vpk[0][1] = PACKS(vac[(tt - 1) & 1][0], 1); }
          if (ks == 1) {
            vpk[1][0] = PACKS(vac[(tt - 1) & 1][1], 0); vpk[1][1] = PACKS(vac[(tt - 1) & 1][1], 1);
            SB();
            O_STEP(tt - 1);
            if (tt == 1) {
              grp_wait_peeked(lds0 + cntB, 4 * iter, cbv);
            }
          }
          if (ks >= 2) { S2_STEP(tt - 1, (ks - 2) >> 1, (ks - 2) & 1); }
        }
        SB();
      }
      vpk[0][0] = PACKS(vac[0][0], 0); vpk[0][1] = PACKS(vac[0][0], 1);
      vpk[1][0] = PACKS(vac[0][1], 0); vpk[1][1] = PACKS(vac[0][1], 1);
      O_STEP(2);
      S2_STEP(2, 0, 0); S2_STEP(2, 0, 1); S2_STEP(2, 1, 0); S2_STEP(2, 1, 1);
#undef O_STEP
#undef S2_STEP
    }
    {
      float mean = merge_add(sum0, sum1) * inv96;
      float var = merge_add(sq0, sq1) * inv96 - mean * mean;
      var = __builtin_fmaxf(var, 0.f);
      float r2 = __builtin_amdgcn_rsqf(var + 1e-5f);
      *(float*)(smem + rbase + t * 256 + (h * 32 + 4 * p + w) * 4) = r2;
    }
    STAMPL(5);
    grp_arrive(lds0 + cntA);
    PRIO_PURE();
    grp_wait(CN(1, 20), 3);
    wb[0] = WLD(36); wb[1] = WLD(37); wb[2] = WLD(38);
    grp_wait(lds0 + cntA, 4 * iter);
    asm volatile("s_mov_b64 exec, %13\n\t"
                 "ds_read_b128 %0, %12\n\tds_read_b128 %1, %12 offset:768\n\tds_read_b128 %2, %12 offset:1536\n\t"
                 "ds_read_b128 %3, %12 offset:2304\n\tds_read_b128 %4, %12 offset:3072\n\tds_read_b128 %5, %12 offset:3840\n\t"
                 "ds_read_b128 %6, %12 offset:4608\n\tds_read_b128 %7, %12 offset:5376\n\tds_read_b128 %8, %12 offset:6144\n\t"
                 "ds_read_b128 %9, %12 offset:6912\n\tds_read_b128 %10, %12 offset:7680\n\tds_read_b128 %11, %12 offset:8448\n\t"
                 "s_mov_b64 exec, -1\n\ts_waitcnt lgkmcnt(0)"
                 : "+v"(x1[0][0]), "+v"(x1[0][1]), "+v"(x1[0][2]), "+v"(x1[0][3]), "+v"(x1[0][4]), "+v"(x1[0][5]),
                   "+v"(x1[1][0]), "+v"(x1[1][1]), "+v"(x1[1][2]), "+v"(x1[1][3]), "+v"(x1[1][4]), "+v"(x1[1][5])
                 : "v"(lds0 + ex_r), "s"(notown) : "memory");
    float rq[2];
    rq[0] = *(const float*)(smem + rbase + w * 256 + col * 4);
    rq[1] = *(const float*)(smem + rbase + w * 256 + (32 + col) * 4);
    const unsigned rka = rbase + w * 256 + h * 16;
#define RKLD(G) (*(const f32x4*)(smem + rka + (G) * 32))
    f32x4 rkb[2];
    STAMPL(6);
    const int nwin = win + 512;
    const bool more = nwin < 784;
    unsigned xo;
    { int nw = more ? nwin : win; int wy = nw / 28, wx = nw - wy * 28; xo = (unsigned)((wy * 8 + 2 * w) * 224 + wx * 8) * 192u; }
    const char* xb = x + xo;
    u32x4 pre[3];
    SB();
#define SW(k) ((k) < 18 ? 36 + (k) : ((k) < 54 ? 18 + (k) : (k) - 54))
    u32x4 pf[2][4];
    u32x4 vp[3][4];
    f32x16 sa[2][2];
    u32x4 uq[6];
    f32x16 ua[2];
    float mxv, ccv, l0, l1, invv;
#define U_STEP(q, i)                                                                                             \
  {                                                                                                              \
    const int tt_ = (i) / 6, ks_ = (i) % 6, k_ = 18 * (q) + (i);                                                 \
    wb[(k_ + 3) & 3] = WLD(SW(k_ + 3));                                                                          \
    ua[tt_ & 1] = mfma32(wb[k_ & 3], x1[q][ks_], ks_ == 0 ? zz : ua[tt_ & 1]);                                   \
    if (tt_ > 0 && (ks_ == 2 || ks_ == 3)) uq[2 * (tt_ - 1) + (ks_ - 2)] = PACKS(ua[(tt_ - 1) & 1], ks_ - 2);    \
  }
#define S_STEP(q, ks_)                                                                                           \
  {                                                                                                              \
    sa[q][0] = mfma32(x1[0][ks_], uq[ks_], (ks_) == 0 ? zz : sa[q][0]);                                          \
    sa[q][1] = mfma32(x1[1][ks_], uq[ks_], (ks_) == 0 ? zz : sa[q][1]);                                          \
    if ((ks_) == 1) uq[4] = PACKS(ua[0], 0);                                                                     \
    if ((ks_) == 2) uq[5] = PACKS(ua[0], 1);                                                                     \
  }
#define P1(q, G)                                                                                                 \
  {                                                                                                              \
    rkb[((G) + 1) & 1] = RKLD(((G) + 1) & 7);                                                                    \
    UNROLL for (int e = 0; e < 4; ++e) sa[q][(G) >> 2][4 * ((G) & 3) + e] *= rkb[(G) & 1][e];                    \
    const float m0_ = __builtin_fmaxf(sa[q][(G) >> 2][4 * ((G) & 3)], sa[q][(G) >> 2][4 * ((G) & 3) + 1]);       \
    mxv = (G) == 0 ? m0_ : __builtin_fmaxf(mxv, m0_);                                                            \
    mxv = __builtin_fmaxf(__builtin_fmaxf(mxv, sa[q][(G) >> 2][4 * ((G) & 3) + 2]), sa[q][(G) >> 2][4 * ((G) & 3) + 3]); \
  }
#define X1(q) { mxv = xhalf_max(mxv); ccv = -rq[q] * mxv; l0 = 0.f; l1 = 0.f; }
#define P2H(q, H)                                                                                                \
  {                                                                                                              \
    const int b_ = (H) >> 3, i_ = 2 * ((H) & 7), G_ = (H) >> 1;                                                  \
    if (((H) & 1) == 0) { MEMB(); rkb[(G_ + 1) & 1] = RKLD((G_ + 1) & 7); }                                      \
    const float ea = __builtin_amdgcn_exp2f(__builtin_fmaf(sa[q][b_][i_], rq[q], ccv));                          \
    const float eb = __builtin_amdgcn_exp2f(__builtin_fmaf(sa[q][b_][i_ + 1], rq[q], ccv));                      \
    l0 += ea; l1 += eb;                                                                                          \
    sa[q][b_][i_] = ea * rkb[G_ & 1][2 * ((H) & 1)]; sa[q][b_][i_ + 1] = eb * rkb[G_ & 1][2 * ((H) & 1) + 1];    \
  }
#define X2(q) { invv = __builtin_amdgcn_rcpf(xhalf_add(l0 + l1)); }
#define P3(q, F)                                                                                                 \
  {                                                                                                              \
    UNROLL for (int e = 0; e < 8; ++e) sa[q][(F) >> 1][8 * ((F) & 1) + e] *= invv;                               \
    pf[q][F] = PACKS(sa[q][(F) >> 1], (F) & 1);                                                                  \
  }
    UNROLL for (int i = 0; i < 18; ++i) {
      U_STEP(0, i);
      if (i >= 6) {
        unsigned lb_ = l16, m1_ = m1in; OPAQUE(lb_); OPAQUE(m1_);
        if (i < 12) dma16nt(lds0 + slot + (i - 6) * 1024, xb + PUNI(i - 6, RS_IN), POFF(i - 6, lb_, m1_));
        else if (i < 15) dma16nt(lds0 + slot + 6144 + (i - 12) * 1024, xb + ROWB + PUNI(i - 12, RS_IN), POFF(i - 12, lb_, m1_));
        else pre[i - 15] = __builtin_amdgcn_raw_buffer_load_b128(xrs, POFF(i - 12, lb_, m1_), xo + ROWB + PUNI(i - 12, RS_IN), 2);
      }
      SB();
    }
    UNROLL for (int ks = 0; ks < 6; ++ks) { S_STEP(0, ks); if (ks == 4) rkb[0] = RKLD(0); SB(); }
    STAMPL(7);
    PRIO_DENSE();
    UNROLL for (int i = 0; i < 18; ++i) {
      U_STEP(1, i);
      if (i >= 1 && i <= 4) { P1(0, 2 * (i - 1)); P1(0, 2 * (i - 1) + 1); }
      if (i == 5) X1(0);
      if (i >= 6) P2H(0, i - 6);
      SB();
    }
    UNROLL for (int ks = 0; ks < 6; ++ks) {
      S_STEP(1, ks);
      if (ks < 2) { P2H(0, 12 + 2 * ks); P2H(0, 13 + 2 * ks); }
      if (ks == 2) X2(0);
      if (ks == 3) { P3(0, 0); }
      if (ks == 4) { P3(0, 1); P3(0, 2); }
      if (ks == 5) { P3(0, 3); }
      SB();
    }
    STAMPL(8);
    {
      f32x16 v0, v1;
      UNROLL for (int j = 0; j < 18; ++j) {
        const int tt = j / 6, ks = j % 6, k = 36 + j;
        wb[(k + 3) & 3] = WLD(SW(k + 3));
        if (ks == 0 && tt > 0) {
          vp[tt - 1][0] = PACKS(v0, 0); vp[tt - 1][1] = PACKS(v0, 1);
          v0 = mfma32(x1[0][ks], wb[k & 3], zz);
          SB();
          vp[tt - 1][2] = PACKS(v1, 0); vp[tt - 1][3] = PACKS(v1, 1);
          v1 = mfma32(x1[1][ks], wb[k & 3], zz);
        } else {
          v0 = mfma32(x1[0][ks], wb[k & 3], ks == 0 ? zz : v0);
          v1 = mfma32(x1[1][ks], wb[k & 3], ks == 0 ? zz : v1);
        }
        if (j == 1) { P1(1, 0); P1(1, 1); P1(1, 2); }
        if (j == 2) { P1(1, 3); P1(1, 4); P1(1, 5); }
        if (j == 3) { P1(1, 6); P1(1, 7); X1(1); }
        if (j == 4) { P2H(1, 0); P2H(1, 1); }
        if (j == 5) { P2H(1, 2); P2H(1, 3); }
        if (j == 7) { P2H(1, 4); }
        if (j == 8) { P2H(1, 5); }
        if (j == 9) { P2H(1, 6); P2H(1, 7); }
        if (j == 10) { P2H(1, 8); P2H(1, 9); }
        if (j == 11) { P2H(1, 10); P2H(1, 11); }
        if (j == 13) { P2H(1, 12); P2H(1, 13); }
        if (j == 14) { P2H(1, 14); P2H(1, 15); }
        if (j == 15) { X2(1); }
        if (j == 16) { P3(1, 0); }
        if (j == 17) { P3(1, 1); P3(1, 2); }
        SB();
      }
      vp[2][0] = PACKS(v0, 0); vp[2][1] = PACKS(v0, 1);
      vp[2][2] = PACKS(v1, 0); vp[2][3] = PACKS(v1, 1);
    }
    STAMPL(9);
    PRIO_PURE();
    {
      f32x16 oc[2];
      UNROLL for (int n = 0; n < 6; ++n) {
        const int q = n / 3, tt = n % 3;
        f32x16 o;
        UNROLL for (int g = 0; g < 4; ++g) {
          f32x4 r4 = mfma4(id0, id1, x1[q][2 * tt + (g >> 1)][2 * (g & 1)], x1[q][2 * tt + (g >> 1)][2 * (g & 1) + 1], z4);
          UNROLL for (int e = 0; e < 4; ++e) o[4 * g + e] = r4[e];
        }
        o = mfma32(vp[tt][0], pf[q][0], o);
        o = mfma32(vp[tt][1], pf[q][1], o);
        if (n == 0) { P3(1, 3); }
        if (n > 0) {
          const int qp = (n - 1) / 3, tp = (n - 1) % 3;
          x1[qp][2 * tp] = PACKS(oc[(n - 1) & 1], 0);
          x1[qp][2 * tp + 1] = PACKS(oc[(n - 1) & 1], 1);
        }
        o = mfma32(vp[tt][2], pf[q][2], o);
        o = mfma32(vp[tt][3], pf[q][3], o);
        oc[n & 1] = o;
        SB();
      }
      { u32x4 t0 = wb[2], t1 = wb[3], t2 = wb[0]; wb[0] = t0; wb[1] = t1; wb[2] = t2; }
      STAMPL(10);
      wait_vm0();
      UNROLL for (int ks = 0; ks < 6; ++ks) xf[0][ks] = lds_ld(smem, XA(ks));
      MEMB();
      UNROLL for (int i = 0; i < 3; ++i) lds_st(smem, sl16 + i * 1024, pre[i]);
      MEMB();
      UNROLL for (int ks = 0; ks < 6; ++ks) xf[1][ks] = lds_ld(smem, t1adj + XA(ks));
      MEMB();
      x1[1][4] = PACKS(oc[1], 0);
      x1[1][5] = PACKS(oc[1], 1);
    }
    STAMPL(11);
    { int wy = win / 28, wx = win - wy * 28; ob = (unsigned)w * RS_IN + (unsigned)((wy * 8) * 224 + wx * 8) * 192u; }
    if (!more) {
      UNROLL for (int a = 0; a < 2; ++a) {
        UNROLL for (int ks = 0; ks < 6; ++ks) lds_st(smem, XA(ks), x1[a][ks]);
        MEMB();
        UNROLL for (int i = 0; i < 6; ++i) x1[a][i] = lds_ld(smem, sl16 + i * 1024);
        MEMB();
      }
      UNROLL for (int a = 0; a < 2; ++a)
        UNROLL for (int i = 0; i < 6; ++i) __builtin_nontemporal_store(x1[a][i], (u32x4*)(out + (size_t)(ob + a * ROWB + PUNI(i, RS_OUT)) + POFF(i, l16, m1out)));
      STAMPL(12);
      break;
    }
    STAMPL(12);
    win = nwin;
    ++iter;
  }
  STAMP_OUT;
  asm volatile(".Lswin_pf_end:");
}

extern "C" void kernel_launch(void* const* d_in, const int* in_sizes, int n_in,
                              void* d_out, int out_size, void* d_ws, size_t ws_size,
                              hipStream_t stream) {
  swin_kernel<<<256, 512, 0, stream>>>((const char*)d_in[0], (const char*)d_in[1], (const char*)d_in[2],
                                       (const char*)d_in[3], (const char*)d_in[4], (char*)d_out, (unsigned long long*)d_ws);
}
```

```cpp
#define BPREP_PRIO 2
#define CODEPF
#define CODEPF_COND wave == 3
#include <hip/hip_runtime.h>
#include <hip/hip_bf16.h>
#include <stdint.h>

#if defined(__HIP_DEVICE_COMPILE__)
#define NOPK __attribute__((target("no-packed-fp32-ops")))
#else
#define NOPK
#endif
#define DI __device__ __forceinline__ NOPK

typedef float f32x16 __attribute__((ext_vector_type(16)));
typedef float f32x4 __attribute__((ext_vector_type(4)));
typedef unsigned u32x4 __attribute__((ext_vector_type(4)));
typedef unsigned u32x2 __attribute__((ext_vector_type(2)));
typedef __bf16 bf16x8 __attribute__((ext_vector_type(8)));
typedef __bf16 bf16x2 __attribute__((ext_vector_type(2)));
typedef short s16x4 __attribute__((ext_vector_type(4)));
#define LAS __attribute__((address_space(3)))

constexpr int WF_OFF = 0;
constexpr int SLOT_OFF = 73728;
constexpr int SLOT_B = 9216;
constexpr int R_OFF = SLOT_OFF + 8 * SLOT_B;
constexpr int RP_OFF = R_OFF + 2048;
constexpr int CNT_OFF = RP_OFF + 1024;
constexpr int DUM_OFF = CNT_OFF + 128;
constexpr int LDS_BYTES = DUM_OFF + 9216;

constexpr unsigned RS_IN = 224u * 224u * 192u;
constexpr unsigned ROWB = 224u * 192u;
constexpr unsigned RS_OUT = 2u * ROWB;

DI f32x16 mfma32(u32x4 a, u32x4 b, f32x16 c) {
  return __builtin_amdgcn_mfma_f32_32x32x16_bf16(__builtin_bit_cast(bf16x8, a), __builtin_bit_cast(bf16x8, b), c, 0, 0, 0);
}
DI f32x4 mfma4(unsigned a0, unsigned a1, unsigned b0, unsigned b1, f32x4 c) {
  u32x2 a = {a0, a1}, b = {b0, b1};
  return __builtin_amdgcn_mfma_f32_4x4x4bf16_1k(__builtin_bit_cast(s16x4, a), __builtin_bit_cast(s16x4, b), c, 0, 0, 0);
}
DI unsigned pk(float lo, float hi) {
  bf16x2 p = {(__bf16)lo, (__bf16)hi};
  return __builtin_bit_cast(unsigned, p);
}
#define PACKS(x, s) u32x4{pk((x)[8 * (s)], (x)[8 * (s) + 1]), pk((x)[8 * (s) + 2], (x)[8 * (s) + 3]), \
                          pk((x)[8 * (s) + 4], (x)[8 * (s) + 5]), pk((x)[8 * (s) + 6], (x)[8 * (s) + 7])}
DI float fbits(unsigned u) { return __builtin_bit_cast(float, u); }
DI unsigned ubits(float f) { return __builtin_bit_cast(unsigned, f); }
DI float merge_add(float a, float b) {
  auto r = __builtin_amdgcn_permlane32_swap(ubits(a), ubits(b), false, false);
  return fbits(r[0]) + fbits(r[1]);
}
DI float xhalf_add(float a) { return merge_add(a, a); }
DI float xhalf_max(float a) {
  auto r = __builtin_amdgcn_permlane32_swap(ubits(a), ubits(a), false, false);
  return __builtin_fmaxf(fbits(r[0]), fbits(r[1]));
}
template <int I> DI float quad_bcast(float v) {
  return fbits((unsigned)__builtin_amdgcn_mov_dpp((int)ubits(v), I * 0x55, 0xf, 0xf, true));
}
DI u32x4 lds_ld(const char* smem, unsigned off) { return *(const u32x4*)(smem + off); }
DI void lds_st(char* smem, unsigned off, u32x4 v) { *(u32x4*)(smem + off) = v; }

DI void dma16(unsigned lds_dst, const char* sbase, unsigned voff) {
  unsigned keep;
  asm volatile("s_mov_b32 %0, m0\n\ts_mov_b32 m0, %1\n\ts_nop 0\n\tglobal_load_lds_dwordx4 %2, %3\n\ts_mov_b32 m0, %0"
               : "=&s"(keep) : "s"(lds_dst), "v"(voff), "s"(sbase) : "memory");
}
DI void dma16nt(unsigned lds_dst, const char* sbase, unsigned voff) {
  unsigned keep;
  asm volatile("s_mov_b32 %0, m0\n\ts_mov_b32 m0, %1\n\ts_nop 0\n\tglobal_load_lds_dwordx4 %2, %3 nt\n\ts_mov_b32 m0, %0"
               : "=&s"(keep) : "s"(lds_dst), "v"(voff), "s"(sbase) : "memory");
}
DI void wait_vm0() { asm volatile("s_waitcnt vmcnt(0)" ::: "memory"); }
DI void wait_lgkm0() { asm volatile("s_waitcnt lgkmcnt(0)" ::: "memory"); }
DI void wg_barrier() { asm volatile("s_waitcnt lgkmcnt(0)\n\ts_barrier" ::: "memory"); }

DI void grp_arrive(unsigned lds_addr) {
  unsigned a, o;
  asm volatile("v_mov_b32 %0, %2\n\tv_mov_b32 %1, 1\n\ts_waitcnt lgkmcnt(0)\n\ts_mov_b64 exec, 1\n\tds_add_u32 %0, %1\n\ts_mov_b64 exec, -1"
               : "=&v"(a), "=&v"(o) : "s"(lds_addr) : "memory");
}
DI void grp_wait(unsigned lds_addr, unsigned target) {
  unsigned a, v, sv;
  asm volatile("v_mov_b32 %0, %3\n"
               "1:\n\tds_read_b32 %1, %0\n\ts_waitcnt lgkmcnt(0)\n\tv_readfirstlane_b32 %2, %1\n\ts_nop 1\n\ts_cmp_ge_u32 %2, %4\n\t"
               "s_cbranch_scc1 2f\n\ts_sleep 1\n\ts_branch 1b\n2:"
               : "=&v"(a), "=&v"(v), "=&s"(sv) : "s"(lds_addr), "s"(target) : "memory", "scc");
}
DI void grp_wait_peeked(unsigned lds_addr, unsigned target, unsigned peeked) {
  unsigned a, v, sv;
  asm volatile("v_readfirstlane_b32 %2, %5\n\tv_mov_b32 %0, %3\n\ts_nop 0\n\ts_cmp_ge_u32 %2, %4\n\ts_cbranch_scc1 2f\n"
               "1:\n\tds_read_b32 %1, %0\n\ts_waitcnt lgkmcnt(0)\n\tv_readfirstlane_b32 %2, %1\n\ts_nop 1\n\ts_cmp_ge_u32 %2, %4\n\t"
               "s_cbranch_scc1 2f\n\ts_sleep 1\n\ts_branch 1b\n2:"
               : "=&v"(a), "=&v"(v), "=&s"(sv) : "s"(lds_addr), "s"(target), "v"(peeked) : "memory", "scc");
}
template <int PA, int PB> DI void set_prio(int grp) {
  asm volatile("s_cmp_eq_u32 %0, 0\n\ts_cbranch_scc1 1f\n\ts_setprio %2\n\ts_branch 2f\n1:\n\ts_setprio %1\n2:"
               :: "s"(grp), "i"(PA), "i"(PB) : "scc");
}

#define STAMP0(i)
#define STAMPL(i)
#define STAMP_DECL
#define STAMP_OUT
DI int swap23(int n) { return (n & 0x13) | ((n & 4) << 1) | ((n & 8) >> 1); }

#define SB() __builtin_amdgcn_sched_barrier(0)
#define MEMB() asm volatile("" ::: "memory")
#define UNROLL _Pragma("unroll")
#define PMODE 5
#define PRIO_DENSE() set_prio<3, 2>(pgrp)
#define PRIO_PURE() set_prio<1, 0>(pgrp)

__global__ __launch_bounds__(512, 2) NOPK void swin_kernel(const char* __restrict__ x, const char* __restrict__ wqkv_t,
                                                           const char* __restrict__ wo_t, const char* __restrict__ wqkv_s,
                                                           const char* __restrict__ wo_s, char* __restrict__ out, unsigned long long* dbg) {
  __shared__ __attribute__((aligned(16))) char smem[LDS_BYTES];
  const unsigned lds0 = (unsigned)(size_t)smem;
  const int lane = threadIdx.x & 63;
  const int wave = __builtin_amdgcn_readfirstlane(threadIdx.x >> 6);
  const int grp = wave >> 2, w = wave & 3;
  const int col = lane & 31, h = lane >> 5, p = col >> 2, t = col & 3;
  const unsigned l16 = (unsigned)lane * 16u;
  const int pgrp = __builtin_amdgcn_readfirstlane(blockIdx.x < 16 ? grp ^ 1 : grp);

  STAMP_DECL
  STAMP0(0);
  {
    unsigned long long pc; unsigned len;
    asm volatile("s_getpc_b64 %0\n.Lswin_pf_start:\n\ts_mov_b32 %1, .Lswin_pf_end-.Lswin_pf_start" : "=s"(pc), "=s"(len));
    if (CODEPF_COND) {
      UNROLL for (int i = 0; i < 8; ++i) {
        unsigned o = (unsigned)lane * 64u + i * 4096u;
        o = o < len ? o : 0u;
        unsigned keep;
        asm volatile("s_mov_b32 %0, m0\n\ts_mov_b32 m0, %1\n\ts_nop 0\n\tglobal_load_lds_dword %2, %3\n\ts_mov_b32 m0, %0"
                     : "=&s"(keep) : "s"(lds0 + DUM_OFF + 1024), "v"(o), "s"(pc) : "memory");
      }
    }
  }
  const unsigned m1in = lane < 32 ? l16 + 1024u : ((l16 - 512u) ^ 16u) + RS_IN;
  const unsigned m1out = lane < 32 ? l16 + 1024u : ((l16 - 512u) ^ 16u) + RS_OUT;
#define POFF(i, lb, m1) ((i) == 0 ? (lb) : (i) == 1 ? (m1) : (i) == 2 ? ((lb) ^ 16u) : (i) == 3 ? ((lb) ^ 32u) : (i) == 4 ? ((m1) ^ 32u) : ((lb) ^ 48u))
#define PUNI(i, RS) ((i) == 0 ? 0u : (i) == 1 ? 0u : (i) == 2 ? (RS) + 512u : (i) == 3 ? 2u * (RS) : (i) == 4 ? 2u * (RS) : 3u * (RS) + 512u)
#define OPAQUE(v) asm volatile("" : "+v"(v))
  const __amdgpu_buffer_rsrc_t xrs = __builtin_amdgcn_make_buffer_rsrc((void*)x, 0, 38535168, 0x00020000);
  int win = blockIdx.x + 256 * grp;
  u32x4 xf[2][6];
  unsigned xb0;
  { int wy = win / 28, wx = win - wy * 28; xb0 = (unsigned)((wy * 8 + 2 * w) * 224 + wx * 8) * 192u; }
#define XLOAD(n) xf[(n) / 6][(n) % 6] = __builtin_amdgcn_raw_buffer_load_b128(xrs, POFF((n) % 6, l16, m1in), xb0 + ((n) / 6) * ROWB + PUNI((n) % 6, RS_IN), 2)

  if (threadIdx.x < 32) *(unsigned*)(smem + CNT_OFF + threadIdx.x * 4) = 0u;
#define CN(g, o) (lds0 + CNT_OFF + (g) * 64 + (o))
  const unsigned slot = SLOT_OFF + wave * SLOT_B;
  const unsigned cntA = CNT_OFF + grp * 64 + 32, cntB = cntA + 4;
  const float inv96 = 1.0f / 96.0f;

  const unsigned xa_e = slot + (unsigned)((t * 96 + 12 * p + (h ^ t)) * 16);
  const unsigned xa_o = slot + (unsigned)((t * 96 + 12 * p + ((2 + h) ^ t)) * 16);
  const unsigned t1adj = t < 2 ? 6144u : (unsigned)-3072;
  const unsigned sl16 = slot + l16;
#define XA(ks) ((((ks) & 1) ? xa_o : xa_e) + 64 * ((ks) >> 1))

  {
    constexpr unsigned QK = SLOT_OFF + 36864, RWO = WF_OFF + 36864, RWV = RWO + 18432;
    const char* xb = x + xb0;
#define XFIRST()                                                                                                 \
  {                                                                                                              \
    UNROLL for (int i = 0; i < 6; ++i) dma16nt(lds0 + slot + i * 1024, xb + PUNI(i, RS_IN), POFF(i, l16, m1in)); \
    UNROLL for (int i = 0; i < 3; ++i) dma16nt(lds0 + slot + 6144 + i * 1024, xb + ROWB + PUNI(i, RS_IN), POFF(i, l16, m1in)); \
    UNROLL for (int n = 9; n < 12; ++n) XLOAD(n);                                                                \
    MEMB();                                                                                                      \
  }
    unsigned qo[3], qf[3], vv[3];
    if (grp) {
      UNROLL for (int m = 0; m < 3; ++m) {
        int idx = m * 64 + lane, row = idx / 24, qq = idx - row * 24;
        int c = qq ^ (row >> 1);
        qo[m] = (unsigned)(row * 576 + c * 16);
        qf[m] = (unsigned)(row * 576 + (c ^ 4) * 16);
      }
#define QTRIPLE(src, tr, odd) UNROLL for (int m = 0; m < 3; ++m) dma16(lds0 + QK + (tr) * 3072 + m * 1024, (src) + (tr) * 8 * 576, (odd) ? qf[m] : qo[m])
      UNROLL for (int m = 0; m < 3; ++m) { const unsigned sel = (w & 1) ? qf[m] : qo[m]; vv[m] = sel; }
      UNROLL for (int i = 0; i < 3; ++i)
        UNROLL for (int m = 0; m < 3; ++m) dma16(lds0 + QK + (w + 4 * i) * 3072 + m * 1024, wqkv_t + (w + 4 * i) * 8 * 576, vv[m]);
    }
    wg_barrier();
    if (!grp) XFIRST();
    const int pn = swap23(col);
    unsigned qb[4], vb[2];
    UNROLL for (int e = 0; e < 4; ++e) qb[e] = QK + (unsigned)(pn * 384 + (((2 * e) | h) ^ ((pn >> 1) & 7)) * 16);
    UNROLL for (int e = 0; e < 2; ++e) vb[e] = RWV + (unsigned)(pn * 192 + (((2 * e) | h) ^ ((pn >> 2) & 3)) * 16);
    const unsigned tb = RWO + (unsigned)((8 * h + ((lane & 15) >> 2)) * 192 + (16 * ((lane >> 4) & 1) + 8 * (lane & 1) + 4 * ((lane >> 1) & 1)) * 2 + w * 64);
    UNROLL for (int e = 0; e < 4; ++e) OPAQUE(qb[e]);
    if (grp) { wait_vm0(); grp_arrive(CN(0, 0)); }
    STAMP0(1);
#define QKF(rt, c2) lds_ld(smem, qb[(c2) & 3] + (rt) * 12288 + ((c2) >> 2) * 128)
#define QKR(ro, c2) lds_ld(smem, qb[(c2) & 3] + (ro) + ((c2) >> 2) * 128)
#define VF(rt, kk) lds_ld(smem, vb[(kk) & 1] + (rt) * 6144 + ((kk) >> 1) * 64)
    if (wave == 7) {
      UNROLL for (int m = 0; m < 3; ++m) {
        int idx = m * 64 + lane, row2 = idx / 12, q2 = idx - row2 * 12;
        vv[m] = (unsigned)(row2 * 576 + 384 + (q2 ^ (row2 >> 2)) * 16);
      }
      for (int g = 0; g < 2; ++g) {
        const char* wq = g ? wqkv_s : wqkv_t;
        const char* wo = g ? wo_s : wo_t;
        if (g) {
          grp_wait(CN(0, 16), 6);
          for (int tr = 0; tr < 12; tr += 2) { QTRIPLE(wq, tr, false); QTRIPLE(wq, tr + 1, true); }
          wait_vm0();
          grp_arrive(CN(1, 0));
          grp_wait(CN(0, 12), 3);
        }
        for (int i = 0; i < 6; ++i) {
          UNROLL for (int m = 0; m < 3; ++m) dma16(lds0 + RWO + i * 3072 + m * 1024, wo + i * 3072 + m * 1024, l16);
          UNROLL for (int m = 0; m < 3; ++m) dma16(lds0 + RWV + i * 3072 + m * 1024, wq + i * 16 * 576, vv[m]);
        }
        wait_vm0();
        grp_arrive(CN(g, 4));
      }
    }
    for (int g = 0; g <= grp && w < 3; ++g) {
      const unsigned RA = WF_OFF + g * 36864, RB = RA + 18432;
      const unsigned rp = RP_OFF + g * 512;
      const unsigned cn = lds0 + CNT_OFF + g * 64;
      grp_wait(cn + 0, g ? 1 : 4);
      if (grp) {
        __builtin_amdgcn_s_setprio(3);
        u32x4 af[3][6], bb[6];
        UNROLL for (int kk = 0; kk < 6; ++kk) { bb[kk] = QKR(w * 12288, kk); af[0][kk] = QKF(0, 6 + kk); }
        UNROLL for (int ct = 1; ct < 3; ++ct)
          UNROLL for (int kk = 0; kk < 6; ++kk) af[ct][kk] = QKF(ct, 6 + kk);
        {
          const int rt = w;
          SB();
          f32x16 d = {};
          UNROLL for (int ct = 0; ct < 3; ++ct)
            UNROLL for (int kk = 0; kk < 6; ++kk) d = mfma32(af[ct][kk], bb[kk], d);
          float r0 = (d[0] + d[1]) + (d[2] + d[3]), r1 = (d[4] + d[5]) + (d[6] + d[7]);
          float r2_ = (d[8] + d[9]) + (d[10] + d[11]), r3 = (d[12] + d[13]) + (d[14] + d[15]);
          float rs = xhalf_add((r0 + r1) + (r2_ + r3));
          *(float*)(smem + rp + (32 * rt + pn) * 4) = rs * -inv96;
          grp_arrive(cn + 8);
          grp_arrive(cn + 16);
        }
        __builtin_amdgcn_s_setprio(BPREP_PRIO);
      }
      {
        f32x16 DA[3];
        float a0 = 0.f, a1 = 0.f;
        if (g == grp) {
          u32x4 bb[6], af[3][6];
          UNROLL for (int kk = 0; kk < 6; ++kk) { bb[kk] = QKR(w * 12288, 6 + kk); af[0][kk] = QKF(0, kk); }
          UNROLL for (int rt = 1; rt < 3; ++rt)
            UNROLL for (int kk = 0; kk < 6; ++kk) af[rt][kk] = QKF(rt, kk);
          SB();
          UNROLL for (int rt = 0; rt < 3; ++rt) {
            f32x16 d = {};
            UNROLL for (int kk = 0; kk < 6; ++kk) d = mfma32(af[rt][kk], bb[kk], d);
            DA[rt] = d;
            if (rt == 1) grp_arrive(cn + 16);
            if (rt > 0) { UNROLL for (int i = 0; i < 16; i += 2) { a0 += DA[rt - 1][i]; a1 += DA[rt - 1][i + 1]; } }
            SB();
          }
          UNROLL for (int i = 0; i < 16; i += 2) { a0 += DA[2][i]; a1 += DA[2][i + 1]; }
        }
        if (grp) {
          grp_wait(cn + 4, 1);
          u32x4 bfr[6], av[2][6];
          UNROLL for (int kk = 0; kk < 6; ++kk) {
            s16x4 lo = __builtin_amdgcn_ds_read_tr16_b64_v4i16((LAS s16x4*)(smem + tb + kk * 3072));
            s16x4 hi = __builtin_amdgcn_ds_read_tr16_b64_v4i16((LAS s16x4*)(smem + tb + kk * 3072 + 768));
            u32x2 l2 = __builtin_bit_cast(u32x2, lo), h2 = __builtin_bit_cast(u32x2, hi);
            bfr[kk] = u32x4{l2[0], l2[1], h2[0], h2[1]};
            av[0][kk] = VF(0, kk);
          }
          UNROLL for (int kk = 0; kk < 6; ++kk) av[1][kk] = VF(1, kk);
          SB();
          f32x16 DB[3];
          float c0 = 0.f, c1 = 0.f;
          UNROLL for (int rt = 0; rt < 3; ++rt) {
            f32x16 d = {};
            UNROLL for (int kk = 0; kk < 6; ++kk) {
              d = mfma32(av[rt & 1][kk], bfr[kk], d);
              if (rt == 0) av[0][kk] = VF(2, kk);
            }
            DB[rt] = d;
            if (rt == 1) grp_arrive(cn + 12);
            if (rt > 0) { UNROLL for (int i = 0; i < 16; i += 2) { c0 += DB[rt - 1][i]; c1 += DB[rt - 1][i + 1]; } }
            SB();
          }
          UNROLL for (int i = 0; i < 16; i += 2) { c0 += DB[2][i]; c1 += DB[2][i + 1]; }
          const float cm = xhalf_add(c0 + c1) * inv96;
          if (g) grp_wait(cn + 12, 3);
          UNROLL for (int rt = 0; rt < 3; ++rt) {
            UNROLL for (int i = 0; i < 16; ++i) DB[rt][i] -= cm;
            lds_st(smem, RB + (w * 6 + 2 * rt) * 1024 + l16, PACKS(DB[rt], 0));
            lds_st(smem, RB + (w * 6 + 2 * rt + 1) * 1024 + l16, PACKS(DB[rt], 1));
          }
          grp_arrive(cn + 24);
        }
        if (g == grp) {
          grp_wait(cn + 8, 3);
          f32x4 rm[12];
          UNROLL for (int n = 0; n < 12; ++n) rm[n] = *(const f32x4*)(smem + rp + (32 * (n >> 2) + 16 * ((n >> 1) & 1) + 8 * h + 4 * (n & 1)) * 4);
          UNROLL for (int n = 0; n < 12; ++n) {
            a0 += rm[n][0] + rm[n][2]; a1 += rm[n][1] + rm[n][3];
            UNROLL for (int i = 0; i < 4; ++i) DA[n >> 2][4 * (n & 3) + i] += rm[n][i];
          }
          const float fA = 0.10206207261596575f * 1.4426950408889634f;
          const float cadd = xhalf_add(a0 + a1) * (-fA * inv96);
          UNROLL for (int rt = 0; rt < 3; ++rt) {
            UNROLL for (int i = 0; i < 16; ++i) DA[rt][i] = __builtin_fmaf(DA[rt][i], fA, cadd);
            lds_st(smem, RA + (w * 6 + 2 * rt) * 1024 + l16, PACKS(DA[rt], 0));
            lds_st(smem, RA + (w * 6 + 2 * rt + 1) * 1024 + l16, PACKS(DA[rt], 1));
          }
          grp_arrive(cn + 20);
        }
      }
    }
#undef QKF
#undef QKR
#undef VF
#undef QTRIPLE
    STAMP0(2);
    if (grp) {
      __builtin_amdgcn_s_setprio(0);
      grp_wait(CN(1, 16), 6);
      XFIRST();
    }
#undef XFIRST
  }
  wait_vm0();
  UNROLL for (int ks = 0; ks < 6; ++ks) xf[0][ks] = lds_ld(smem, XA(ks));
  MEMB();
  UNROLL for (int i = 0; i < 3; ++i) lds_st(smem, sl16 + i * 1024, xf[1][3 + i]);
  MEMB();
  UNROLL for (int ks = 0; ks < 6; ++ks) xf[1][ks] = lds_ld(smem, t1adj + XA(ks));
  MEMB();

  const bool own = (t == w);
  const unsigned exo = (unsigned)(h * 384 + p * 48 + ((t ^ w) - 1) * 16);
  const unsigned ex_w = own ? (unsigned)(DUM_OFF + h * 128 + ((3 * (p & 6) + 6 + (p & 1)) & 7) * 16)
                            : SLOT_OFF + (grp * 4 + t) * SLOT_B + exo;
  const unsigned ex_r = slot + exo;
  const unsigned long long notown = ~(0x1111111111111111ull << w);
  const unsigned rbase = R_OFF + wave * 256;
  unsigned wS = l16 + 36864u;
  asm volatile("" : "+v"(wS));
#define WLD(i) lds_ld(smem, ((i) < 36 ? l16 + (i) * 1024 : wS + ((i) - 36) * 1024))

  const unsigned ONES = 0x3F803F80u;
  const unsigned id0 = t == 0 ? 0x00003F80u : (t == 1 ? 0x3F800000u : 0u);
  const unsigned id1 = t == 2 ? 0x00003F80u : (t == 3 ? 0x3F800000u : 0u);
  unsigned msk[4];
  UNROLL for (int g = 0; g < 4; ++g) msk[g] = ((col >> 3) == g && h == (p & 1)) ? 0xffffffffu : 0u;
  const f32x4 z4 = {0.f, 0.f, 0.f, 0.f};
  const f32x16 zz = {};

  grp_wait(CN(0, 20), 3);

  u32x4 wb[4];
  wb[0] = WLD(0); wb[1] = WLD(1); wb[2] = WLD(2);
  u32x4 x1[2][6];
  UNROLL for (int a = 0; a < 2; ++a) UNROLL for (int i = 0; i < 6; ++i) x1[a][i] = wb[0];
  unsigned iter = 1;

#define T1_STAGE(OUTP, ob)                                                                                       \
  {                                                                                                              \
    f32x4 sm0 = z4, sm1 = z4, g0 = z4, g1 = z4;                                                                  \
    unsigned b1v;                                                                                                \
    UNROLL for (int ks = 0; ks < 6; ++ks) {                                                                      \
      if (ks < 4) {                                                                                              \
        if (ks == 0) { UNROLL for (int k2 = 0; k2 < 6; ++k2) lds_st(smem, XA(k2), x1[0][k2]); MEMB(); }          \
        if (ks == 1) { UNROLL for (int k2 = 0; k2 < 6; ++k2) x1[0][k2] = lds_ld(smem, sl16 + k2 * 1024); MEMB(); } \
        if (ks == 2) { UNROLL for (int k2 = 0; k2 < 6; ++k2) lds_st(smem, XA(k2), x1[1][k2]); MEMB(); }          \
        if (ks == 3) { UNROLL for (int k2 = 0; k2 < 6; ++k2) x1[1][k2] = lds_ld(smem, sl16 + k2 * 1024); MEMB(); } \
      }                                                                                                          \
      UNROLL for (int c = 0; c < 2; ++c) {                                                                       \
        sm0 = mfma4(ONES, ONES, xf[0][ks][2 * c], xf[0][ks][2 * c + 1], sm0);                                    \
        sm1 = mfma4(ONES, ONES, xf[1][ks][2 * c], xf[1][ks][2 * c + 1], sm1);                                    \
        g0 = mfma4(xf[0][ks][2 * c], xf[0][ks][2 * c + 1], xf[0][ks][2 * c], xf[0][ks][2 * c + 1], g0);          \
        g1 = mfma4(xf[1][ks][2 * c], xf[1][ks][2 * c + 1], xf[1][ks][2 * c], xf[1][ks][2 * c + 1], g1);          \
      }                                                                                                          \
      SB();                                                                                                      \
    }                                                                                                            \
    UNROLL for (int i = 0; i < 18; ++i) {                                                                        \
      const int tt = i / 6, ks = i % 6;                                                                          \
      wb[(i + 3) & 3] = WLD(i + 3);                                                                              \
      acc[tt & 1][0] = mfma32(wb[i & 3], xf[0][ks], ks == 0 ? zz : acc[tt & 1][0]);                              \
      acc[tt & 1][1] = mfma32(wb[i & 3], xf[1][ks], ks == 0 ? zz : acc[tt & 1][1]);                              \
      if (tt > 0 && ks >= 1 && ks <= 4) {                                                                        \
        const int a = (ks - 1) >> 1, s = (ks - 1) & 1;                                                           \
        uf[a][2 * (tt - 1) + s] = PACKS(acc[(tt - 1) & 1][a], s);                                                \
      }                                                                                                          \
      if ((i % 3) != 2) {                                                                                        \
        const int n_ = 2 * (i / 3) + (i % 3);                                                                    \
        unsigned lb_ = l16, m1_ = m1out; OPAQUE(lb_); OPAQUE(m1_);                                               \
        __builtin_amdgcn_raw_buffer_store_b128(x1[n_ / 6][n_ % 6],                                               \
            __builtin_amdgcn_make_buffer_rsrc(out + (size_t)((ob) + (n_ / 6) * ROWB + PUNI(n_ % 6, RS_OUT)), 0, (OUTP) ? 0x40000 : 0, 0x00020000), \
            POFF(n_ % 6, lb_, m1_), 0, 2);                                                                       \
      }                                                                                                          \
      if (i == 2) grp_arrive(lds0 + cntB);                                                                       \
      if (i == 11) { MEMB(); b1v = *(const unsigned*)(smem + CNT_OFF + 24); }                                    \
      if (i == 14) grp_wait_peeked(CN(0, 24), 3, b1v);                                                           \
      if (i == 1) {                                                                                              \
        float q0a = (t & 1) ? g0[1] : g0[0], q0b = (t & 1) ? g0[3] : g0[2];                                      \
        float q1a = (t & 1) ? g1[1] : g1[0], q1b = (t & 1) ? g1[3] : g1[2];                                      \
        float q0 = (t & 2) ? q0b : q0a, q1 = (t & 2) ? q1b : q1a;                                                \
        float mean = merge_add(sm0[0], sm1[0]) * inv96;                                                          \
        float var = merge_add(q0, q1) * inv96 - mean * mean;                                                     \
        var = __builtin_fmaxf(var, 0.f);                                                                         \
        rr = __builtin_amdgcn_rsqf(var + 1e-5f);                                                                 \
      }                                                                                                          \
      SB();                                                                                                      \
    }                                                                                                            \
  }

  unsigned ob = 0;
  for (;;) {
    STAMPL(3);
    float rr;
    u32x4 uf[2][6];
    f32x16 acc[2][2];
    T1_STAGE((iter > 1), ob);
    STAMPL(4);
    u32x4 pt[2][2];
    {
      f32x4 s0, s1, s0b, s1b;
      f32x16 vac[2][2];
      f32x16 oac[2];
      u32x4 vpk[2][2];
      float rs0, rs1, rs2, rs3, y0, y1, y2, y3, e0, e1, e2, e3;
      unsigned cbv;
#define O_STEP(tt)                                                                                               \
  UNROLL for (int a = 0; a < 2; ++a) {                                                                           \
    f32x16 o;                                                                                                    \
    UNROLL for (int g = 0; g < 4; ++g) {                                                                         \
      f32x4 r4 = mfma4(id0, id1, xf[a][2 * (tt) + (g >> 1)][2 * (g & 1)], xf[a][2 * (tt) + (g >> 1)][2 * (g & 1) + 1], z4); \
      UNROLL for (int e = 0; e < 4; ++e) o[4 * g + e] = r4[e];                                                   \
    }                                                                                                            \
    oac[a] = o;                                                                                                  \
  }                                                                                                              \
  oac[0] = mfma32(vpk[0][0], pt[0][0], oac[0]);                                                                  \
  oac[0] = mfma32(vpk[0][1], pt[0][1], oac[0]);                                                                  \
  oac[1] = mfma32(vpk[1][0], pt[1][0], oac[1]);                                                                  \
  oac[1] = mfma32(vpk[1][1], pt[1][1], oac[1]);
#define S2_STEP(tt, a, s)                                                                                        \
  x1[a][2 * (tt) + (s)] = PACKS(oac[a], s);                                                                      \
  lds_st(smem, ex_w + ((a) * 6 + 2 * (tt) + (s)) * 768, x1[a][2 * (tt) + (s)]);
      UNROLL for (int j = 0; j < 18; ++j) {
        const int tt = j / 6, ks = j % 6;
        if (j < 15) wb[(j + 18 + 3) & 3] = WLD(j + 18 + 3);
        vac[tt & 1][0] = mfma32(xf[0][ks], wb[(j + 18) & 3], ks == 0 ? zz : vac[tt & 1][0]);
        vac[tt & 1][1] = mfma32(xf[1][ks], wb[(j + 18) & 3], ks == 0 ? zz : vac[tt & 1][1]);
        if (j == 0) { uf[0][4] = PACKS(acc[0][0], 0); uf[0][5] = PACKS(acc[0][0], 1); }
        if (j == 4) { MEMB(); cbv = *(const unsigned*)(smem + cntB); }
        if (j == 1) {
          uf[1][4] = PACKS(acc[0][1], 0); uf[1][5] = PACKS(acc[0][1], 1);
          SB();
          s0 = z4; s1 = z4; s0b = z4; s1b = z4;
          UNROLL for (int k2 = 0; k2 < 6; ++k2) {
            s0 = mfma4(xf[0][k2][0], xf[0][k2][1], uf[0][k2][0], uf[0][k2][1], s0);
            s1 = mfma4(xf[1][k2][0], xf[1][k2][1], uf[1][k2][0], uf[1][k2][1], s1);
            s0b = mfma4(xf[0][k2][2], xf[0][k2][3], uf[0][k2][2], uf[0][k2][3], s0b);
            s1b = mfma4(xf[1][k2][2], xf[1][k2][3], uf[1][k2][2], uf[1][k2][3], s1b);
          }
          rs0 = quad_bcast<0>(rr); rs1 = quad_bcast<1>(rr); rs2 = quad_bcast<2>(rr); rs3 = quad_bcast<3>(rr);
        }
        if (j == 2) {
          y0 = merge_add(s0[0] + s0b[0], s1[0] + s1b[0]) * rs0;
          y1 = merge_add(s0[1] + s0b[1], s1[1] + s1b[1]) * rs1;
        }
        if (j == 3) {
          y2 = merge_add(s0[2] + s0b[2], s1[2] + s1b[2]) * rs2;
          y3 = merge_add(s0[3] + s0b[3], s1[3] + s1b[3]) * rs3;
        }
        if (j == 4) {
          float mx = __builtin_fmaxf(__builtin_fmaxf(y0, y1), __builtin_fmaxf(y2, y3));
          float cc = -rr * mx;
          e0 = __builtin_amdgcn_exp2f(__builtin_fmaf(y0, rr, cc));
          e1 = __builtin_amdgcn_exp2f(__builtin_fmaf(y1, rr, cc));
          e2 = __builtin_amdgcn_exp2f(__builtin_fmaf(y2, rr, cc));
          e3 = __builtin_amdgcn_exp2f(__builtin_fmaf(y3, rr, cc));
        }
        if (j == 5) {
          float inv = __builtin_amdgcn_rcpf((e0 + e1) + (e2 + e3));
          unsigned p01 = pk(e0 * rs0 * inv, e1 * rs1 * inv);
          unsigned p23 = pk(e2 * rs2 * inv, e3 * rs3 * inv);
          auto a = __builtin_amdgcn_permlane32_swap(p01, p01, false, false);
          auto b = __builtin_amdgcn_permlane32_swap(p23, p23, false, false);
          UNROLL for (int ti = 0; ti < 2; ++ti)
            UNROLL for (int s = 0; s < 2; ++s)
              pt[ti][s] = u32x4{a[ti] & msk[2 * s], b[ti] & msk[2 * s], a[ti] & msk[2 * s + 1], b[ti] & msk[2 * s + 1]};
        }
        if (tt > 0) {
          if (ks == 0) { vpk[0][0] = PACKS(vac[(tt - 1) & 1][0], 0); vpk[0][1] = PACKS(vac[(tt - 1) & 1][0], 1); }
          if (ks == 1) {
            vpk[1][0] = PACKS(vac[(tt - 1) & 1][1], 0); vpk[1][1] = PACKS(vac[(tt - 1) & 1][1], 1);
            SB();
            O_STEP(tt - 1);
            if (tt == 1) {
              grp_wait_peeked(lds0 + cntB, 4 * iter, cbv);
            }
          }
          if (ks >= 2) { S2_STEP(tt - 1, (ks - 2) >> 1, (ks - 2) & 1); }
        }
        SB();
      }
      vpk[0][0] = PACKS(vac[0][0], 0); vpk[0][1] = PACKS(vac[0][0], 1);
      vpk[1][0] = PACKS(vac[0][1], 0); vpk[1][1] = PACKS(vac[0][1], 1);
      O_STEP(2);
      S2_STEP(2, 0, 0); S2_STEP(2, 0, 1); S2_STEP(2, 1, 0); S2_STEP(2, 1, 1);
#undef O_STEP
#undef S2_STEP
    }
    STAMPL(5);
    grp_arrive(lds0 + cntA);
    grp_wait(CN(1, 20), 3);
    wb[0] = WLD(36); wb[1] = WLD(37); wb[2] = WLD(38);
    grp_wait(lds0 + cntA, 4 * iter);
    asm volatile("s_mov_b64 exec, %13\n\t"
                 "ds_read_b128 %0, %12\n\tds_read_b128 %1, %12 offset:768\n\tds_read_b128 %2, %12 offset:1536\n\t"
                 "ds_read_b128 %3, %12 offset:2304\n\tds_read_b128 %4, %12 offset:3072\n\tds_read_b128 %5, %12 offset:3840\n\t"
                 "ds_read_b128 %6, %12 offset:4608\n\tds_read_b128 %7, %12 offset:5376\n\tds_read_b128 %8, %12 offset:6144\n\t"
                 "ds_read_b128 %9, %12 offset:6912\n\tds_read_b128 %10, %12 offset:7680\n\tds_read_b128 %11, %12 offset:8448\n\t"
                 "s_mov_b64 exec, -1\n\ts_waitcnt lgkmcnt(0)"
                 : "+v"(x1[0][0]), "+v"(x1[0][1]), "+v"(x1[0][2]), "+v"(x1[0][3]), "+v"(x1[0][4]), "+v"(x1[0][5]),
                   "+v"(x1[1][0]), "+v"(x1[1][1]), "+v"(x1[1][2]), "+v"(x1[1][3]), "+v"(x1[1][4]), "+v"(x1[1][5])
                 : "v"(lds0 + ex_r), "s"(notown) : "memory");
    float rq[2];
    const unsigned rka = rbase + h * 16;
#define RKLD(G) (*(const f32x4*)(smem + rka + (G) * 32))
    f32x4 rkb[3];
    STAMPL(6);
    const int nwin = win + 512;
    const bool more = nwin < 784;
    unsigned xo;
    { int nw = more ? nwin : win; int wy = nw / 28, wx = nw - wy * 28; xo = (unsigned)((wy * 8 + 2 * w) * 224 + wx * 8) * 192u; }
    const char* xb = x + xo;
    u32x4 pre[3];
    SB();
#define SW(k) ((k) < 18 ? 36 + (k) : ((k) < 54 ? 18 + (k) : (k) - 54))
    u32x4 pf[2][4];
    u32x4 vp[3][4];
    f32x16 sa[2][2];
    u32x4 uq[6];
    f32x16 ua[2];
    float mxv, ccv, l0, l1, invv;
#define U_STEP(q, i)                                                                                             \
  {                                                                                                              \
    const int tt_ = (i) / 6, ks_ = (i) % 6, k_ = 18 * (q) + (i);                                                 \
    wb[(k_ + 3) & 3] = WLD(SW(k_ + 3));                                                                          \
    ua[tt_ & 1] = mfma32(wb[k_ & 3], x1[q][ks_], ks_ == 0 ? zz : ua[tt_ & 1]);                                   \
    if (tt_ > 0 && (ks_ == 2 || ks_ == 3)) uq[2 * (tt_ - 1) + (ks_ - 2)] = PACKS(ua[(tt_ - 1) & 1], ks_ - 2);    \
  }
#define S_STEP(q, ks_)                                                                                           \
  {                                                                                                              \
    sa[q][0] = mfma32(x1[0][ks_], uq[ks_], (ks_) == 0 ? zz : sa[q][0]);                                          \
    sa[q][1] = mfma32(x1[1][ks_], uq[ks_], (ks_) == 0 ? zz : sa[q][1]);                                          \
    if ((ks_) == 1) uq[4] = PACKS(ua[0], 0);                                                                     \
    if ((ks_) == 2) uq[5] = PACKS(ua[0], 1);                                                                     \
  }
#define P1(q, G)                                                                                                 \
  {                                                                                                              \
    const int n_ = 16 * (q) + (G);                                                                               \
    MEMB(); rkb[(n_ + 2) % 3] = RKLD(((G) + 2) & 7);                                                             \
    UNROLL for (int e = 0; e < 4; ++e) sa[q][(G) >> 2][4 * ((G) & 3) + e] *= rkb[n_ % 3][e];                     \
    mxv = (G) == 0 ? sa[q][0][0] : __builtin_fmaxf(mxv, sa[q][(G) >> 2][4 * ((G) & 3)]);                         \
    mxv = __builtin_fmaxf(mxv, sa[q][(G) >> 2][4 * ((G) & 3) + 1]);                                              \
    mxv = __builtin_fmaxf(__builtin_fmaxf(mxv, sa[q][(G) >> 2][4 * ((G) & 3) + 2]), sa[q][(G) >> 2][4 * ((G) & 3) + 3]); \
  }
#define X1(q) { mxv = xhalf_max(mxv); ccv = -rq[q] * mxv; l0 = 0.f; l1 = 0.f; }
#define P2H(q, H)                                                                                                \
  {                                                                                                              \
    const int b_ = (H) >> 3, i_ = 2 * ((H) & 7), G_ = (H) >> 1, n_ = 16 * (q) + 8 + G_;                          \
    if (((H) & 1) == 0 && n_ < 30) { MEMB(); rkb[(n_ + 2) % 3] = RKLD((G_ + 2) & 7); }                           \
    const float ea = __builtin_amdgcn_exp2f(__builtin_fmaf(sa[q][b_][i_], rq[q], ccv));                          \
    const float eb = __builtin_amdgcn_exp2f(__builtin_fmaf(sa[q][b_][i_ + 1], rq[q], ccv));                      \
    l0 += ea; l1 += eb;                                                                                          \
    sa[q][b_][i_] = ea * rkb[n_ % 3][2 * ((H) & 1)]; sa[q][b_][i_ + 1] = eb * rkb[n_ % 3][2 * ((H) & 1) + 1];    \
  }
#define X2(q) { invv = __builtin_amdgcn_rcpf(xhalf_add(l0 + l1)); }
#define P3(q, F)                                                                                                 \
  {                                                                                                              \
    UNROLL for (int e = 0; e < 8; ++e) sa[q][(F) >> 1][8 * ((F) & 1) + e] *= invv;                               \
    pf[q][F] = PACKS(sa[q][(F) >> 1], (F) & 1);                                                                  \
  }
#define XPF(n)                                                                                                   \
  {                                                                                                              \
    unsigned lb_ = l16, m1_ = m1in; OPAQUE(lb_); OPAQUE(m1_);                                                    \
    if ((n) < 6) dma16nt(lds0 + slot + (n) * 1024, xb + PUNI((n) % 6, RS_IN), POFF((n) % 6, lb_, m1_));          \
    else if ((n) < 9) dma16nt(lds0 + slot + (n) * 1024, xb + ROWB + PUNI((n) % 6, RS_IN), POFF((n) % 6, lb_, m1_)); \
    else pre[(n) >= 9 ? (n) - 9 : 0] = __builtin_amdgcn_raw_buffer_load_b128(xrs, POFF((n) % 6, lb_, m1_), xo + ROWB + PUNI((n) % 6, RS_IN), 2); \
  }
    {
      f32x4 sm0 = z4, sm1 = z4, g0 = z4, g1 = z4;
      UNROLL for (int ks = 0; ks < 6; ++ks) {
        UNROLL for (int c = 0; c < 2; ++c) {
          sm0 = mfma4(ONES, ONES, x1[0][ks][2 * c], x1[0][ks][2 * c + 1], sm0);
          sm1 = mfma4(ONES, ONES, x1[1][ks][2 * c], x1[1][ks][2 * c + 1], sm1);
          g0 = mfma4(x1[0][ks][2 * c], x1[0][ks][2 * c + 1], x1[0][ks][2 * c], x1[0][ks][2 * c + 1], g0);
          g1 = mfma4(x1[1][ks][2 * c], x1[1][ks][2 * c + 1], x1[1][ks][2 * c], x1[1][ks][2 * c + 1], g1);
        }
        SB();
      }
      UNROLL for (int i = 0; i < 18; ++i) {
        U_STEP(0, i);
        if (i == 1) {
          float q0a = (t & 1) ? g0[1] : g0[0], q0b = (t & 1) ? g0[3] : g0[2];
          float q1a = (t & 1) ? g1[1] : g1[0], q1b = (t & 1) ? g1[3] : g1[2];
          float q0 = (t & 2) ? q0b : q0a, q1 = (t & 2) ? q1b : q1a;
          float mean = merge_add(sm0[0], sm1[0]) * inv96;
          float var = merge_add(q0, q1) * inv96 - mean * mean;
          var = __builtin_fmaxf(var, 0.f);
          float r2 = __builtin_amdgcn_rsqf(var + 1e-5f);
          *(float*)(smem + rbase + lane * 4) = r2;
          auto rb = __builtin_amdgcn_permlane32_swap(ubits(r2), ubits(r2), false, false);
          rq[0] = fbits(rb[0]); rq[1] = fbits(rb[1]);
          MEMB();
        }
        if (i >= 3 && i < 15) XPF(i - 3);
        SB();
      }
    }
    UNROLL for (int ks = 0; ks < 6; ++ks) { S_STEP(0, ks); if (ks == 3) rkb[0] = RKLD(0); if (ks == 4) rkb[1] = RKLD(1); SB(); }
    STAMPL(7);
    PRIO_DENSE();
    UNROLL for (int i = 0; i < 18; ++i) {
      U_STEP(1, i);
      if (i >= 1 && i <= 4) { P1(0, 2 * (i - 1)); P1(0, 2 * (i - 1) + 1); }
      if (i == 5) X1(0);
      if (i >= 6) P2H(0, i - 6);
      SB();
    }
    UNROLL for (int ks = 0; ks < 6; ++ks) {
      S_STEP(1, ks);
      if (ks < 2) { P2H(0, 12 + 2 * ks); P2H(0, 13 + 2 * ks); }
      if (ks == 2) X2(0);
      if (ks == 3) { P3(0, 0); }
      if (ks == 4) { P3(0, 1); P3(0, 2); }
      if (ks == 5) { P3(0, 3); }
      SB();
    }
    STAMPL(8);
    {
      f32x16 v0, v1;
      UNROLL for (int j = 0; j < 18; ++j) {
        const int tt = j / 6, ks = j % 6, k = 36 + j;
        wb[(k + 3) & 3] = WLD(SW(k + 3));
        if (ks == 0 && tt > 0) {
          vp[tt - 1][0] = PACKS(v0, 0); vp[tt - 1][1] = PACKS(v0, 1);
          v0 = mfma32(x1[0][ks], wb[k & 3], zz);
          SB();
          vp[tt - 1][2] = PACKS(v1, 0); vp[tt - 1][3] = PACKS(v1, 1);
          v1 = mfma32(x1[1][ks], wb[k & 3], zz);
        } else {
          v0 = mfma32(x1[0][ks], wb[k & 3], ks == 0 ? zz : v0);
          v1 = mfma32(x1[1][ks], wb[k & 3], ks == 0 ? zz : v1);
        }
        if (j == 1) { P1(1, 0); P1(1, 1); P1(1, 2); }
        if (j == 2) { P1(1, 3); P1(1, 4); P1(1, 5); }
        if (j == 3) { P1(1, 6); P1(1, 7); X1(1); }
        if (j == 4) { P2H(1, 0); P2H(1, 1); }
        if (j == 5) { P2H(1, 2); P2H(1, 3); }
        if (j == 7) { P2H(1, 4); }
        if (j == 8) { P2H(1, 5); }
        if (j == 9) { P2H(1, 6); P2H(1, 7); }
        if (j == 10) { P2H(1, 8); P2H(1, 9); }
        if (j == 11) { P2H(1, 10); P2H(1, 11); }
        if (j == 13) { P2H(1, 12); P2H(1, 13); }
        if (j == 14) { P2H(1, 14); P2H(1, 15); }
        if (j == 15) { X2(1); }
        if (j == 16) { P3(1, 0); }
        if (j == 17) { P3(1, 1); P3(1, 2); }
        SB();
      }
      vp[2][0] = PACKS(v0, 0); vp[2][1] = PACKS(v0, 1);
      vp[2][2] = PACKS(v1, 0); vp[2][3] = PACKS(v1, 1);
    }
    STAMPL(9);
    PRIO_PURE();
    {
      f32x16 oc[2];
      UNROLL for (int n = 0; n < 6; ++n) {
        const int q = n / 3, tt = n % 3;
        f32x16 o;
        UNROLL for (int g = 0; g < 4; ++g) {
          f32x4 r4 = mfma4(id0, id1, x1[q][2 * tt + (g >> 1)][2 * (g & 1)], x1[q][2 * tt + (g >> 1)][2 * (g & 1) + 1], z4);
          UNROLL for (int e = 0; e < 4; ++e) o[4 * g + e] = r4[e];
        }
        o = mfma32(vp[tt][0], pf[q][0], o);
        o = mfma32(vp[tt][1], pf[q][1], o);
        if (n == 0) { P3(1, 3); }
        if (n > 0) {
          const int qp = (n - 1) / 3, tp = (n - 1) % 3;
          x1[qp][2 * tp] = PACKS(oc[(n - 1) & 1], 0);
          x1[qp][2 * tp + 1] = PACKS(oc[(n - 1) & 1], 1);
        }
        o = mfma32(vp[tt][2], pf[q][2], o);
        o = mfma32(vp[tt][3], pf[q][3], o);
        oc[n & 1] = o;
        SB();
      }
      { u32x4 t0 = wb[2], t1 = wb[3], t2 = wb[0]; wb[0] = t0; wb[1] = t1; wb[2] = t2; }
      STAMPL(10);
      wait_vm0();
      UNROLL for (int ks = 0; ks < 6; ++ks) xf[0][ks] = lds_ld(smem, XA(ks));
      MEMB();
      UNROLL for (int i = 0; i < 3; ++i) lds_st(smem, sl16 + i * 1024, pre[i]);
      MEMB();
      UNROLL for (int ks = 0; ks < 6; ++ks) xf[1][ks] = lds_ld(smem, t1adj + XA(ks));
      MEMB();
      x1[1][4] = PACKS(oc[1], 0);
      x1[1][5] = PACKS(oc[1], 1);
    }
    STAMPL(11);
    { int wy = win / 28, wx = win - wy * 28; ob = (unsigned)w * RS_IN + (unsigned)((wy * 8) * 224 + wx * 8) * 192u; }
    if (!more) {
      UNROLL for (int a = 0; a < 2; ++a) {
        UNROLL for (int ks = 0; ks < 6; ++ks) lds_st(smem, XA(ks), x1[a][ks]);
        MEMB();
        UNROLL for (int i = 0; i < 6; ++i) x1[a][i] = lds_ld(smem, sl16 + i * 1024);
        MEMB();
      }
      UNROLL for (int a = 0; a < 2; ++a)
        UNROLL for (int i = 0; i < 6; ++i) __builtin_nontemporal_store(x1[a][i], (u32x4*)(out + (size_t)(ob + a * ROWB + PUNI(i, RS_OUT)) + POFF(i, l16, m1out)));
      STAMPL(12);
      break;
    }
    STAMPL(12);
    win = nwin;
    ++iter;
  }
  STAMP_OUT;
  asm volatile(".Lswin_pf_end:");
}

extern "C" void kernel_launch(void* const* d_in, const int* in_sizes, int n_in,
                              void* d_out, int out_size, void* d_ws, size_t ws_size,
                              hipStream_t stream) {
  swin_kernel<<<256, 512, 0, stream>>>((const char*)d_in[0], (const char*)d_in[1], (const char*)d_in[2],
                                       (const char*)d_in[3], (const char*)d_in[4], (char*)d_out, (unsigned long long*)d_ws);
}
```

```cpp
#define NOLOOPST 0
#define T2BASE 2
#define BPREP_PRIO 2
#define CODEPF
#define CODEPF_COND wave == 3
#include <hip/hip_runtime.h>
#include <hip/hip_bf16.h>
#include <stdint.h>

#if defined(__HIP_DEVICE_COMPILE__)
#define NOPK __attribute__((target("no-packed-fp32-ops")))
#else
#define NOPK
#endif
#define DI __device__ __forceinline__ NOPK

typedef float f32x16 __attribute__((ext_vector_type(16)));
typedef float f32x4 __attribute__((ext_vector_type(4)));
typedef unsigned u32x4 __attribute__((ext_vector_type(4)));
typedef unsigned u32x2 __attribute__((ext_vector_type(2)));
typedef __bf16 bf16x8 __attribute__((ext_vector_type(8)));
typedef __bf16 bf16x2 __attribute__((ext_vector_type(2)));
typedef short s16x4 __attribute__((ext_vector_type(4)));
#define LAS __attribute__((address_space(3)))

constexpr int WF_OFF = 0;
constexpr int SLOT_OFF = 73728;
constexpr int SLOT_B = 9216;
constexpr int R_OFF = SLOT_OFF + 8 * SLOT_B;
constexpr int RP_OFF = R_OFF + 2048;
constexpr int CNT_OFF = RP_OFF + 1024;
constexpr int DUM_OFF = CNT_OFF + 256;
constexpr int LDS_BYTES = DUM_OFF + 9216;

constexpr unsigned RS_IN = 224u * 224u * 192u;
constexpr unsigned ROWB = 224u * 192u;
constexpr unsigned RS_OUT = 2u * ROWB;

DI f32x16 mfma32(u32x4 a, u32x4 b, f32x16 c) {
  return __builtin_amdgcn_mfma_f32_32x32x16_bf16(__builtin_bit_cast(bf16x8, a), __builtin_bit_cast(bf16x8, b), c, 0, 0, 0);
}
DI f32x4 mfma4(unsigned a0, unsigned a1, unsigned b0, unsigned b1, f32x4 c) {
  u32x2 a = {a0, a1}, b = {b0, b1};
  return __builtin_amdgcn_mfma_f32_4x4x4bf16_1k(__builtin_bit_cast(s16x4, a), __builtin_bit_cast(s16x4, b), c, 0, 0, 0);
}
DI unsigned pk(float lo, float hi) {
  bf16x2 p = {(__bf16)lo, (__bf16)hi};
  return __builtin_bit_cast(unsigned, p);
}
#define PACKS(x, s) u32x4{pk((x)[8 * (s)], (x)[8 * (s) + 1]), pk((x)[8 * (s) + 2], (x)[8 * (s) + 3]), \
                          pk((x)[8 * (s) + 4], (x)[8 * (s) + 5]), pk((x)[8 * (s) + 6], (x)[8 * (s) + 7])}
DI float fbits(unsigned u) { return __builtin_bit_cast(float, u); }
DI unsigned ubits(float f) { return __builtin_bit_cast(unsigned, f); }
DI float merge_add(float a, float b) {
  auto r = __builtin_amdgcn_permlane32_swap(ubits(a), ubits(b), false, false);
  return fbits(r[0]) + fbits(r[1]);
}
DI float xhalf_add(float a) { return merge_add(a, a); }
DI float xhalf_max(float a) {
  auto r = __builtin_amdgcn_permlane32_swap(ubits(a), ubits(a), false, false);
  return __builtin_fmaxf(fbits(r[0]), fbits(r[1]));
}
template <int I> DI float quad_bcast(float v) {
  return fbits((unsigned)__builtin_amdgcn_mov_dpp((int)ubits(v), I * 0x55, 0xf, 0xf, true));
}
DI u32x4 lds_ld(const char* smem, unsigned off) { return *(const u32x4*)(smem + off); }
DI void lds_st(char* smem, unsigned off, u32x4 v) { *(u32x4*)(smem + off) = v; }

DI void dma16(unsigned lds_dst, const char* sbase, unsigned voff) {
  unsigned keep;
  asm volatile("s_mov_b32 %0, m0\n\ts_mov_b32 m0, %1\n\ts_nop 0\n\tglobal_load_lds_dwordx4 %2, %3\n\ts_mov_b32 m0, %0"
               : "=&s"(keep) : "s"(lds_dst), "v"(voff), "s"(sbase) : "memory");
}
DI void dma16nt(unsigned lds_dst, const char* sbase, unsigned voff) {
  unsigned keep;
  asm volatile("s_mov_b32 %0, m0\n\ts_mov_b32 m0, %1\n\ts_nop 0\n\tglobal_load_lds_dwordx4 %2, %3 nt\n\ts_mov_b32 m0, %0"
               : "=&s"(keep) : "s"(lds_dst), "v"(voff), "s"(sbase) : "memory");
}
DI void dma16ntc(unsigned lds_dst, const char* sbase, unsigned voff, unsigned on) {
  unsigned keep;
  asm volatile("s_cmp_eq_u32 %4, 0\n\ts_cbranch_scc1 1f\n\ts_mov_b32 %0, m0\n\ts_mov_b32 m0, %1\n\ts_nop 0\n\t"
               "global_load_lds_dwordx4 %2, %3 nt\n\ts_mov_b32 m0, %0\n1:"
               : "=&s"(keep) : "s"(lds_dst), "v"(voff), "s"(sbase), "s"(on) : "memory", "scc");
}
template <int K> DI unsigned xad(unsigned a, unsigned c) {
  unsigned r;
  asm volatile("v_xad_u32 %0, %1, %2, %3" : "=v"(r) : "v"(a), "i"(K), "s"(c));
  return r;
}
DI void dma1ntc(unsigned lds_dst, const char* sbase, unsigned v0, unsigned on) {
  asm volatile("s_cmp_eq_u32 %3, 0\n\ts_mov_b32 m0, %0\n\ts_cbranch_scc1 1f\n\tglobal_load_lds_dwordx4 %1, %2 nt\n1:"
               :: "s"(lds_dst), "v"(v0), "s"(sbase), "s"(on) : "memory", "scc");
}
DI void dma3ntc(unsigned lds_dst, const char* sbase, unsigned v0, unsigned v1, unsigned v2, unsigned on) {
  asm volatile("s_cmp_eq_u32 %5, 0\n\ts_cbranch_scc1 1f\n\t"
               "s_mov_b32 m0, %0\n\ts_nop 0\n\tglobal_load_lds_dwordx4 %1, %4 nt\n\t"
               "s_add_u32 m0, m0, 0x400\n\ts_nop 0\n\tglobal_load_lds_dwordx4 %2, %4 nt\n\t"
               "s_add_u32 m0, m0, 0x400\n\ts_nop 0\n\tglobal_load_lds_dwordx4 %3, %4 nt\n1:"
               :: "s"(lds_dst), "v"(v0), "v"(v1), "v"(v2), "s"(sbase), "s"(on) : "memory", "scc");
}
DI void wait_vm0() { asm volatile("s_waitcnt vmcnt(0)" ::: "memory"); }
DI void wait_lgkm0() { asm volatile("s_waitcnt lgkmcnt(0)" ::: "memory"); }
DI void wg_barrier() { asm volatile("s_waitcnt lgkmcnt(0)\n\ts_barrier" ::: "memory"); }

DI void grp_arrive(unsigned lds_addr) {
  unsigned a, o;
  asm volatile("v_mov_b32 %0, %2\n\tv_mov_b32 %1, 1\n\ts_waitcnt lgkmcnt(0)\n\ts_mov_b64 exec, 1\n\tds_add_u32 %0, %1\n\ts_mov_b64 exec, -1"
               : "=&v"(a), "=&v"(o) : "s"(lds_addr) : "memory");
}
DI void grp_wait(unsigned lds_addr, unsigned target) {
  unsigned a, v, sv;
  asm volatile("v_mov_b32 %0, %3\n"
               "1:\n\tds_read_b32 %1, %0\n\ts_waitcnt lgkmcnt(0)\n\tv_readfirstlane_b32 %2, %1\n\ts_nop 1\n\ts_cmp_ge_u32 %2, %4\n\t"
               "s_cbranch_scc1 2f\n\ts_sleep 1\n\ts_branch 1b\n2:"
               : "=&v"(a), "=&v"(v), "=&s"(sv) : "s"(lds_addr), "s"(target) : "memory", "scc");
}
DI void grp_wait_peeked(unsigned lds_addr, unsigned target, unsigned peeked) {
  unsigned a, v, sv;
  asm volatile("v_readfirstlane_b32 %2, %5\n\tv_mov_b32 %0, %3\n\ts_nop 0\n\ts_cmp_ge_u32 %2, %4\n\ts_cbranch_scc1 2f\n"
               "1:\n\tds_read_b32 %1, %0\n\ts_waitcnt lgkmcnt(0)\n\tv_readfirstlane_b32 %2, %1\n\ts_nop 1\n\ts_cmp_ge_u32 %2, %4\n\t"
               "s_cbranch_scc1 2f\n\ts_sleep 1\n\ts_branch 1b\n2:"
               : "=&v"(a), "=&v"(v), "=&s"(sv) : "s"(lds_addr), "s"(target), "v"(peeked) : "memory", "scc");
}
template <int BASE> DI void set_prio_rem(unsigned rem, unsigned partner) {
  unsigned sv;
  asm volatile("v_readfirstlane_b32 %0, %2\n\ts_nop 1\n\ts_cmp_gt_u32 %1, %0\n\ts_cbranch_scc1 1f\n\ts_setprio %3\n\ts_branch 2f\n1:\n\ts_setprio %4\n2:"
               : "=&s"(sv) : "s"(rem), "v"(partner), "i"(BASE), "i"(BASE + 1) : "scc");
}
template <int PA, int PB> DI void set_prio(int grp) {
  asm volatile("s_cmp_eq_u32 %0, 0\n\ts_cbranch_scc1 1f\n\ts_setprio %2\n\ts_branch 2f\n1:\n\ts_setprio %1\n2:"
               :: "s"(grp), "i"(PA), "i"(PB) : "scc");
}

#define STAMP0(i)
#define STAMPL(i)
#define STAMP_DECL
#define STAMP_OUT
DI int swap23(int n) { return (n & 0x13) | ((n & 4) << 1) | ((n & 8) >> 1); }

#define SB() __builtin_amdgcn_sched_barrier(0)
#define MEMB() asm volatile("" ::: "memory")
#define UNROLL _Pragma("unroll")
#define PMODE 6
#define PRIO_DENSE()
#define PRIO_PURE()

__global__ __launch_bounds__(512, 2) NOPK void swin_kernel(const char* __restrict__ x, const char* __restrict__ wqkv_t,
                                                           const char* __restrict__ wo_t, const char* __restrict__ wqkv_s,
                                                           const char* __restrict__ wo_s, char* __restrict__ out, unsigned long long* dbg) {
  __shared__ __attribute__((aligned(16))) char smem[LDS_BYTES];
  const unsigned lds0 = (unsigned)(size_t)smem;
  const int lane = threadIdx.x & 63;
  const int wave = __builtin_amdgcn_readfirstlane(threadIdx.x >> 6);
  const int grp = wave >> 2, w = wave & 3;
  const int col = lane & 31, h = lane >> 5, p = col >> 2, t = col & 3;
  const unsigned l16 = (unsigned)lane * 16u;
  const int pgrp = __builtin_amdgcn_readfirstlane(blockIdx.x < 16 ? grp ^ 1 : grp);

  STAMP_DECL
  STAMP0(0);
  {
    unsigned long long pc; unsigned len;
    asm volatile("s_getpc_b64 %0\n.Lswin_pf_start:\n\ts_mov_b32 %1, .Lswin_pf_end-.Lswin_pf_start" : "=s"(pc), "=s"(len));
    if (CODEPF_COND) {
      UNROLL for (int i = 0; i < 8; ++i) {
        unsigned o = (unsigned)lane * 64u + i * 4096u;
        o = o < len ? o : 0u;
        unsigned keep;
        asm volatile("s_mov_b32 %0, m0\n\ts_mov_b32 m0, %1\n\ts_nop 0\n\tglobal_load_lds_dword %2, %3\n\ts_mov_b32 m0, %0"
                     : "=&s"(keep) : "s"(lds0 + DUM_OFF + 1024), "v"(o), "s"(pc) : "memory");
      }
    }
  }
  const unsigned m1in = lane < 32 ? l16 + 1024u : ((l16 - 512u) ^ 16u) + RS_IN;
  const unsigned m1out = lane < 32 ? l16 + 1024u : ((l16 - 512u) ^ 16u) + RS_OUT;
#define POFF(i, lb, m1) ((i) == 0 ? (lb) : (i) == 1 ? (m1) : (i) == 2 ? ((lb) ^ 16u) : (i) == 3 ? ((lb) ^ 32u) : (i) == 4 ? ((m1) ^ 32u) : ((lb) ^ 48u))
#define PUNI(i, RS) ((i) == 0 ? 0u : (i) == 1 ? 0u : (i) == 2 ? (RS) + 512u : (i) == 3 ? 2u * (RS) : (i) == 4 ? 2u * (RS) : 3u * (RS) + 512u)
#define OPAQUE(v) asm volatile("" : "+v"(v))
#define PIN(v) asm volatile("" : "+v"(v))
#define PFULL(i, m1, RS) ((i) == 0 ? l16 : (i) == 1 ? (m1) : (i) == 2 ? xad<16>(l16, PUNI(2, RS)) : (i) == 3 ? xad<32>(l16, PUNI(3, RS)) : (i) == 4 ? xad<32>(m1, PUNI(4, RS)) : xad<48>(l16, PUNI(5, RS)))
  const __amdgpu_buffer_rsrc_t xrs = __builtin_amdgcn_make_buffer_rsrc((void*)x, 0, 38535168, 0x00020000);
  int win = blockIdx.x + 256 * grp;
  u32x4 xf[2][6];
  unsigned xb0;
  { int wy = win / 28, wx = win - wy * 28; xb0 = (unsigned)((wy * 8 + 2 * w) * 224 + wx * 8) * 192u; }
#define XLOAD(n) xf[(n) / 6][(n) % 6] = __builtin_amdgcn_raw_buffer_load_b128(xrs, POFF((n) % 6, l16, m1in), xb0 + ((n) / 6) * ROWB + PUNI((n) % 6, RS_IN), 2)

  if (threadIdx.x < 64) *(unsigned*)(smem + CNT_OFF + threadIdx.x * 4) = 0u;
#define CN(g, o) (lds0 + CNT_OFF + (g) * 64 + (o))
  const unsigned slot = SLOT_OFF + wave * SLOT_B;
  const unsigned cntA = CNT_OFF + grp * 64 + 32, cntB = cntA + 4;
  const float inv96 = 1.0f / 96.0f;

  const unsigned xa_e = slot + (unsigned)((t * 96 + 12 * p + (h ^ t)) * 16);
  const unsigned xa_o = slot + (unsigned)((t * 96 + 12 * p + ((2 + h) ^ t)) * 16);
  const unsigned t1adj = t < 2 ? 6144u : (unsigned)-3072;
  const unsigned sl16 = slot + l16;
#define XA(ks) ((((ks) & 1) ? xa_o : xa_e) + 64 * ((ks) >> 1))

  {
    constexpr unsigned QK = SLOT_OFF + 36864;
    const char* xb = x + xb0;
    unsigned qo[3], qf[3], vv[3];
    if (grp) {
      UNROLL for (int m = 0; m < 3; ++m) {
        int idx = m * 64 + lane, row = idx / 24, qq = idx - row * 24;
        int c = qq ^ (row >> 1);
        qo[m] = (unsigned)(row * 576 + c * 16);
        qf[m] = (unsigned)(row * 576 + (c ^ 4) * 16);
      }
      UNROLL for (int i = 0; i < 3; ++i)
        UNROLL for (int m = 0; m < 3; ++m)
          dma16(lds0 + QK + (w + 4 * i) * 3072 + m * 1024, wqkv_t + (w + 4 * i) * 8 * 576, (w & 1) ? qf[m] : qo[m]);
    }
    wg_barrier();
    if (!grp) {
      UNROLL for (int i = 0; i < 6; ++i) dma16nt(lds0 + slot + i * 1024, xb + PUNI(i, RS_IN), POFF(i, l16, m1in));
      UNROLL for (int i = 0; i < 3; ++i) dma16nt(lds0 + slot + 6144 + i * 1024, xb + ROWB + PUNI(i, RS_IN), POFF(i, l16, m1in));
      UNROLL for (int n = 9; n < 12; ++n) XLOAD(n);
      MEMB();
    } else {
      UNROLL for (int m = 0; m < 3; ++m) {
        int idx = m * 64 + lane, row2 = idx / 12, q2 = idx - row2 * 12;
        vv[m] = (unsigned)(row2 * 576 + 384 + (q2 ^ (row2 >> 2)) * 16);
      }
    }
#define VOSIXTH(base, wq_, wo_, i)                                                                               \
  {                                                                                                              \
    UNROLL for (int m = 0; m < 3; ++m) dma16(lds0 + (base) + (i) * 3072 + m * 1024, (wo_) + (i) * 3072 + m * 1024, l16); \
    UNROLL for (int m = 0; m < 3; ++m) dma16(lds0 + (base) + 18432 + (i) * 3072 + m * 1024, (wq_) + (i) * 16 * 576, vv[m]); \
  }
    const int pn = swap23(col);
    unsigned qb[4], vb[2];
    UNROLL for (int e = 0; e < 4; ++e) qb[e] = QK + (unsigned)(pn * 384 + (((2 * e) | h) ^ ((pn >> 1) & 7)) * 16);
    UNROLL for (int e = 0; e < 2; ++e) vb[e] = (unsigned)(18432 + pn * 192 + (((2 * e) | h) ^ ((pn >> 2) & 3)) * 16);
    const unsigned tb = (unsigned)((8 * h + ((lane & 15) >> 2)) * 192 + (16 * ((lane >> 4) & 1) + 8 * (lane & 1) + 4 * ((lane >> 1) & 1)) * 2 + w * 64);
    UNROLL for (int e = 0; e < 4; ++e) OPAQUE(qb[e]);
    if (grp) { wait_vm0(); grp_arrive(CN(0, 0)); }
    STAMP0(1);
#define QKF(rt, c2) lds_ld(smem, qb[(c2) & 3] + (rt) * 12288 + ((c2) >> 2) * 128)
#define QKR(ro, c2) lds_ld(smem, qb[(c2) & 3] + (ro) + ((c2) >> 2) * 128)
    if (wave == 7) {
      grp_wait(CN(0, 16), 6);
      for (int tr = 0; tr < 12; tr += 2) {
        UNROLL for (int m = 0; m < 3; ++m) dma16(lds0 + QK + tr * 3072 + m * 1024, wqkv_s + tr * 8 * 576, qo[m]);
        UNROLL for (int m = 0; m < 3; ++m) dma16(lds0 + QK + (tr + 1) * 3072 + m * 1024, wqkv_s + (tr + 1) * 8 * 576, qf[m]);
      }
      wait_vm0();
      grp_arrive(CN(1, 0));
      grp_wait(CN(1, 16), 6);
      for (int i = 0; i < 6; ++i) VOSIXTH(QK, wqkv_s, wo_s, i);
      wait_vm0();
      grp_arrive(CN(1, 4));
    }
    for (int g = 0; g <= grp && w < 3; ++g) {
      const unsigned RA = WF_OFF + g * 36864, RB = RA + 18432;
      const unsigned rp = RP_OFF + g * 512;
      const unsigned cn = lds0 + CNT_OFF + g * 64;
      grp_wait(cn + 0, g ? 1 : 4);
      if (grp) {
        __builtin_amdgcn_s_setprio(3);
        u32x4 af[3][6], bb[6];
        UNROLL for (int kk = 0; kk < 6; ++kk) { bb[kk] = QKR(w * 12288, kk); af[0][kk] = QKF(0, 6 + kk); }
        UNROLL for (int ct = 1; ct < 3; ++ct)
          UNROLL for (int kk = 0; kk < 6; ++kk) af[ct][kk] = QKF(ct, 6 + kk);
        SB();
        f32x16 d = {};
        UNROLL for (int ct = 0; ct < 3; ++ct)
          UNROLL for (int kk = 0; kk < 6; ++kk) d = mfma32(af[ct][kk], bb[kk], d);
        float r0 = (d[0] + d[1]) + (d[2] + d[3]), r1 = (d[4] + d[5]) + (d[6] + d[7]);
        float r2_ = (d[8] + d[9]) + (d[10] + d[11]), r3 = (d[12] + d[13]) + (d[14] + d[15]);
        float rs = xhalf_add((r0 + r1) + (r2_ + r3));
        *(float*)(smem + rp + (32 * w + pn) * 4) = rs * -inv96;
        grp_arrive(cn + 8);
        grp_arrive(cn + 16);
        if (g == 0) {
          VOSIXTH(WF_OFF + 36864, wqkv_t, wo_t, 2 * w);
          VOSIXTH(WF_OFF + 36864, wqkv_t, wo_t, 2 * w + 1);
          wait_vm0();
          grp_arrive(cn + 4);
        }
      }
      if (g == grp) {
        f32x16 DA[3];
        float a0 = 0.f, a1 = 0.f;
        {
          u32x4 bb[6], af[3][6];
          UNROLL for (int kk = 0; kk < 6; ++kk) { bb[kk] = QKR(w * 12288, 6 + kk); af[0][kk] = QKF(0, kk); }
          UNROLL for (int rt = 1; rt < 3; ++rt)
            UNROLL for (int kk = 0; kk < 6; ++kk) af[rt][kk] = QKF(rt, kk);
          SB();
          UNROLL for (int rt = 0; rt < 3; ++rt) {
            f32x16 d = {};
            UNROLL for (int kk = 0; kk < 6; ++kk) d = mfma32(af[rt][kk], bb[kk], d);
            DA[rt] = d;
            if (rt == 0) grp_arrive(cn + 16);
            if (rt > 0) { UNROLL for (int i = 0; i < 16; i += 2) { a0 += DA[rt - 1][i]; a1 += DA[rt - 1][i + 1]; } }
            SB();
          }
          UNROLL for (int i = 0; i < 16; i += 2) { a0 += DA[2][i]; a1 += DA[2][i + 1]; }
        }
        grp_wait(cn + 8, 3);
        f32x4 rm[12];
        UNROLL for (int n = 0; n < 12; ++n) rm[n] = *(const f32x4*)(smem + rp + (32 * (n >> 2) + 16 * ((n >> 1) & 1) + 8 * h + 4 * (n & 1)) * 4);
        UNROLL for (int n = 0; n < 12; ++n) {
          a0 += rm[n][0] + rm[n][2]; a1 += rm[n][1] + rm[n][3];
          UNROLL for (int i = 0; i < 4; ++i) DA[n >> 2][4 * (n & 3) + i] += rm[n][i];
        }
        const float fA = 0.10206207261596575f * 1.4426950408889634f;
        const float cadd = xhalf_add(a0 + a1) * (-fA * inv96);
        if (g) grp_wait(CN(0, 12), 3);
        UNROLL for (int rt = 0; rt < 3; ++rt) {
          UNROLL for (int i = 0; i < 16; ++i) DA[rt][i] = __builtin_fmaf(DA[rt][i], fA, cadd);
          lds_st(smem, RA + (w * 6 + 2 * rt) * 1024 + l16, PACKS(DA[rt], 0));
          lds_st(smem, RA + (w * 6 + 2 * rt + 1) * 1024 + l16, PACKS(DA[rt], 1));
        }
        grp_arrive(cn + 20);
      }
      if (grp) {
        if (g) { UNROLL for (int n = 0; n < 12; ++n) XLOAD(n); MEMB(); }
        const unsigned VO = g ? QK : WF_OFF + 36864;
        grp_wait(cn + 4, g ? 1 : 3);
#define VF(rt, kk) lds_ld(smem, VO + vb[(kk) & 1] + (rt) * 6144 + ((kk) >> 1) * 64)
        u32x4 bfr[6], av[2][6];
        UNROLL for (int kk = 0; kk < 6; ++kk) {
          s16x4 lo = __builtin_amdgcn_ds_read_tr16_b64_v4i16((LAS s16x4*)(smem + VO + tb + kk * 3072));
          s16x4 hi = __builtin_amdgcn_ds_read_tr16_b64_v4i16((LAS s16x4*)(smem + VO + tb + kk * 3072 + 768));
          u32x2 l2 = __builtin_bit_cast(u32x2, lo), h2 = __builtin_bit_cast(u32x2, hi);
          bfr[kk] = u32x4{l2[0], l2[1], h2[0], h2[1]};
          av[0][kk] = VF(0, kk);
        }
        UNROLL for (int kk = 0; kk < 6; ++kk) av[1][kk] = VF(1, kk);
        SB();
        f32x16 DB[3];
        float c0 = 0.f, c1 = 0.f;
        UNROLL for (int rt = 0; rt < 3; ++rt) {
          f32x16 d = {};
          UNROLL for (int kk = 0; kk < 6; ++kk) {
            d = mfma32(av[rt & 1][kk], bfr[kk], d);
            if (rt == 0) av[0][kk] = VF(2, kk);
          }
          DB[rt] = d;
          if (rt == 1) grp_arrive(cn + 12);
          if (rt > 0) { UNROLL for (int i = 0; i < 16; i += 2) { c0 += DB[rt - 1][i]; c1 += DB[rt - 1][i + 1]; } }
          SB();
        }
        UNROLL for (int i = 0; i < 16; i += 2) { c0 += DB[2][i]; c1 += DB[2][i + 1]; }
        const float cm = xhalf_add(c0 + c1) * inv96;
        UNROLL for (int rt = 0; rt < 3; ++rt) {
          UNROLL for (int i = 0; i < 16; ++i) DB[rt][i] -= cm;
          lds_st(smem, RB + (w * 6 + 2 * rt) * 1024 + l16, PACKS(DB[rt], 0));
          lds_st(smem, RB + (w * 6 + 2 * rt + 1) * 1024 + l16, PACKS(DB[rt], 1));
        }
        grp_arrive(cn + 24);
#undef VF
      }
    }
#undef QKF
#undef QKR
#undef VOSIXTH
    STAMP0(2);
    if (grp) {
      __builtin_amdgcn_s_setprio(0);
      if (w == 3) { UNROLL for (int n = 0; n < 12; ++n) XLOAD(n); MEMB(); }
      grp_wait(CN(1, 12), 3);
      UNROLL for (int i = 0; i < 6; ++i) lds_st(smem, sl16 + i * 1024, xf[0][i]);
      UNROLL for (int i = 0; i < 3; ++i) lds_st(smem, sl16 + 6144 + i * 1024, xf[1][i]);
      MEMB();
    }
  }
  wait_vm0();
  UNROLL for (int ks = 0; ks < 6; ++ks) xf[0][ks] = lds_ld(smem, XA(ks));
  MEMB();
  UNROLL for (int i = 0; i < 3; ++i) lds_st(smem, sl16 + i * 1024, xf[1][3 + i]);
  MEMB();
  UNROLL for (int ks = 0; ks < 6; ++ks) xf[1][ks] = lds_ld(smem, t1adj + XA(ks));
  MEMB();

  const bool own = (t == w);
  const unsigned exo = (unsigned)(h * 384 + p * 48 + ((t ^ w) - 1) * 16);
  const unsigned ex_w = own ? (unsigned)(DUM_OFF + h * 128 + ((3 * (p & 6) + 6 + (p & 1)) & 7) * 16)
                            : SLOT_OFF + (grp * 4 + t) * SLOT_B + exo;
  const unsigned ex_r = slot + exo;
  const unsigned long long notown = ~(0x1111111111111111ull << w);
  const unsigned rbase = R_OFF + wave * 256;
  unsigned wS = l16 + 36864u;
  asm volatile("" : "+v"(wS));
#define WLD(i) lds_ld(smem, ((i) < 36 ? l16 + (i) * 1024 : wS + ((i) - 36) * 1024))

  const unsigned ONES = 0x3F803F80u;
  const unsigned id0 = t == 0 ? 0x00003F80u : (t == 1 ? 0x3F800000u : 0u);
  const unsigned id1 = t == 2 ? 0x00003F80u : (t == 3 ? 0x3F800000u : 0u);
  unsigned msk[4];
  UNROLL for (int g = 0; g < 4; ++g) msk[g] = ((col >> 3) == g && h == (p & 1)) ? 0xffffffffu : 0u;
  const f32x4 z4 = {0.f, 0.f, 0.f, 0.f};
  const f32x16 zz = {};

  grp_wait(CN(0, 20), 3);

  u32x4 wb[4];
  wb[2] = WLD(0); wb[3] = WLD(1); wb[0] = WLD(2);
  u32x4 x1[2][6];
  UNROLL for (int a = 0; a < 2; ++a) UNROLL for (int i = 0; i < 6; ++i) x1[a][i] = wb[0];
  unsigned iter = 1;

#define OSTORE(n_) __builtin_amdgcn_raw_buffer_store_b128(x1[(n_) / 6][(n_) % 6], (n_) < 6 ? ors0 : ors1, PFULL((n_) % 6, m1out, RS_OUT), 0, 2)
#define LNFIN1(g0, g1, q0, q1)                                                                                    \
  {                                                                                                              \
    PIN(g0); PIN(g1);                                                                                            \
    float q0a = (t & 1) ? g0[1] : g0[0], q0b = (t & 1) ? g0[3] : g0[2];                                          \
    float q1a = (t & 1) ? g1[1] : g1[0], q1b = (t & 1) ? g1[3] : g1[2];                                          \
    q0 = (t & 2) ? q0b : q0a; q1 = (t & 2) ? q1b : q1a;                                                          \
  }
#define LNFIN2(sm0, sm1, q0, q1, r)                                                                              \
  {                                                                                                              \
    PIN(q0);                                                                                                     \
    float mean = merge_add(sm0[0], sm1[0]) * inv96;                                                              \
    float var = merge_add(q0, q1) * inv96 - mean * mean;                                                         \
    var = __builtin_fmaxf(var, 0.f);                                                                             \
    r = __builtin_amdgcn_rsqf(var + 1e-5f);                                                                      \
  }
#define T1_FILL(g)                                                                                               \
  {                                                                                                              \
    if ((g) < 3) { MEMB(); x1[0][2 * (g)] = lds_ld(smem, sl16 + (2 * (g)) * 1024); x1[0][2 * (g) + 1] = lds_ld(smem, sl16 + (2 * (g) + 1) * 1024); MEMB(); } \
    if ((g) >= 3 && (g) < 9) lds_st(smem, XA((g) - 3), x1[1][(g) >= 3 && (g) < 9 ? (g) - 3 : 0]);                \
    if ((g) >= 9 && (g) < 12) { const int k_ = (g) >= 9 && (g) < 12 ? 2 * ((g) - 9) : 0; MEMB(); x1[1][k_] = lds_ld(smem, sl16 + k_ * 1024); x1[1][k_ + 1] = lds_ld(smem, sl16 + (k_ + 1) * 1024); MEMB(); } \
    if ((g) == 10) LNFIN1(g0, g1, q0, q1);                                                                       \
    if ((g) == 12) LNFIN2(sm0, sm1, q0, q1, rr);                                                                 \
    if ((g) >= 13 && ((g) & 1)) OSTORE(((g) - 13) / 2);                                                          \
    if (((g) >= 14 && (g) <= 20 && !((g) & 1)) || ((g) >= 26 && (g) <= 32 && !((g) & 1))) {                      \
      const int tp_ = (g) >= 26 ? 1 : 0, k_ = (((g) - 14) % 12) / 2, a_ = (k_ >> 1) & 1, s_ = k_ & 1;           \
      PIN(acc[tp_][a_]);                                                                                         \
      uf[a_][2 * tp_ + s_] = PACKS(acc[tp_][a_], s_);                                                            \
    }                                                                                                            \
    if ((g) == 22) grp_arrive(lds0 + cntB);                                                                      \
    if ((g) == 24) { MEMB(); b1v = *(const unsigned*)(smem + CNT_OFF + 24); }                                    \
    if ((g) == 28) grp_wait_peeked(CN(0, 24), 3, b1v);                                                           \
  }
#define T1_STAGE()                                                                                               \
  {                                                                                                              \
    f32x4 sm0 = z4, sm1 = z4, g0 = z4, g1 = z4;                                                                  \
    unsigned b1v;                                                                                                \
    float q0, q1;                                                                                                \
    UNROLL for (int ks = 0; ks < 6; ++ks) {                                                                      \
      lds_st(smem, XA(ks), x1[0][ks]);                                                                           \
      UNROLL for (int c = 0; c < 2; ++c) {                                                                       \
        sm0 = mfma4(ONES, ONES, xf[0][ks][2 * c], xf[0][ks][2 * c + 1], sm0);                                    \
        sm1 = mfma4(ONES, ONES, xf[1][ks][2 * c], xf[1][ks][2 * c + 1], sm1);                                    \
        g0 = mfma4(xf[0][ks][2 * c], xf[0][ks][2 * c + 1], xf[0][ks][2 * c], xf[0][ks][2 * c + 1], g0);          \
        g1 = mfma4(xf[1][ks][2 * c], xf[1][ks][2 * c + 1], xf[1][ks][2 * c], xf[1][ks][2 * c + 1], g1);          \
      }                                                                                                          \
      SB();                                                                                                      \
    }                                                                                                            \
    UNROLL for (int i = 0; i < 18; ++i) {                                                                        \
      const int tt = i / 6, ks = i % 6;                                                                          \
      acc[tt & 1][0] = mfma32(wb[(i + 2) & 3], xf[0][ks], ks == 0 ? zz : acc[tt & 1][0]);                        \
      SB();                                                                                                      \
      wb[(i + 5) & 3] = WLD(i + 3);                                                                              \
      T1_FILL(2 * i);                                                                                            \
      SB();                                                                                                      \
      acc[tt & 1][1] = mfma32(wb[(i + 2) & 3], xf[1][ks], ks == 0 ? zz : acc[tt & 1][1]);                        \
      SB();                                                                                                      \
      T1_FILL(2 * i + 1);                                                                                        \
      SB();                                                                                                      \
    }                                                                                                            \
  }

  unsigned rem = (win + 512 < 784 ? 2u : 1u) * 90u + (unsigned)grp;
  unsigned prem = 0;
#define STAGE(BASE, WGT)                                                                                         \
  {                                                                                                              \
    set_prio_rem<BASE>(rem, prem);                                                                               \
    MEMB();                                                                                                      \
    *(unsigned*)(smem + CNT_OFF + 128 + wave * 4) = rem;                                                                \
    prem = *(const unsigned*)(smem + CNT_OFF + 128 + (wave ^ 4) * 4);                                                   \
    MEMB();                                                                                                      \
    rem -= (WGT);                                                                                                \
  }
  unsigned ob = 0;
  for (;;) {
    STAMPL(3);
    float rr;
    u32x4 uf[2][6];
    f32x16 acc[2][2];
    STAGE(0, 16);
    const __amdgpu_buffer_rsrc_t ors0 = __builtin_amdgcn_make_buffer_rsrc(out + (size_t)ob, 0, iter > 1 ? 0x41000 : 0, 0x00020000);
    const __amdgpu_buffer_rsrc_t ors1 = __builtin_amdgcn_make_buffer_rsrc(out + (size_t)(ob + ROWB), 0, iter > 1 ? 0x41000 : 0, 0x00020000);
    T1_STAGE();
    STAMPL(4);
    STAGE(T2BASE, 20);
    u32x4 pt[2][2];
    unsigned a2v;
    const int nwin = win + 512;
    const bool more = nwin < 784;
    const char* xb;
    unsigned pfon;
#define NEXTADDR()                                                                                               \
  {                                                                                                              \
    int nw = more ? nwin : win; int wy = nw / 28, wx = nw - wy * 28;                                             \
    xb = x + (unsigned)((wy * 8 + 2 * w) * 224 + wx * 8) * 192u;                                                 \
    pfon = (unsigned)__builtin_amdgcn_readfirstlane(more ? 0x7fffffff : 0);                                      \
    asm volatile("" : "+s"(xb), "+s"(pfon));                                                                     \
  }
    {
      f32x4 s0, s1, s0b, s1b;
      f32x16 vac[2][2];
      f32x16 oac[2];
      u32x4 vpk[2][2];
      float rs0, rs1, rs2, rs3, y0, y1, y2, y3, e0, e1, e2, e3;
      unsigned cbv;
      unsigned pa0, pa1, pb0, pb1;
      float sinv;
#define O_INIT(tt)                                                                                               \
  UNROLL for (int a = 0; a < 2; ++a) {                                                                           \
    f32x16 o;                                                                                                    \
    UNROLL for (int g = 0; g < 4; ++g) {                                                                         \
      f32x4 r4 = mfma4(id0, id1, xf[a][2 * (tt) + (g >> 1)][2 * (g & 1)], xf[a][2 * (tt) + (g >> 1)][2 * (g & 1) + 1], z4); \
      UNROLL for (int e = 0; e < 4; ++e) o[4 * g + e] = r4[e];                                                   \
    }                                                                                                            \
    oac[a] = o;                                                                                                  \
  }
#define O_STEP(tt)                                                                                               \
  O_INIT(tt);                                                                                                    \
  SB();                                                                                                          \
  oac[0] = mfma32(vpk[0][0], pt[0][0], oac[0]);                                                                  \
  oac[0] = mfma32(vpk[0][1], pt[0][1], oac[0]);                                                                  \
  oac[1] = mfma32(vpk[1][0], pt[1][0], oac[1]);                                                                  \
  oac[1] = mfma32(vpk[1][1], pt[1][1], oac[1]);
#define S2_STEP(tt, a, s)                                                                                        \
  PIN(oac[a]);                                                                                                   \
  x1[a][2 * (tt) + (s)] = PACKS(oac[a], s);                                                                      \
  lds_st(smem, ex_w + ((a) * 6 + 2 * (tt) + (s)) * 768, x1[a][2 * (tt) + (s)]);
#define VP_STEP(tt, k) { PIN(vac[(tt) & 1][(k) >> 1]); vpk[(k) >> 1][(k) & 1] = PACKS(vac[(tt) & 1][(k) >> 1], (k) & 1); }
#define T2_FILL(g)                                                                                               \
  {                                                                                                              \
    if ((g) >= 1 && (g) <= 4) { const int k_ = (g) >= 1 && (g) <= 4 ? (g) - 1 : 0; PIN(acc[0][k_ >> 1]); uf[k_ >> 1][4 + (k_ & 1)] = PACKS(acc[0][k_ >> 1], k_ & 1); } \
    if ((g) == 5) {                                                                                              \
      s0 = z4; s1 = z4; s0b = z4; s1b = z4;                                                                      \
      UNROLL for (int k2 = 0; k2 < 6; ++k2) {                                                                    \
        s0 = mfma4(xf[0][k2][0], xf[0][k2][1], uf[0][k2][0], uf[0][k2][1], s0);                                  \
        s1 = mfma4(xf[1][k2][0], xf[1][k2][1], uf[1][k2][0], uf[1][k2][1], s1);                                  \
        s0b = mfma4(xf[0][k2][2], xf[0][k2][3], uf[0][k2][2], uf[0][k2][3], s0b);                                \
        s1b = mfma4(xf[1][k2][2], xf[1][k2][3], uf[1][k2][2], uf[1][k2][3], s1b);                                \
      }                                                                                                          \
      rs0 = quad_bcast<0>(rr); rs1 = quad_bcast<1>(rr); rs2 = quad_bcast<2>(rr); rs3 = quad_bcast<3>(rr);        \
    }                                                                                                            \
    if ((g) == 6) { PIN(s0); PIN(s1); UNROLL for (int e = 0; e < 4; ++e) { s0[e] += s0b[e]; } s1[0] += s1b[0]; s1[1] += s1b[1]; } \
    if ((g) == 7) { PIN(s1); s1[2] += s1b[2]; s1[3] += s1b[3]; y0 = merge_add(s0[0], s1[0]) * rs0; }             \
    if ((g) == 8) { PIN(s0); y1 = merge_add(s0[1], s1[1]) * rs1; y2 = merge_add(s0[2], s1[2]); }                 \
    if ((g) == 9) { PIN(s0); y2 *= rs2; y3 = merge_add(s0[3], s1[3]) * rs3; }                                    \
    if ((g) == 10) {                                                                                             \
      PIN(y3);                                                                                                   \
      float mx = __builtin_fmaxf(__builtin_fmaxf(y0, y1), __builtin_fmaxf(y2, y3));                              \
      float cc = -rr * mx;                                                                                       \
      y0 = __builtin_fmaf(y0, rr, cc); y1 = __builtin_fmaf(y1, rr, cc);                                          \
      y2 = __builtin_fmaf(y2, rr, cc); y3 = __builtin_fmaf(y3, rr, cc);                                          \
    }                                                                                                            \
    if ((g) == 11) { PIN(y0); e0 = __builtin_amdgcn_exp2f(y0); e1 = __builtin_amdgcn_exp2f(y1); e2 = __builtin_amdgcn_exp2f(y2); } \
    if ((g) == 12) { PIN(y3); e3 = __builtin_amdgcn_exp2f(y3); sinv = __builtin_amdgcn_rcpf((e0 + e1) + (e2 + e3)); } \
    if ((g) == 13) { PIN(e0); e0 *= rs0; e1 *= rs1; e2 *= rs2; e3 *= rs3; e0 *= sinv; e1 *= sinv; }              \
    if ((g) == 14) {                                                                                             \
      PIN(e2);                                                                                                   \
      unsigned p01 = pk(e0, e1), p23 = pk(e2 * sinv, e3 * sinv);                                                 \
      auto a_ = __builtin_amdgcn_permlane32_swap(p01, p01, false, false);                                        \
      auto b_ = __builtin_amdgcn_permlane32_swap(p23, p23, false, false);                                        \
      pa0 = a_[0]; pa1 = a_[1]; pb0 = b_[0]; pb1 = b_[1];                                                        \
    }                                                                                                            \
    if ((g) == 15) { PIN(pa0); pt[0][0] = u32x4{pa0 & msk[0], pb0 & msk[0], pa0 & msk[1], pb0 & msk[1]}; { MEMB(); cbv = *(const unsigned*)(smem + cntB); } } \
    if ((g) == 16) { PIN(pa0); pt[0][1] = u32x4{pa0 & msk[2], pb0 & msk[2], pa0 & msk[3], pb0 & msk[3]}; }       \
    if ((g) == 17) { PIN(pa1); pt[1][0] = u32x4{pa1 & msk[0], pb1 & msk[0], pa1 & msk[1], pb1 & msk[1]}; }       \
    if ((g) == 18) { PIN(pa1); pt[1][1] = u32x4{pa1 & msk[2], pb1 & msk[2], pa1 & msk[3], pb1 & msk[3]}; }       \
    if ((g) >= 19 && (g) <= 22) VP_STEP(0, (g) >= 19 && (g) <= 22 ? (g) - 19 : 0);                               \
    if ((g) == 23) { grp_wait_peeked(lds0 + cntB, 4 * iter, cbv); O_STEP(0); }                                   \
    if ((g) >= 25 && (g) <= 28) { const int k_ = (g) >= 25 && (g) <= 28 ? (g) - 25 : 0; S2_STEP(0, k_ >> 1, k_ & 1); } \
    if ((g) >= 29 && (g) <= 32) VP_STEP(1, (g) >= 29 && (g) <= 32 ? (g) - 29 : 0);                               \
    if ((g) == 30) { MEMB(); a2v = *(const unsigned*)(smem + CNT_OFF + 64 + 20); }                               \
    if ((g) == 33) { O_STEP(1); }                                                                                \
    if ((g) == 35) { S2_STEP(1, 0, 0); S2_STEP(1, 0, 1); }                                                       \
  }
      UNROLL for (int j = 0; j < 18; ++j) {
        const int tt = j / 6, ks = j % 6;
        vac[tt & 1][0] = mfma32(xf[0][ks], wb[(j + 20) & 3], ks == 0 ? zz : vac[tt & 1][0]);
        SB();
        if (j < 15) wb[(j + 23) & 3] = WLD(j + 18 + 3);
        T2_FILL(2 * j);
        SB();
        vac[tt & 1][1] = mfma32(xf[1][ks], wb[(j + 20) & 3], ks == 0 ? zz : vac[tt & 1][1]);
        SB();
        T2_FILL(2 * j + 1);
        SB();
      }
      S2_STEP(1, 1, 0); S2_STEP(1, 1, 1);
      SB();
      O_INIT(2);
      VP_STEP(2, 0); VP_STEP(2, 1);
      SB();
      oac[0] = mfma32(vpk[0][0], pt[0][0], oac[0]);
      oac[0] = mfma32(vpk[0][1], pt[0][1], oac[0]);
      SB();
      VP_STEP(2, 2); VP_STEP(2, 3);
      SB();
      oac[1] = mfma32(vpk[1][0], pt[1][0], oac[1]);
      oac[1] = mfma32(vpk[1][1], pt[1][1], oac[1]);
      SB();
      NEXTADDR();
      S2_STEP(2, 0, 0); S2_STEP(2, 0, 1);
      SB();
      S2_STEP(2, 1, 0); S2_STEP(2, 1, 1);
#undef O_INIT
#undef VP_STEP
#undef T2_FILL
#undef O_STEP
#undef S2_STEP
    }
    STAMPL(5);
    grp_arrive(lds0 + cntA);
    grp_wait_peeked(CN(1, 20), 3, a2v);
    wb[0] = WLD(36); wb[1] = WLD(37); wb[2] = WLD(38);
    grp_wait(lds0 + cntA, 4 * iter);
    asm volatile("s_mov_b64 exec, %13\n\t"
                 "ds_read_b128 %0, %12\n\tds_read_b128 %1, %12 offset:768\n\tds_read_b128 %2, %12 offset:1536\n\t"
                 "ds_read_b128 %3, %12 offset:2304\n\tds_read_b128 %4, %12 offset:3072\n\tds_read_b128 %5, %12 offset:3840\n\t"
                 "ds_read_b128 %6, %12 offset:4608\n\tds_read_b128 %7, %12 offset:5376\n\tds_read_b128 %8, %12 offset:6144\n\t"
                 "ds_read_b128 %9, %12 offset:6912\n\tds_read_b128 %10, %12 offset:7680\n\tds_read_b128 %11, %12 offset:8448\n\t"
                 "s_mov_b64 exec, -1\n\ts_waitcnt lgkmcnt(0)"
                 : "+v"(x1[0][0]), "+v"(x1[0][1]), "+v"(x1[0][2]), "+v"(x1[0][3]), "+v"(x1[0][4]), "+v"(x1[0][5]),
                   "+v"(x1[1][0]), "+v"(x1[1][1]), "+v"(x1[1][2]), "+v"(x1[1][3]), "+v"(x1[1][4]), "+v"(x1[1][5])
                 : "v"(lds0 + ex_r), "s"(notown) : "memory");
    float rq[2];
    const unsigned rka = rbase + h * 16;
#define RKLD(G) (*(const f32x4*)(smem + rka + (G) * 32))
    f32x4 rkb[3];
    STAMPL(6);
    STAGE(0, 14);
    const __amdgpu_buffer_rsrc_t nrs = __builtin_amdgcn_make_buffer_rsrc((void*)(xb + ROWB), 0, pfon, 0x00020000);
    u32x4 pre[3];
    SB();
#define SW(k) ((k) < 18 ? 36 + (k) : ((k) < 54 ? 18 + (k) : (k) - 54))
    u32x4 pf[2][4];
    u32x4 vp[3][4];
    f32x16 sa[2][2];
    u32x4 uq[6];
    f32x16 ua[2];
    float mxv, ccv, l0, l1, invv;
    unsigned b2v;
#define U_STEP(q, i)                                                                                             \
  {                                                                                                              \
    const int tt_ = (i) / 6, ks_ = (i) % 6, k_ = 18 * (q) + (i);                                                 \
    ua[tt_ & 1] = mfma32(wb[k_ & 3], x1[q][ks_], ks_ == 0 ? zz : ua[tt_ & 1]);                                   \
    SB();                                                                                                        \
    wb[(k_ + 3) & 3] = WLD(SW(k_ + 3));                                                                          \
    if (tt_ > 0 && (ks_ == 2 || ks_ == 3)) { PIN(ua[(tt_ + 1) & 1]); uq[2 * (tt_ - 1) + (ks_ - 2)] = PACKS(ua[(tt_ + 1) & 1], ks_ - 2); } \
  }
#define S_STEP(q, ks_)                                                                                           \
  {                                                                                                              \
    sa[q][0] = mfma32(x1[0][ks_], uq[ks_], (ks_) == 0 ? zz : sa[q][0]);                                          \
    SB();                                                                                                        \
    if ((ks_) == 1) { PIN(ua[0]); uq[4] = PACKS(ua[0], 0); }                                                     \
    if ((ks_) == 2) { PIN(ua[0]); uq[5] = PACKS(ua[0], 1); }                                                     \
    SFILL;                                                                                                       \
    SB();                                                                                                        \
    sa[q][1] = mfma32(x1[1][ks_], uq[ks_], (ks_) == 0 ? zz : sa[q][1]);                                          \
    SB();                                                                                                        \
  }
#define SFILL
#define P1(q, G)                                                                                                 \
  {                                                                                                              \
    const int n_ = 16 * (q) + (G);                                                                               \
    MEMB(); rkb[(n_ + 2) % 3] = RKLD(((G) + 2) & 7);                                                             \
    UNROLL for (int e = 0; e < 4; ++e) sa[q][(G) >> 2][4 * ((G) & 3) + e] *= rkb[n_ % 3][e];                     \
    mxv = (G) == 0 ? sa[q][0][0] : __builtin_fmaxf(mxv, sa[q][(G) >> 2][4 * ((G) & 3)]);                         \
    mxv = __builtin_fmaxf(mxv, sa[q][(G) >> 2][4 * ((G) & 3) + 1]);                                              \
    mxv = __builtin_fmaxf(__builtin_fmaxf(mxv, sa[q][(G) >> 2][4 * ((G) & 3) + 2]), sa[q][(G) >> 2][4 * ((G) & 3) + 3]); \
  }
#define X1(q) { mxv = xhalf_max(mxv); ccv = -rq[q] * mxv; l0 = 0.f; l1 = 0.f; }
#define P2H(q, H)                                                                                                \
  {                                                                                                              \
    const int b_ = (H) >> 3, i_ = 2 * ((H) & 7), G_ = (H) >> 1, n_ = 16 * (q) + 8 + G_;                          \
    if (((H) & 1) == 0 && n_ < 30) { MEMB(); rkb[(n_ + 2) % 3] = RKLD((G_ + 2) & 7); }                           \
    const float ea = __builtin_amdgcn_exp2f(__builtin_fmaf(sa[q][b_][i_], rq[q], ccv));                          \
    const float eb = __builtin_amdgcn_exp2f(__builtin_fmaf(sa[q][b_][i_ + 1], rq[q], ccv));                      \
    l0 += ea; l1 += eb;                                                                                          \
    sa[q][b_][i_] = ea * rkb[n_ % 3][2 * ((H) & 1)]; sa[q][b_][i_ + 1] = eb * rkb[n_ % 3][2 * ((H) & 1) + 1];    \
  }
#define X2(q) { invv = __builtin_amdgcn_rcpf(xhalf_add(l0 + l1)); }
#define P3(q, F)                                                                                                 \
  {                                                                                                              \
    UNROLL for (int e = 0; e < 8; ++e) sa[q][(F) >> 1][8 * ((F) & 1) + e] *= invv;                               \
    pf[q][F] = PACKS(sa[q][(F) >> 1], (F) & 1);                                                                  \
  }
    {
      f32x4 sm0 = z4, sm1 = z4, g0 = z4, g1 = z4;
      UNROLL for (int ks = 0; ks < 6; ++ks) {
        UNROLL for (int c = 0; c < 2; ++c) {
          sm0 = mfma4(ONES, ONES, x1[0][ks][2 * c], x1[0][ks][2 * c + 1], sm0);
          sm1 = mfma4(ONES, ONES, x1[1][ks][2 * c], x1[1][ks][2 * c + 1], sm1);
          g0 = mfma4(x1[0][ks][2 * c], x1[0][ks][2 * c + 1], x1[0][ks][2 * c], x1[0][ks][2 * c + 1], g0);
          g1 = mfma4(x1[1][ks][2 * c], x1[1][ks][2 * c + 1], x1[1][ks][2 * c], x1[1][ks][2 * c + 1], g1);
        }
        SB();
      }
      float q0, q1;
      UNROLL for (int i = 0; i < 18; ++i) {
        U_STEP(0, i);
        if (i == 1) LNFIN1(g0, g1, q0, q1);
        if (i == 2) {
          float r2;
          LNFIN2(sm0, sm1, q0, q1, r2);
          *(float*)(smem + rbase + lane * 4) = r2;
          auto rb = __builtin_amdgcn_permlane32_swap(ubits(r2), ubits(r2), false, false);
          rq[0] = fbits(rb[0]); rq[1] = fbits(rb[1]);
          MEMB();
        }
        {
          const int n = i < 8 ? i - 3 : (i < 10 ? -1 : (i < 14 ? i - 5 : -1));
          if (n >= 0 && n < 6) dma1ntc(lds0 + slot + n * 1024, xb, PFULL(n >= 0 && n < 6 ? n : 0, m1in, RS_IN), pfon);
          if (n >= 6) dma1ntc(lds0 + slot + n * 1024, xb + ROWB, PFULL(n >= 6 ? n - 6 : 0, m1in, RS_IN), pfon);
        }
        if (i >= 16) pre[i - 16] = __builtin_amdgcn_raw_buffer_load_b128(nrs, PFULL(i >= 16 ? i - 13 : 0, m1in, RS_IN), 0, 2);
        SB();
      }
    }
    UNROLL for (int ks = 0; ks < 6; ++ks) {
      S_STEP(0, ks);
      if (ks == 3) pre[2] = __builtin_amdgcn_raw_buffer_load_b128(nrs, PFULL(5, m1in, RS_IN), 0, 2);
      if (ks == 4) { rkb[0] = RKLD(0); rkb[1] = RKLD(1); }
      SB();
    }
    STAMPL(7);
    PRIO_DENSE();
    STAGE(2, 30);
    UNROLL for (int i = 0; i < 18; ++i) {
      U_STEP(1, i);
      if (i >= 1 && i <= 4) { P1(0, 2 * (i - 1)); P1(0, 2 * (i - 1) + 1); }
      if (i == 5) X1(0);
      if (i == 10) { MEMB(); b2v = *(const unsigned*)(smem + CNT_OFF + 64 + 24); }
      if (i == 14) grp_wait_peeked(CN(1, 24), 3, b2v);
      if (i >= 6) P2H(0, i - 6);
      SB();
    }
#undef SFILL
#define SFILL { if (ks == 0) P2H(0, 12); if (ks == 1) P2H(0, 14); if (ks == 3) P3(0, 0); if (ks == 4) P3(0, 2); }
    UNROLL for (int ks = 0; ks < 6; ++ks) {
      S_STEP(1, ks);
      if (ks == 0) P2H(0, 13);
      if (ks == 1) P2H(0, 15);
      if (ks == 2) X2(0);
      if (ks == 3) P3(0, 1);
      if (ks == 4) P3(0, 3);
      SB();
    }
    STAMPL(8);
    {
      f32x16 v0, v1;
#define R2_FILL(g)                                                                                               \
  {                                                                                                              \
    if ((g) >= 1 && (g) <= 8) P1(1, (g) >= 1 && (g) <= 8 ? (g) - 1 : 0);                                         \
    if ((g) == 9) X1(1);                                                                                         \
    { const int H_ = (g) == 10 ? 0 : ((g) >= 14 && (g) <= 22 ? (g) - 13 : ((g) >= 26 && (g) <= 31 ? (g) - 16 : -1)); \
      if (H_ >= 0) P2H(1, H_ >= 0 ? H_ : 0); }                                                                   \
    if ((g) == 32) X2(1);                                                                                        \
    if ((g) == 33) P3(1, 0);                                                                                     \
    if ((g) == 34) P3(1, 1);                                                                                     \
    if ((g) == 11 || (g) == 23 || (g) == 35) { PIN(v0); vp[(g) / 12][0] = PACKS(v0, 0); vp[(g) / 12][1] = PACKS(v0, 1); } \
    if ((g) == 12 || (g) == 24) { PIN(v1); vp[(g) / 12 - 1][2] = PACKS(v1, 0); vp[(g) / 12 - 1][3] = PACKS(v1, 1); } \
  }
      UNROLL for (int j = 0; j < 18; ++j) {
        const int ks = j % 6, k = 36 + j;
        v0 = mfma32(x1[0][ks], wb[k & 3], ks == 0 ? zz : v0);
        SB();
        wb[(k + 3) & 3] = WLD(SW(k + 3));
        R2_FILL(2 * j);
        SB();
        v1 = mfma32(x1[1][ks], wb[k & 3], ks == 0 ? zz : v1);
        SB();
        R2_FILL(2 * j + 1);
        SB();
      }
#undef R2_FILL
      STAMPL(9);
      PRIO_PURE();
      STAGE(0, 10);
      f32x16 oc[2];
      UNROLL for (int n = 0; n < 6; ++n) {
        const int q = n / 3, tt = n % 3;
        f32x16 o;
        UNROLL for (int g = 0; g < 4; ++g) {
          f32x4 r4 = mfma4(id0, id1, x1[q][2 * tt + (g >> 1)][2 * (g & 1)], x1[q][2 * tt + (g >> 1)][2 * (g & 1) + 1], z4);
          UNROLL for (int e = 0; e < 4; ++e) o[4 * g + e] = r4[e];
        }
        SB();
        if (n == 0) { PIN(v1); vp[2][2] = PACKS(v1, 0); }
        o = mfma32(vp[tt][0], pf[q][0], o);
        SB();
        if (n == 0) { PIN(v1); vp[2][3] = PACKS(v1, 1); }
        if (n == 1) P3(1, 2);
        if (n == 2) P3(1, 3);
        SB();
        o = mfma32(vp[tt][1], pf[q][1], o);
        SB();
        if (n > 0) { PIN(oc[(n + 1) & 1]); x1[(n - 1) / 3][2 * ((n - 1) % 3)] = PACKS(oc[(n + 1) & 1], 0); }
        SB();
        o = mfma32(vp[tt][2], pf[q][2], o);
        SB();
        if (n > 0) { PIN(oc[(n + 1) & 1]); x1[(n - 1) / 3][2 * ((n - 1) % 3) + 1] = PACKS(oc[(n + 1) & 1], 1); }
        SB();
        o = mfma32(vp[tt][3], pf[q][3], o);
        oc[n & 1] = o;
        SB();
      }
      STAMPL(10);
      wait_vm0();
      UNROLL for (int ks = 0; ks < 6; ++ks) xf[0][ks] = lds_ld(smem, XA(ks));
      MEMB();
      UNROLL for (int i = 0; i < 3; ++i) lds_st(smem, sl16 + i * 1024, pre[i]);
      MEMB();
      UNROLL for (int ks = 0; ks < 6; ++ks) xf[1][ks] = lds_ld(smem, t1adj + XA(ks));
      MEMB();
      x1[1][4] = PACKS(oc[1], 0);
      x1[1][5] = PACKS(oc[1], 1);
    }
    STAMPL(11);
    { int wy = win / 28, wx = win - wy * 28; ob = (unsigned)w * RS_IN + (unsigned)((wy * 8) * 224 + wx * 8) * 192u; }
    if (!more) {
      UNROLL for (int a = 0; a < 2; ++a) {
        UNROLL for (int ks = 0; ks < 6; ++ks) lds_st(smem, XA(ks), x1[a][ks]);
        MEMB();
        UNROLL for (int i = 0; i < 6; ++i) x1[a][i] = lds_ld(smem, sl16 + i * 1024);
        MEMB();
      }
      const __amdgpu_buffer_rsrc_t ors0 = __builtin_amdgcn_make_buffer_rsrc(out + (size_t)ob, 0, 0x41000, 0x00020000);
      const __amdgpu_buffer_rsrc_t ors1 = __builtin_amdgcn_make_buffer_rsrc(out + (size_t)(ob + ROWB), 0, 0x41000, 0x00020000);
      UNROLL for (int n = 0; n < 12; ++n) OSTORE(n);
      STAMPL(12);
      break;
    }
    STAMPL(12);
    win = nwin;
    ++iter;
  }
  *(unsigned*)(smem + CNT_OFF + 128 + wave * 4) = 0u;
  STAMP_OUT;
  asm volatile(".Lswin_pf_end:");
}

extern "C" void kernel_launch(void* const* d_in, const int* in_sizes, int n_in,
                              void* d_out, int out_size, void* d_ws, size_t ws_size,
                              hipStream_t stream) {
  swin_kernel<<<256, 512, 0, stream>>>((const char*)d_in[0], (const char*)d_in[1], (const char*)d_in[2],
                                       (const char*)d_in[3], (const char*)d_in[4], (char*)d_out, (unsigned long long*)d_ws);
}
```

```cpp
#define PB_PURE 0
#define PB_DENSE 2
#define FPK_GAP 12
#define JB_PRIO 1
#define XB_BEFORE 1
#define NBLK 196
#define NOLOOPST 0
#define T2BASE 2
#define BPREP_PRIO 2
#define CODEPF
#define CODEPF_COND wave == 3
#include <hip/hip_runtime.h>
#include <hip/hip_bf16.h>
#include <stdint.h>

#if defined(__HIP_DEVICE_COMPILE__)
#define NOPK __attribute__((target("no-packed-fp32-ops")))
#else
#define NOPK
#endif
#define DI __device__ __forceinline__ NOPK

typedef float f32x16 __attribute__((ext_vector_type(16)));
typedef float f32x4 __attribute__((ext_vector_type(4)));
typedef unsigned u32x4 __attribute__((ext_vector_type(4)));
typedef unsigned u32x2 __attribute__((ext_vector_type(2)));
typedef __bf16 bf16x8 __attribute__((ext_vector_type(8)));
typedef __bf16 bf16x2 __attribute__((ext_vector_type(2)));
typedef short s16x4 __attribute__((ext_vector_type(4)));
#define LAS __attribute__((address_space(3)))

constexpr int WF_OFF = 0;
constexpr int SLOT_OFF = 73728;
constexpr int SLOT_B = 9216;
constexpr int R_OFF = SLOT_OFF + 8 * SLOT_B;
constexpr int RP_OFF = R_OFF + 2048;
constexpr int CNT_OFF = RP_OFF + 1024;
constexpr int DUM_OFF = CNT_OFF + 256;
constexpr int LDS_BYTES = DUM_OFF + 9216;

constexpr unsigned RS_IN = 224u * 224u * 192u;
constexpr unsigned ROWB = 224u * 192u;
constexpr unsigned RS_OUT = 2u * ROWB;

DI f32x16 mfma32(u32x4 a, u32x4 b, f32x16 c) {
  return __builtin_amdgcn_mfma_f32_32x32x16_bf16(__builtin_bit_cast(bf16x8, a), __builtin_bit_cast(bf16x8, b), c, 0, 0, 0);
}
DI f32x4 mfma4(unsigned a0, unsigned a1, unsigned b0, unsigned b1, f32x4 c) {
  u32x2 a = {a0, a1}, b = {b0, b1};
  return __builtin_amdgcn_mfma_f32_4x4x4bf16_1k(__builtin_bit_cast(s16x4, a), __builtin_bit_cast(s16x4, b), c, 0, 0, 0);
}
DI unsigned pk(float lo, float hi) {
  bf16x2 p = {(__bf16)lo, (__bf16)hi};
  return __builtin_bit_cast(unsigned, p);
}
#define PACKS(x, s) u32x4{pk((x)[8 * (s)], (x)[8 * (s) + 1]), pk((x)[8 * (s) + 2], (x)[8 * (s) + 3]), \
                          pk((x)[8 * (s) + 4], (x)[8 * (s) + 5]), pk((x)[8 * (s) + 6], (x)[8 * (s) + 7])}
DI float fbits(unsigned u) { return __builtin_bit_cast(float, u); }
DI unsigned ubits(float f) { return __builtin_bit_cast(unsigned, f); }
DI float merge_add(float a, float b) {
  auto r = __builtin_amdgcn_permlane32_swap(ubits(a), ubits(b), false, false);
  return fbits(r[0]) + fbits(r[1]);
}
DI float xhalf_add(float a) { return merge_add(a, a); }
DI float xhalf_max(float a) {
  auto r = __builtin_amdgcn_permlane32_swap(ubits(a), ubits(a), false, false);
  return __builtin_fmaxf(fbits(r[0]), fbits(r[1]));
}
template <int I> DI float quad_bcast(float v) {
  return fbits((unsigned)__builtin_amdgcn_mov_dpp((int)ubits(v), I * 0x55, 0xf, 0xf, true));
}
DI u32x4 lds_ld(const char* smem, unsigned off) { return *(const u32x4*)(smem + off); }
DI void lds_st(char* smem, unsigned off, u32x4 v) { *(u32x4*)(smem + off) = v; }

DI void dma16(unsigned lds_dst, const char* sbase, unsigned voff) {
  unsigned keep;
  asm volatile("s_mov_b32 %0, m0\n\ts_mov_b32 m0, %1\n\ts_nop 2\n\tglobal_load_lds_dwordx4 %2, %3\n\ts_mov_b32 m0, %0"
               : "=&s"(keep) : "s"(lds_dst), "v"(voff), "s"(sbase) : "memory");
}
DI void dma16nt(unsigned lds_dst, const char* sbase, unsigned voff) {
  unsigned keep;
  asm volatile("s_mov_b32 %0, m0\n\ts_mov_b32 m0, %1\n\ts_nop 2\n\tglobal_load_lds_dwordx4 %2, %3 nt\n\ts_mov_b32 m0, %0"
               : "=&s"(keep) : "s"(lds_dst), "v"(voff), "s"(sbase) : "memory");
}
DI void dma16ntc(unsigned lds_dst, const char* sbase, unsigned voff, unsigned on) {
  unsigned keep;
  asm volatile("s_cmp_eq_u32 %4, 0\n\ts_cbranch_scc1 1f\n\ts_mov_b32 %0, m0\n\ts_mov_b32 m0, %1\n\ts_nop 0\n\t"
               "global_load_lds_dwordx4 %2, %3 nt\n\ts_mov_b32 m0, %0\n1:"
               : "=&s"(keep) : "s"(lds_dst), "v"(voff), "s"(sbase), "s"(on) : "memory", "scc");
}
template <int K> DI unsigned xad(unsigned a, unsigned c) {
  unsigned r;
  asm volatile("v_xad_u32 %0, %1, %2, %3" : "=v"(r) : "v"(a), "i"(K), "s"(c));
  return r;
}
DI void dma1ntc(unsigned lds_dst, const char* sbase, unsigned v0, unsigned on) {
  asm volatile("s_cmp_eq_u32 %3, 0\n\ts_mov_b32 m0, %0\n\ts_cbranch_scc1 1f\n\ts_nop 1\n\tglobal_load_lds_dwordx4 %1, %2 nt\n1:"
               :: "s"(lds_dst), "v"(v0), "s"(sbase), "s"(on) : "memory", "scc");
}
DI void dma3ntc(unsigned lds_dst, const char* sbase, unsigned v0, unsigned v1, unsigned v2, unsigned on) {
  asm volatile("s_cmp_eq_u32 %5, 0\n\ts_cbranch_scc1 1f\n\t"
               "s_mov_b32 m0, %0\n\ts_nop 0\n\tglobal_load_lds_dwordx4 %1, %4 nt\n\t"
               "s_add_u32 m0, m0, 0x400\n\ts_nop 0\n\tglobal_load_lds_dwordx4 %2, %4 nt\n\t"
               "s_add_u32 m0, m0, 0x400\n\ts_nop 0\n\tglobal_load_lds_dwordx4 %3, %4 nt\n1:"
               :: "s"(lds_dst), "v"(v0), "v"(v1), "v"(v2), "s"(sbase), "s"(on) : "memory", "scc");
}
DI void flag_st(const char* p, unsigned v) {
  unsigned a, d;
  asm volatile("s_nop 4\n\tv_mov_b32 %0, 0\n\tv_mov_b32 %1, %3\n\ts_mov_b64 exec, 1\n\tglobal_store_dword %0, %1, %2 sc1\n\ts_mov_b64 exec, -1"
               : "=&v"(a), "=&v"(d) : "s"(p), "s"(v) : "memory");
}
DI void flag_wait(const char* p, unsigned v) {
  unsigned a, d, sv;
  asm volatile("s_nop 4\n\tv_mov_b32 %0, 0\n"
               "1:\n\tglobal_load_dword %1, %0, %3 sc1\n\ts_waitcnt vmcnt(0)\n\tv_readfirstlane_b32 %2, %1\n\ts_nop 1\n\ts_cmp_eq_u32 %2, %4\n\t"
               "s_cbranch_scc1 2f\n\ts_sleep 8\n\ts_branch 1b\n2:"
               : "=&v"(a), "=&v"(d), "=&s"(sv) : "s"(p), "s"(v) : "memory", "scc");
}
DI void flags_init(const char* p, unsigned voff, unsigned long long mask) {
  unsigned d;
  asm volatile("s_nop 4\n\tv_mov_b32 %0, 0\n\ts_mov_b64 exec, %3\n\tglobal_store_dword %1, %0, %2 offset:128 sc1\n\ts_mov_b64 exec, -1"
               : "=&v"(d) : "v"(voff), "s"(p), "s"(mask) : "memory");
}
DI void flags_publish(const char* p, unsigned voff, unsigned long long mask) {
  unsigned d, e;
  asm volatile("s_nop 4\n\tv_mov_b32 %1, 2\n\ts_mov_b64 exec, %4\n"
               "1:\n\tglobal_store_dword %2, %1, %3 sc1\n\tglobal_load_dword %0, %2, %3 offset:128 sc1\n\ts_waitcnt vmcnt(0)\n\t"
               "v_cmp_ne_u32 vcc, 3, %0\n\ts_nop 4\n\ts_cbranch_vccz 2f\n\ts_sleep 16\n\ts_branch 1b\n2:\n\ts_mov_b64 exec, -1"
               : "=&v"(d), "=&v"(e) : "v"(voff), "s"(p), "s"(mask) : "memory", "vcc");
}
DI void fetch_s(unsigned on, unsigned peek, const char* flag, const char* src, unsigned lds_dst, unsigned l16) {
  unsigned a, d, sv;
  asm volatile("s_cmp_eq_u32 %3, 0\n\ts_cbranch_scc1 9f\n\t"
               "v_readfirstlane_b32 %2, %4\n\tv_mov_b32 %0, 0\n\ts_nop 0\n\ts_cmp_eq_u32 %2, 2\n\ts_cbranch_scc1 2f\n"
               "1:\n\tglobal_load_dword %1, %0, %5 sc1\n\ts_waitcnt vmcnt(0)\n\tv_readfirstlane_b32 %2, %1\n\ts_nop 1\n\ts_cmp_eq_u32 %2, 2\n\t"
               "s_cbranch_scc1 2f\n\ts_sleep 8\n\ts_branch 1b\n"
               "2:\n\tv_mov_b32 %1, 3\n\ts_mov_b64 exec, 1\n\tglobal_store_dword %0, %1, %5 offset:128 sc1\n\ts_mov_b64 exec, -1\n\t"
               "s_mov_b32 m0, %7\n\ts_nop 0\n\t"
               "global_load_lds_dwordx4 %8, %6 sc1\n\tglobal_load_lds_dwordx4 %8, %6 offset:1024 sc1\n\tglobal_load_lds_dwordx4 %8, %6 offset:2048 sc1\n\t"
               "global_load_lds_dwordx4 %8, %6 offset:3072 sc1\n\t"
               "v_add_u32 %0, 0x1000, %8\n\ts_add_u32 m0, m0, 0x1000\n\ts_nop 0\n\t"
               "global_load_lds_dwordx4 %0, %6 sc1\n\tglobal_load_lds_dwordx4 %0, %6 offset:1024 sc1\n\tglobal_load_lds_dwordx4 %0, %6 offset:2048 sc1\n\t"
               "global_load_lds_dwordx4 %0, %6 offset:3072 sc1\n\t"
               "v_add_u32 %0, 0x2000, %8\n\ts_add_u32 m0, m0, 0x1000\n\ts_nop 0\n\t"
               "global_load_lds_dwordx4 %0, %6 sc1\n"
               "9:"
               : "=&v"(a), "=&v"(d), "=&s"(sv)
               : "s"(on), "v"(peek), "s"(flag), "s"(src), "s"(lds_dst), "v"(l16) : "memory", "scc");
}
DI void drain_arrive(unsigned on, unsigned lds_addr) {
  unsigned a, o;
  asm volatile("s_cmp_eq_u32 %3, 0\n\ts_cbranch_scc1 9f\n\tv_mov_b32 %0, %2\n\tv_mov_b32 %1, 1\n\ts_waitcnt vmcnt(0)\n\t"
               "s_mov_b64 exec, 1\n\tds_add_u32 %0, %1\n\ts_mov_b64 exec, -1\n9:"
               : "=&v"(a), "=&v"(o) : "s"(lds_addr), "s"(on) : "memory", "scc");
}
DI void wait_vm0() { asm volatile("s_waitcnt vmcnt(0)" ::: "memory"); }
DI void wait_lgkm0() { asm volatile("s_waitcnt lgkmcnt(0)" ::: "memory"); }
DI void wg_barrier() { asm volatile("s_waitcnt lgkmcnt(0)\n\ts_barrier" ::: "memory"); }

DI void grp_arrive(unsigned lds_addr) {
  unsigned a, o;
  asm volatile("v_mov_b32 %1, 1\n\ts_waitcnt lgkmcnt(0)\n\tv_mov_b32 %0, %2\n\ts_mov_b64 exec, 1\n\tds_add_u32 %0, %1\n\ts_mov_b64 exec, -1"
               : "=&v"(a), "=&v"(o) : "s"(lds_addr) : "memory");
}
DI void grp_wait(unsigned lds_addr, unsigned target) {
  unsigned a, v, sv;
  asm volatile("s_nop 1\n\tv_mov_b32 %0, %3\n"
               "1:\n\tds_read_b32 %1, %0\n\ts_waitcnt lgkmcnt(0)\n\tv_readfirstlane_b32 %2, %1\n\ts_nop 1\n\ts_cmp_ge_u32 %2, %4\n\t"
               "s_cbranch_scc1 2f\n\ts_sleep 1\n\ts_branch 1b\n2:"
               : "=&v"(a), "=&v"(v), "=&s"(sv) : "s"(lds_addr), "s"(target) : "memory", "scc");
}
DI void grp_wait_peeked(unsigned lds_addr, unsigned target, unsigned peeked) {
  unsigned a, v, sv;
  asm volatile("s_nop 0\n\tv_readfirstlane_b32 %2, %5\n\tv_mov_b32 %0, %3\n\ts_cmp_ge_u32 %2, %4\n\ts_cbranch_scc1 2f\n"
               "1:\n\tds_read_b32 %1, %0\n\ts_waitcnt lgkmcnt(0)\n\tv_readfirstlane_b32 %2, %1\n\ts_nop 1\n\ts_cmp_ge_u32 %2, %4\n\t"
               "s_cbranch_scc1 2f\n\ts_sleep 1\n\ts_branch 1b\n2:"
               : "=&v"(a), "=&v"(v), "=&s"(sv) : "s"(lds_addr), "s"(target), "v"(peeked) : "memory", "scc");
}
template <int BASE> DI void set_prio_rem(unsigned rem, unsigned partner) {
  unsigned sv;
  asm volatile("s_nop 0\n\tv_readfirstlane_b32 %0, %2\n\ts_nop 0\n\ts_cmp_gt_u32 %1, %0\n\ts_cbranch_scc1 1f\n\ts_setprio %3\n\ts_branch 2f\n1:\n\ts_setprio %4\n2:"
               : "=&s"(sv) : "s"(rem), "v"(partner), "i"(BASE), "i"(BASE + 1) : "scc");
}
template <int PA, int PB> DI void set_prio(int grp) {
  asm volatile("s_cmp_eq_u32 %0, 0\n\ts_cbranch_scc1 1f\n\ts_setprio %2\n\ts_branch 2f\n1:\n\ts_setprio %1\n2:"
               :: "s"(grp), "i"(PA), "i"(PB) : "scc");
}

#define STAMP0(i)
#define STAMPL(i)
#define STAMP_DECL
#define STAMP_OUT
DI int swap23(int n) { return (n & 0x13) | ((n & 4) << 1) | ((n & 8) >> 1); }

#define SB() __builtin_amdgcn_sched_barrier(0)
#define MEMB() asm volatile("" ::: "memory")
#define UNROLL _Pragma("unroll")
#define PMODE 6
#define PRIO_DENSE()
#define PRIO_PURE()

__global__ __launch_bounds__(512, 2) NOPK void swin_kernel(const char* __restrict__ x, const char* __restrict__ wqkv_t,
                                                           const char* __restrict__ wo_t, const char* __restrict__ wqkv_s,
                                                           const char* __restrict__ wo_s, char* __restrict__ out, char* ws, unsigned long long* dbg) {
  __shared__ __attribute__((aligned(16))) char smem[LDS_BYTES];
  const unsigned lds0 = (unsigned)(size_t)smem;
  const int lane = threadIdx.x & 63;
  const int wave = __builtin_amdgcn_readfirstlane(threadIdx.x >> 6);
  const int grp = wave >> 2, w = wave & 3;
  const int col = lane & 31, h = lane >> 5, p = col >> 2, t = col & 3;
  const unsigned l16 = (unsigned)lane * 16u;
  const bool prod = blockIdx.x < 16;
  const bool prodB = prod && blockIdx.x >= 8;
  const int bid = prod ? 0 : blockIdx.x - 16;
  char* const hflagsS = ws + (blockIdx.x & 7) * 8192;
  char* const hflags = hflagsS + (prodB ? 65536 : 0);
  char* const hflag = hflagsS + ((blockIdx.x >> 3) - 2) * 256;
  char* const hdata = ws + 131072 + (blockIdx.x & 7) * 55296;
  const unsigned long long cmask = (1ull << ((NBLK + 7 - (blockIdx.x & 7)) >> 3)) - 1;
  const char* const wq = (prod && !prodB) ? wqkv_s : wqkv_t;
  const char* const wo = (prod && !prodB) ? wo_s : wo_t;

  STAMP_DECL
  STAMP0(0);
  {
    unsigned long long pc; unsigned len;
    asm volatile("s_getpc_b64 %0\n.Lswin_pf_start:\n\ts_mov_b32 %1, .Lswin_pf_end-.Lswin_pf_start" : "=s"(pc), "=s"(len));
    if (CODEPF_COND) {
      UNROLL for (int i = 0; i < 8; ++i) {
        unsigned o = (unsigned)lane * 64u + i * 4096u;
        o = o < len ? o : 0u;
        unsigned keep;
        asm volatile("s_mov_b32 %0, m0\n\ts_mov_b32 m0, %1\n\ts_nop 2\n\tglobal_load_lds_dword %2, %3\n\ts_mov_b32 m0, %0"
                     : "=&s"(keep) : "s"(lds0 + DUM_OFF + 1024), "v"(o), "s"(pc) : "memory");
      }
    }
  }
  const unsigned m1in = lane < 32 ? l16 + 1024u : ((l16 - 512u) ^ 16u) + RS_IN;
  const unsigned m1out = lane < 32 ? l16 + 1024u : ((l16 - 512u) ^ 16u) + RS_OUT;
#define POFF(i, lb, m1) ((i) == 0 ? (lb) : (i) == 1 ? (m1) : (i) == 2 ? ((lb) ^ 16u) : (i) == 3 ? ((lb) ^ 32u) : (i) == 4 ? ((m1) ^ 32u) : ((lb) ^ 48u))
#define PUNI(i, RS) ((i) == 0 ? 0u : (i) == 1 ? 0u : (i) == 2 ? (RS) + 512u : (i) == 3 ? 2u * (RS) : (i) == 4 ? 2u * (RS) : 3u * (RS) + 512u)
#define OPAQUE(v) asm volatile("" : "+v"(v))
#define PIN(v) asm volatile("" : "+v"(v))
#define PFULL(i, m1, RS) ((i) == 0 ? l16 : (i) == 1 ? (m1) : (i) == 2 ? xad<16>(l16, PUNI(2, RS)) : (i) == 3 ? xad<32>(l16, PUNI(3, RS)) : (i) == 4 ? xad<32>(m1, PUNI(4, RS)) : xad<48>(l16, PUNI(5, RS)))
  const __amdgpu_buffer_rsrc_t xrs = __builtin_amdgcn_make_buffer_rsrc((void*)x, 0, 38535168, 0x00020000);
  int win = bid + NBLK * grp;
  u32x4 xf[2][6];
  unsigned xb0;
  { int wy = win / 28, wx = win - wy * 28; xb0 = (unsigned)((wy * 8 + 2 * w) * 224 + wx * 8) * 192u; }
#define XLOAD(n) xf[(n) / 6][(n) % 6] = __builtin_amdgcn_raw_buffer_load_b128(xrs, POFF((n) % 6, l16, m1in), xb0 + ((n) / 6) * ROWB + PUNI((n) % 6, RS_IN), 2)

  if (threadIdx.x < 64) *(unsigned*)(smem + CNT_OFF + threadIdx.x * 4) = 0u;
#define CN(g, o) (lds0 + CNT_OFF + (g) * 64 + (o))
  const unsigned slot = SLOT_OFF + wave * SLOT_B;
  const unsigned cntA = CNT_OFF + grp * 64 + 32, cntB = cntA + 4;
  const float inv96 = 1.0f / 96.0f;

  const unsigned xa_e = slot + (unsigned)((t * 96 + 12 * p + (h ^ t)) * 16);
  const unsigned xa_o = slot + (unsigned)((t * 96 + 12 * p + ((2 + h) ^ t)) * 16);
  const unsigned t1adj = t < 2 ? 6144u : (unsigned)-3072;
  const unsigned sl16 = slot + l16;
#define XA(ks) ((((ks) & 1) ? xa_o : xa_e) + 64 * ((ks) >> 1))

  {
    constexpr unsigned QK = SLOT_OFF + 36864, VO = WF_OFF + 36864;
    const char* xb = x + xb0;
#define XFIRST()                                                                                                 \
  {                                                                                                              \
    UNROLL for (int i = 0; i < 6; ++i) dma16nt(lds0 + slot + i * 1024, xb + PUNI(i, RS_IN), POFF(i, l16, m1in)); \
    UNROLL for (int i = 0; i < 3; ++i) dma16nt(lds0 + slot + 6144 + i * 1024, xb + ROWB + PUNI(i, RS_IN), POFF(i, l16, m1in)); \
  }
    if (grp && w < 3 && !prodB) {
      unsigned qo[3], qf[3];
      UNROLL for (int m = 0; m < 3; ++m) {
        int idx = m * 64 + lane, row = idx / 24, qq = idx - row * 24;
        int c = qq ^ (row >> 1);
        qo[m] = (unsigned)(row * 576 + c * 16);
        qf[m] = (unsigned)(row * 576 + (c ^ 4) * 16);
      }
      UNROLL for (int i = 0; i < 4; ++i)
        UNROLL for (int m = 0; m < 3; ++m)
          dma16(lds0 + QK + (4 * w + i) * 3072 + m * 1024, wq + (4 * w + i) * 8 * 576, (i & 1) ? qf[m] : qo[m]);
    }
    if (wave == 0 && prod) flags_init(hflags, (unsigned)lane * 256u, cmask);
    if (wave == 3 && !prod) flag_st(hflag, 1u);
    if (wave == 7 && !prod) flag_st(hflag + 65536, 1u);
    wg_barrier();
    if (!grp && !prod) XFIRST();
    UNROLL for (int n = 9; n < 12; ++n) XLOAD(n);
    MEMB();
    if (wave == 3) {
      asm volatile("s_waitcnt vmcnt(12)" ::: "memory");
      grp_arrive(CN(0, 20));
    }
    const int pn = swap23(col);
    unsigned qb[4];
    UNROLL for (int e = 0; e < 4; ++e) qb[e] = QK + (unsigned)(pn * 384 + (((2 * e) | h) ^ ((pn >> 1) & 7)) * 16);
    UNROLL for (int e = 0; e < 4; ++e) OPAQUE(qb[e]);
    STAMP0(1);
#define QKF(rt, c2) lds_ld(smem, qb[(c2) & 3] + (rt) * 12288 + ((c2) >> 2) * 128)
#define QKR(ro, c2) lds_ld(smem, qb[(c2) & 3] + (ro) + ((c2) >> 2) * 128)
    if (wave == 7) {
      if (prod) {
        unsigned vv[3];
        UNROLL for (int m = 0; m < 3; ++m) {
          int idx = m * 64 + lane, row2 = idx / 12, q2 = idx - row2 * 12;
          vv[m] = (unsigned)(row2 * 576 + 384 + (q2 ^ (row2 >> 2)) * 16);
        }
        for (int i = 0; i < 6; ++i) {
          UNROLL for (int m = 0; m < 3; ++m) dma16(lds0 + VO + i * 3072 + m * 1024, wo + i * 3072 + m * 1024, l16);
          UNROLL for (int m = 0; m < 3; ++m) dma16(lds0 + VO + 18432 + i * 3072 + m * 1024, wq + i * 16 * 576, vv[m]);
        }
        wait_vm0();
        grp_arrive(CN(0, 4));
      } else {
        grp_wait(CN(0, 16), 6);
        XFIRST();
        wait_vm0();
        fetch_s(1u, 0u, hflag + 65536, hdata + 36864, lds0 + WF_OFF + 18432, l16);
        fetch_s(1u, 2u, hflag + 65536, hdata + 36864 + 9216, lds0 + WF_OFF + 18432 + 9216, l16);
        wait_vm0();
        grp_arrive(CN(0, 24));
      }
    } else if (w < 3) {
      const unsigned cn = lds0 + CNT_OFF;
      if (grp) {
        asm volatile("s_waitcnt vmcnt(3)" ::: "memory");
        grp_arrive(cn + 0);
      }
      grp_wait(cn + 0, 3);
      if (grp) {
        __builtin_amdgcn_s_setprio(3);
        if (!prodB) {
          u32x4 af[3][6], bb[6];
          UNROLL for (int kk = 0; kk < 6; ++kk) { bb[kk] = QKR(w * 12288, kk); af[0][kk] = QKF(0, 6 + kk); }
          UNROLL for (int ct = 1; ct < 3; ++ct)
            UNROLL for (int kk = 0; kk < 6; ++kk) af[ct][kk] = QKF(ct, 6 + kk);
          SB();
          f32x16 d = {};
          UNROLL for (int ct = 0; ct < 3; ++ct)
            UNROLL for (int kk = 0; kk < 6; ++kk) d = mfma32(af[ct][kk], bb[kk], d);
          float r0 = (d[0] + d[1]) + (d[2] + d[3]), r1 = (d[4] + d[5]) + (d[6] + d[7]);
          float r2_ = (d[8] + d[9]) + (d[10] + d[11]), r3 = (d[12] + d[13]) + (d[14] + d[15]);
          float rs = xhalf_add((r0 + r1) + (r2_ + r3));
          *(float*)(smem + RP_OFF + (32 * w + pn) * 4) = rs * -inv96;
          grp_arrive(cn + 8);
          grp_arrive(cn + 16);
        }
        __builtin_amdgcn_s_setprio(JB_PRIO);
        if (!prod) {
          grp_wait(cn + 16, 6);
          XFIRST();
        } else {
        unsigned vb[2];
        UNROLL for (int e = 0; e < 2; ++e) vb[e] = VO + (unsigned)(18432 + pn * 192 + (((2 * e) | h) ^ ((pn >> 2) & 3)) * 16);
        const unsigned tb = VO + (unsigned)((8 * h + ((lane & 15) >> 2)) * 192 + (16 * ((lane >> 4) & 1) + 8 * (lane & 1) + 4 * ((lane >> 1) & 1)) * 2 + w * 64);
        grp_wait(cn + 4, 1);
#define VF(rt, kk) lds_ld(smem, vb[(kk) & 1] + (rt) * 6144 + ((kk) >> 1) * 64)
        u32x4 bfr[6], av[3][6];
        UNROLL for (int kk = 0; kk < 6; ++kk) {
          s16x4 lo = __builtin_amdgcn_ds_read_tr16_b64_v4i16((LAS s16x4*)(smem + tb + kk * 3072));
          s16x4 hi = __builtin_amdgcn_ds_read_tr16_b64_v4i16((LAS s16x4*)(smem + tb + kk * 3072 + 768));
          u32x2 l2 = __builtin_bit_cast(u32x2, lo), h2 = __builtin_bit_cast(u32x2, hi);
          bfr[kk] = u32x4{l2[0], l2[1], h2[0], h2[1]};
          av[0][kk] = VF(0, kk);
        }
        UNROLL for (int rt = 1; rt < 3; ++rt)
          UNROLL for (int kk = 0; kk < 6; ++kk) av[rt][kk] = VF(rt, kk);
        SB();
        f32x16 DB[3];
        float c0 = 0.f, c1 = 0.f;
        UNROLL for (int rt = 0; rt < 3; ++rt) {
          f32x16 d = {};
          UNROLL for (int kk = 0; kk < 6; ++kk) d = mfma32(av[rt][kk], bfr[kk], d);
          DB[rt] = d;
          if (rt == 0) grp_arrive(cn + 12);
          if (rt > 0) { UNROLL for (int i = 0; i < 16; i += 2) { c0 += DB[rt - 1][i]; c1 += DB[rt - 1][i + 1]; } }
          SB();
        }
        UNROLL for (int i = 0; i < 16; i += 2) { c0 += DB[2][i]; c1 += DB[2][i + 1]; }
        const float cm = xhalf_add(c0 + c1) * inv96;
        UNROLL for (int rt = 0; rt < 3; ++rt) {
          UNROLL for (int i = 0; i < 16; ++i) DB[rt][i] -= cm;
          lds_st(smem, WF_OFF + 18432 + (w * 6 + 2 * rt) * 1024 + l16, PACKS(DB[rt], 0));
          lds_st(smem, WF_OFF + 18432 + (w * 6 + 2 * rt + 1) * 1024 + l16, PACKS(DB[rt], 1));
        }
        grp_arrive(cn + 24);
#undef VF
        }
        __builtin_amdgcn_s_setprio(0);
      } else if (!prodB) {
        __builtin_amdgcn_s_setprio(2);
        f32x16 DA[3];
        float a0 = 0.f, a1 = 0.f;
        {
          u32x4 bb[6], af[3][6];
          UNROLL for (int kk = 0; kk < 6; ++kk) { bb[kk] = QKR(w * 12288, 6 + kk); af[0][kk] = QKF(0, kk); }
          UNROLL for (int rt = 1; rt < 3; ++rt)
            UNROLL for (int kk = 0; kk < 6; ++kk) af[rt][kk] = QKF(rt, kk);
          SB();
          UNROLL for (int rt = 0; rt < 3; ++rt) {
            f32x16 d = {};
            UNROLL for (int kk = 0; kk < 6; ++kk) d = mfma32(af[rt][kk], bb[kk], d);
            DA[rt] = d;
            if (rt == 0) grp_arrive(cn + 16);
            if (rt > 0) { UNROLL for (int i = 0; i < 16; i += 2) { a0 += DA[rt - 1][i]; a1 += DA[rt - 1][i + 1]; } }
            SB();
          }
          UNROLL for (int i = 0; i < 16; i += 2) { a0 += DA[2][i]; a1 += DA[2][i + 1]; }
        }
        grp_wait(cn + 8, 3);
        f32x4 rm[12];
        UNROLL for (int n = 0; n < 12; ++n) rm[n] = *(const f32x4*)(smem + RP_OFF + (32 * (n >> 2) + 16 * ((n >> 1) & 1) + 8 * h + 4 * (n & 1)) * 4);
        UNROLL for (int n = 0; n < 12; ++n) {
          a0 += rm[n][0] + rm[n][2]; a1 += rm[n][1] + rm[n][3];
          UNROLL for (int i = 0; i < 4; ++i) DA[n >> 2][4 * (n & 3) + i] += rm[n][i];
        }
        const float fA = 0.10206207261596575f * 1.4426950408889634f;
        const float cadd = xhalf_add(a0 + a1) * (-fA * inv96);
        UNROLL for (int rt = 0; rt < 3; ++rt) {
          UNROLL for (int i = 0; i < 16; ++i) DA[rt][i] = __builtin_fmaf(DA[rt][i], fA, cadd);
          lds_st(smem, WF_OFF + (w * 6 + 2 * rt) * 1024 + l16, PACKS(DA[rt], 0));
          lds_st(smem, WF_OFF + (w * 6 + 2 * rt + 1) * 1024 + l16, PACKS(DA[rt], 1));
        }
        grp_arrive(cn + 20);
      }
    }
#undef QKF
#undef QKR
#undef XFIRST
    STAMP0(2);
    if (prod) {
      grp_wait(CN(0, 20), prodB ? 0 : 4);
      grp_wait(CN(0, 24), 3);
      const unsigned so = prodB ? 18432u : 0u;
      const __amdgpu_buffer_rsrc_t hrs = __builtin_amdgcn_make_buffer_rsrc(hdata + 2 * so, 0, 36864 - so, 0x00020000);
      UNROLL for (int i = 0; i < 5; ++i) {
        const unsigned o = (unsigned)(wave + 8 * i) * 1024u + l16;
        __builtin_amdgcn_raw_buffer_store_b128(lds_ld(smem, WF_OFF + so + o), hrs, o, 0, 16);
      }
      wait_vm0();
      wg_barrier();
      if (wave == 0) flags_publish(hflags, (unsigned)lane * 256u, cmask);
      return;
    }
  }
  wait_vm0();
  UNROLL for (int ks = 0; ks < 6; ++ks) xf[0][ks] = lds_ld(smem, XA(ks));
  MEMB();
  UNROLL for (int i = 0; i < 3; ++i) lds_st(smem, sl16 + i * 1024, xf[1][3 + i]);
  MEMB();
  UNROLL for (int ks = 0; ks < 6; ++ks) xf[1][ks] = lds_ld(smem, t1adj + XA(ks));
  MEMB();

  const bool own = (t == w);
  const unsigned exo = (unsigned)(h * 384 + p * 48 + ((t ^ w) - 1) * 16);
  const unsigned ex_w = own ? (unsigned)(DUM_OFF + h * 128 + ((3 * (p & 6) + 6 + (p & 1)) & 7) * 16)
                            : SLOT_OFF + (grp * 4 + t) * SLOT_B + exo;
  const unsigned ex_r = slot + exo;
  const unsigned long long notown = ~(0x1111111111111111ull << w);
  const unsigned rbase = R_OFF + wave * 256;
  unsigned wS = l16 + 36864u;
  asm volatile("" : "+v"(wS));
#define WLD(i) lds_ld(smem, ((i) < 36 ? l16 + (i) * 1024 : wS + ((i) - 36) * 1024))

  const unsigned ONES = 0x3F803F80u;
  const unsigned id0 = t == 0 ? 0x00003F80u : (t == 1 ? 0x3F800000u : 0u);
  const unsigned id1 = t == 2 ? 0x00003F80u : (t == 3 ? 0x3F800000u : 0u);
  unsigned msk[4];
  UNROLL for (int g = 0; g < 4; ++g) msk[g] = ((col >> 3) == g && h == (p & 1)) ? 0xffffffffu : 0u;
  const f32x4 z4 = {0.f, 0.f, 0.f, 0.f};
  const f32x16 zz = {};

  grp_wait(CN(0, 20), 4);

  u32x4 wb[4];
  wb[2] = WLD(0); wb[3] = WLD(1); wb[0] = WLD(2);
  u32x4 x1[2][6];
  UNROLL for (int a = 0; a < 2; ++a) UNROLL for (int i = 0; i < 6; ++i) x1[a][i] = wb[0];
  unsigned iter = 1;

#define OSTORE(n_) __builtin_amdgcn_raw_buffer_store_b128(x1[(n_) / 6][(n_) % 6], (n_) < 6 ? ors0 : ors1, PFULL((n_) % 6, m1out, RS_OUT), 0, 2)
#define LNFIN1(g0, g1, q0, q1)                                                                                    \
  {                                                                                                              \
    PIN(g0); PIN(g1);                                                                                            \
    float q0a = (t & 1) ? g0[1] : g0[0], q0b = (t & 1) ? g0[3] : g0[2];                                          \
    float q1a = (t & 1) ? g1[1] : g1[0], q1b = (t & 1) ? g1[3] : g1[2];                                          \
    q0 = (t & 2) ? q0b : q0a; q1 = (t & 2) ? q1b : q1a;                                                          \
  }
#define LNFIN2(sm0, sm1, q0, q1, r)                                                                              \
  {                                                                                                              \
    PIN(q0);                                                                                                     \
    float mean = merge_add(sm0[0], sm1[0]) * inv96;                                                              \
    float var = merge_add(q0, q1) * inv96 - mean * mean;                                                         \
    var = __builtin_fmaxf(var, 0.f);                                                                             \
    r = __builtin_amdgcn_rsqf(var + 1e-5f);                                                                      \
  }
#define T1_FILL(g)                                                                                               \
  {                                                                                                              \
    if ((g) < 3) { MEMB(); x1[0][2 * (g)] = lds_ld(smem, sl16 + (2 * (g)) * 1024); x1[0][2 * (g) + 1] = lds_ld(smem, sl16 + (2 * (g) + 1) * 1024); MEMB(); } \
    if ((g) >= 3 && (g) < 9) lds_st(smem, XA((g) - 3), x1[1][(g) >= 3 && (g) < 9 ? (g) - 3 : 0]);                \
    if ((g) >= 9 && (g) < 12) { const int k_ = (g) >= 9 && (g) < 12 ? 2 * ((g) - 9) : 0; MEMB(); x1[1][k_] = lds_ld(smem, sl16 + k_ * 1024); x1[1][k_ + 1] = lds_ld(smem, sl16 + (k_ + 1) * 1024); MEMB(); } \
    if ((g) == 10) LNFIN1(g0, g1, q0, q1);                                                                       \
    if ((g) == 12) LNFIN2(sm0, sm1, q0, q1, rr);                                                                 \
    if ((g) >= 13 && ((g) & 1)) OSTORE(((g) - 13) / 2);                                                          \
    if (((g) >= 14 && (g) <= 20 && !((g) & 1)) || ((g) >= 26 && (g) <= 32 && !((g) & 1))) {                      \
      const int tp_ = (g) >= 26 ? 1 : 0, k_ = (((g) - 14) % 12) / 2, a_ = (k_ >> 1) & 1, s_ = k_ & 1;           \
      PIN(acc[tp_][a_]);                                                                                         \
      uf[a_][2 * tp_ + s_] = PACKS(acc[tp_][a_], s_);                                                            \
    }                                                                                                            \
    if ((g) == 22) grp_arrive(lds0 + cntB);                                                                      \
    if ((g) == 24) { MEMB(); b1v = *(const unsigned*)(smem + CNT_OFF + 24); }                                    \
    if ((g) == 28) grp_wait_peeked(CN(0, 24), 1, b1v);                                                           \
  }
#define T1_STAGE()                                                                                               \
  {                                                                                                              \
    f32x4 sm0 = z4, sm1 = z4, g0 = z4, g1 = z4;                                                                  \
    unsigned b1v;                                                                                                \
    float q0, q1;                                                                                                \
    UNROLL for (int ks = 0; ks < 6; ++ks) {                                                                      \
      lds_st(smem, XA(ks), x1[0][ks]);                                                                           \
      UNROLL for (int c = 0; c < 2; ++c) {                                                                       \
        sm0 = mfma4(ONES, ONES, xf[0][ks][2 * c], xf[0][ks][2 * c + 1], sm0);                                    \
        sm1 = mfma4(ONES, ONES, xf[1][ks][2 * c], xf[1][ks][2 * c + 1], sm1);                                    \
        g0 = mfma4(xf[0][ks][2 * c], xf[0][ks][2 * c + 1], xf[0][ks][2 * c], xf[0][ks][2 * c + 1], g0);          \
        g1 = mfma4(xf[1][ks][2 * c], xf[1][ks][2 * c + 1], xf[1][ks][2 * c], xf[1][ks][2 * c + 1], g1);          \
      }                                                                                                          \
      SB();                                                                                                      \
    }                                                                                                            \
    UNROLL for (int i = 0; i < 18; ++i) {                                                                        \
      const int tt = i / 6, ks = i % 6;                                                                          \
      acc[tt & 1][0] = mfma32(wb[(i + 2) & 3], xf[0][ks], ks == 0 ? zz : acc[tt & 1][0]);                        \
      SB();                                                                                                      \
      wb[(i + 5) & 3] = WLD(i + 3);                                                                              \
      T1_FILL(2 * i);                                                                                            \
      SB();                                                                                                      \
      acc[tt & 1][1] = mfma32(wb[(i + 2) & 3], xf[1][ks], ks == 0 ? zz : acc[tt & 1][1]);                        \
      SB();                                                                                                      \
      T1_FILL(2 * i + 1);                                                                                        \
      SB();                                                                                                      \
    }                                                                                                            \
  }

  unsigned rem = (unsigned)((784 - win + 2 * NBLK - 1) / (2 * NBLK)) * 90u + (unsigned)grp;
  unsigned prem = 0;
#define STAGE(BASE, WGT)                                                                                         \
  {                                                                                                              \
    set_prio_rem<BASE>(rem, prem);                                                                               \
    MEMB();                                                                                                      \
    *(unsigned*)(smem + CNT_OFF + 128 + wave * 4) = rem;                                                                \
    prem = *(const unsigned*)(smem + CNT_OFF + 128 + (wave ^ 4) * 4);                                                   \
    MEMB();                                                                                                      \
    rem -= (WGT);                                                                                                \
  }
  unsigned ob = 0;
  PRIO_PURE();
  for (;;) {
    STAMPL(3);
    float rr;
    u32x4 uf[2][6];
    f32x16 acc[2][2];
    STAGE(PB_PURE, 16);
    const __amdgpu_buffer_rsrc_t ors0 = __builtin_amdgcn_make_buffer_rsrc(out + (size_t)ob, 0, iter > 1 ? 0x41000 : 0, 0x00020000);
    const __amdgpu_buffer_rsrc_t ors1 = __builtin_amdgcn_make_buffer_rsrc(out + (size_t)(ob + ROWB), 0, iter > 1 ? 0x41000 : 0, 0x00020000);
    T1_STAGE();
    STAMPL(4);
    STAGE(T2BASE, 20);
    u32x4 pt[2][2];
    unsigned a2v, fpk;
    const unsigned fon = (unsigned)__builtin_amdgcn_readfirstlane(!grp && iter == 1);
    const __amdgpu_buffer_rsrc_t frs = __builtin_amdgcn_make_buffer_rsrc(hflag, 0, fon * 4, 0x00020000);
    const int nwin = win + 2 * NBLK;
    const bool more = nwin < 784;
    const char* xb;
    unsigned pfon;
#define NEXTADDR()                                                                                               \
  {                                                                                                              \
    int nw = more ? nwin : win; int wy = nw / 28, wx = nw - wy * 28;                                             \
    xb = x + (unsigned)((wy * 8 + 2 * w) * 224 + wx * 8) * 192u;                                                 \
    pfon = (unsigned)__builtin_amdgcn_readfirstlane(more ? 0x7fffffff : 0);                                      \
    asm volatile("" : "+s"(xb), "+s"(pfon));                                                                     \
  }
    {
      f32x4 s0, s1, s0b, s1b;
      f32x16 vac[2][2];
      f32x16 oac[2];
      u32x4 vpk[2][2];
      float rs0, rs1, rs2, rs3, y0, y1, y2, y3, e0, e1, e2, e3;
      unsigned cbv;
      unsigned pa0, pa1, pb0, pb1;
      float sinv;
#define O_INIT(tt)                                                                                               \
  UNROLL for (int a = 0; a < 2; ++a) {                                                                           \
    f32x16 o;                                                                                                    \
    UNROLL for (int g = 0; g < 4; ++g) {                                                                         \
      f32x4 r4 = mfma4(id0, id1, xf[a][2 * (tt) + (g >> 1)][2 * (g & 1)], xf[a][2 * (tt) + (g >> 1)][2 * (g & 1) + 1], z4); \
      UNROLL for (int e = 0; e < 4; ++e) o[4 * g + e] = r4[e];                                                   \
    }                                                                                                            \
    oac[a] = o;                                                                                                  \
  }
#define O_STEP(tt)                                                                                               \
  O_INIT(tt);                                                                                                    \
  SB();                                                                                                          \
  oac[0] = mfma32(vpk[0][0], pt[0][0], oac[0]);                                                                  \
  oac[0] = mfma32(vpk[0][1], pt[0][1], oac[0]);                                                                  \
  oac[1] = mfma32(vpk[1][0], pt[1][0], oac[1]);                                                                  \
  oac[1] = mfma32(vpk[1][1], pt[1][1], oac[1]);
#define S2_STEP(tt, a, s)                                                                                        \
  PIN(oac[a]);                                                                                                   \
  x1[a][2 * (tt) + (s)] = PACKS(oac[a], s);                                                                      \
  lds_st(smem, ex_w + ((a) * 6 + 2 * (tt) + (s)) * 768, x1[a][2 * (tt) + (s)]);
#define VP_STEP(tt, k) { PIN(vac[(tt) & 1][(k) >> 1]); vpk[(k) >> 1][(k) & 1] = PACKS(vac[(tt) & 1][(k) >> 1], (k) & 1); }
#define T2_FILL(g)                                                                                               \
  {                                                                                                              \
    if ((g) >= 1 && (g) <= 4) { const int k_ = (g) >= 1 && (g) <= 4 ? (g) - 1 : 0; PIN(acc[0][k_ >> 1]); uf[k_ >> 1][4 + (k_ & 1)] = PACKS(acc[0][k_ >> 1], k_ & 1); } \
    if ((g) == 5) {                                                                                              \
      s0 = z4; s1 = z4; s0b = z4; s1b = z4;                                                                      \
      UNROLL for (int k2 = 0; k2 < 6; ++k2) {                                                                    \
        s0 = mfma4(xf[0][k2][0], xf[0][k2][1], uf[0][k2][0], uf[0][k2][1], s0);                                  \
        s1 = mfma4(xf[1][k2][0], xf[1][k2][1], uf[1][k2][0], uf[1][k2][1], s1);                                  \
        s0b = mfma4(xf[0][k2][2], xf[0][k2][3], uf[0][k2][2], uf[0][k2][3], s0b);                                \
        s1b = mfma4(xf[1][k2][2], xf[1][k2][3], uf[1][k2][2], uf[1][k2][3], s1b);                                \
      }                                                                                                          \
      rs0 = quad_bcast<0>(rr); rs1 = quad_bcast<1>(rr); rs2 = quad_bcast<2>(rr); rs3 = quad_bcast<3>(rr);        \
    }                                                                                                            \
    if ((g) == 6) { PIN(s0); PIN(s1); UNROLL for (int e = 0; e < 4; ++e) { s0[e] += s0b[e]; } s1[0] += s1b[0]; s1[1] += s1b[1]; } \
    if ((g) == 7) { PIN(s1); s1[2] += s1b[2]; s1[3] += s1b[3]; y0 = merge_add(s0[0], s1[0]) * rs0; }             \
    if ((g) == 8) { PIN(s0); y1 = merge_add(s0[1], s1[1]) * rs1; y2 = merge_add(s0[2], s1[2]); }                 \
    if ((g) == 9) { PIN(s0); y2 *= rs2; y3 = merge_add(s0[3], s1[3]) * rs3; }                                    \
    if ((g) == 10) {                                                                                             \
      PIN(y3);                                                                                                   \
      float mx = __builtin_fmaxf(__builtin_fmaxf(y0, y1), __builtin_fmaxf(y2, y3));                              \
      float cc = -rr * mx;                                                                                       \
      y0 = __builtin_fmaf(y0, rr, cc); y1 = __builtin_fmaf(y1, rr, cc);                                          \
      y2 = __builtin_fmaf(y2, rr, cc); y3 = __builtin_fmaf(y3, rr, cc);                                          \
    }                                                                                                            \
    if ((g) == 11) { PIN(y0); e0 = __builtin_amdgcn_exp2f(y0); e1 = __builtin_amdgcn_exp2f(y1); e2 = __builtin_amdgcn_exp2f(y2); } \
    if ((g) == 12) { PIN(y3); e3 = __builtin_amdgcn_exp2f(y3); sinv = __builtin_amdgcn_rcpf((e0 + e1) + (e2 + e3)); } \
    if ((g) == 13) { PIN(e0); e0 *= rs0; e1 *= rs1; e2 *= rs2; e3 *= rs3; e0 *= sinv; e1 *= sinv; }              \
    if ((g) == 14) {                                                                                             \
      PIN(e2);                                                                                                   \
      unsigned p01 = pk(e0, e1), p23 = pk(e2 * sinv, e3 * sinv);                                                 \
      auto a_ = __builtin_amdgcn_permlane32_swap(p01, p01, false, false);                                        \
      auto b_ = __builtin_amdgcn_permlane32_swap(p23, p23, false, false);                                        \
      pa0 = a_[0]; pa1 = a_[1]; pb0 = b_[0]; pb1 = b_[1];                                                        \
    }                                                                                                            \
    if ((g) == 15) { PIN(pa0); pt[0][0] = u32x4{pa0 & msk[0], pb0 & msk[0], pa0 & msk[1], pb0 & msk[1]}; { MEMB(); cbv = *(const unsigned*)(smem + cntB); } } \
    if ((g) == 16) { PIN(pa0); pt[0][1] = u32x4{pa0 & msk[2], pb0 & msk[2], pa0 & msk[3], pb0 & msk[3]}; }       \
    if ((g) == 17) { PIN(pa1); pt[1][0] = u32x4{pa1 & msk[0], pb1 & msk[0], pa1 & msk[1], pb1 & msk[1]}; }       \
    if ((g) == 18) { PIN(pa1); pt[1][1] = u32x4{pa1 & msk[2], pb1 & msk[2], pa1 & msk[3], pb1 & msk[3]}; }       \
    if ((g) >= 19 && (g) <= 22) VP_STEP(0, (g) >= 19 && (g) <= 22 ? (g) - 19 : 0);                               \
    if ((g) == 23) { grp_wait_peeked(lds0 + cntB, 4 * iter, cbv); O_STEP(0); }                                   \
    if ((g) >= 25 && (g) <= 28) { const int k_ = (g) >= 25 && (g) <= 28 ? (g) - 25 : 0; S2_STEP(0, k_ >> 1, k_ & 1); } \
    if ((g) >= 29 && (g) <= 32) VP_STEP(1, (g) >= 29 && (g) <= 32 ? (g) - 29 : 0);                               \
    if ((g) == FPK_GAP) fpk = __builtin_amdgcn_raw_buffer_load_b32(frs, 0, 0, 16);                               \
    if ((g) == 24) fetch_s(fon, fpk, hflag, hdata + w * 9216, lds0 + WF_OFF + 36864 + w * 9216, l16);            \
    if ((g) == 30) { MEMB(); a2v = *(const unsigned*)(smem + CNT_OFF + 64 + 20); }                               \
    if ((g) == 33) { O_STEP(1); }                                                                                \
    if ((g) == 35) { S2_STEP(1, 0, 0); S2_STEP(1, 0, 1); }                                                       \
  }
      UNROLL for (int j = 0; j < 18; ++j) {
        const int tt = j / 6, ks = j % 6;
        vac[tt & 1][0] = mfma32(xf[0][ks], wb[(j + 20) & 3], ks == 0 ? zz : vac[tt & 1][0]);
        SB();
        if (j < 15) wb[(j + 23) & 3] = WLD(j + 18 + 3);
        T2_FILL(2 * j);
        SB();
        vac[tt & 1][1] = mfma32(xf[1][ks], wb[(j + 20) & 3], ks == 0 ? zz : vac[tt & 1][1]);
        SB();
        T2_FILL(2 * j + 1);
        SB();
      }
      S2_STEP(1, 1, 0); S2_STEP(1, 1, 1);
      SB();
      O_INIT(2);
      VP_STEP(2, 0); VP_STEP(2, 1);
      SB();
      oac[0] = mfma32(vpk[0][0], pt[0][0], oac[0]);
      oac[0] = mfma32(vpk[0][1], pt[0][1], oac[0]);
      SB();
      VP_STEP(2, 2); VP_STEP(2, 3);
      SB();
      oac[1] = mfma32(vpk[1][0], pt[1][0], oac[1]);
      oac[1] = mfma32(vpk[1][1], pt[1][1], oac[1]);
      SB();
      NEXTADDR();
      S2_STEP(2, 0, 0); S2_STEP(2, 0, 1);
      SB();
      S2_STEP(2, 1, 0); S2_STEP(2, 1, 1);
#undef O_INIT
#undef VP_STEP
#undef T2_FILL
#undef O_STEP
#undef S2_STEP
    }
    STAMPL(5);
    grp_arrive(lds0 + cntA);
    drain_arrive(fon, CN(1, 20));
    grp_wait_peeked(CN(1, 20), 4, a2v);
    wb[0] = WLD(36); wb[1] = WLD(37); wb[2] = WLD(38);
    grp_wait(lds0 + cntA, 4 * iter);
    asm volatile("s_mov_b64 exec, %13\n\t"
                 "ds_read_b128 %0, %12\n\tds_read_b128 %1, %12 offset:768\n\tds_read_b128 %2, %12 offset:1536\n\t"
                 "ds_read_b128 %3, %12 offset:2304\n\tds_read_b128 %4, %12 offset:3072\n\tds_read_b128 %5, %12 offset:3840\n\t"
                 "ds_read_b128 %6, %12 offset:4608\n\tds_read_b128 %7, %12 offset:5376\n\tds_read_b128 %8, %12 offset:6144\n\t"
                 "ds_read_b128 %9, %12 offset:6912\n\tds_read_b128 %10, %12 offset:7680\n\tds_read_b128 %11, %12 offset:8448\n\t"
                 "s_mov_b64 exec, -1\n\ts_waitcnt lgkmcnt(0)"
                 : "+v"(x1[0][0]), "+v"(x1[0][1]), "+v"(x1[0][2]), "+v"(x1[0][3]), "+v"(x1[0][4]), "+v"(x1[0][5]),
                   "+v"(x1[1][0]), "+v"(x1[1][1]), "+v"(x1[1][2]), "+v"(x1[1][3]), "+v"(x1[1][4]), "+v"(x1[1][5])
                 : "v"(lds0 + ex_r), "s"(notown) : "memory");
    float rq[2];
    const unsigned rka = rbase + h * 16;
#define RKLD(G) (*(const f32x4*)(smem + rka + (G) * 32))
    f32x4 rkb[3];
    STAMPL(6);
    STAGE(PB_PURE, 14);
    const __amdgpu_buffer_rsrc_t nrs = __builtin_amdgcn_make_buffer_rsrc((void*)(xb + ROWB), 0, pfon, 0x00020000);
    u32x4 pre[3];
    SB();
#define SW(k) ((k) < 18 ? 36 + (k) : ((k) < 54 ? 18 + (k) : (k) - 54))
    u32x4 pf[2][4];
    u32x4 vp[3][4];
    f32x16 sa[2][2];
    u32x4 uq[6];
    f32x16 ua[2];
    float mxv, ccv, l0, l1, invv;
#define U_STEP(q, i)                                                                                             \
  {                                                                                                              \
    const int tt_ = (i) / 6, ks_ = (i) % 6, k_ = 18 * (q) + (i);                                                 \
    ua[tt_ & 1] = mfma32(wb[k_ & 3], x1[q][ks_], ks_ == 0 ? zz : ua[tt_ & 1]);                                   \
    SB();                                                                                                        \
    wb[(k_ + 3) & 3] = WLD(SW(k_ + 3));                                                                          \
    if (tt_ > 0 && (ks_ == 2 || ks_ == 3)) { PIN(ua[(tt_ + 1) & 1]); uq[2 * (tt_ - 1) + (ks_ - 2)] = PACKS(ua[(tt_ + 1) & 1], ks_ - 2); } \
  }
#define S_STEP(q, ks_)                                                                                           \
  {                                                                                                              \
    sa[q][0] = mfma32(x1[0][ks_], uq[ks_], (ks_) == 0 ? zz : sa[q][0]);                                          \
    SB();                                                                                                        \
    if ((ks_) == 1) { PIN(ua[0]); uq[4] = PACKS(ua[0], 0); }                                                     \
    if ((ks_) == 2) { PIN(ua[0]); uq[5] = PACKS(ua[0], 1); }                                                     \
    SFILL;                                                                                                       \
    SB();                                                                                                        \
    sa[q][1] = mfma32(x1[1][ks_], uq[ks_], (ks_) == 0 ? zz : sa[q][1]);                                          \
    SB();                                                                                                        \
  }
#define SFILL
#define P1(q, G)                                                                                                 \
  {                                                                                                              \
    const int n_ = 16 * (q) + (G);                                                                               \
    MEMB(); rkb[(n_ + 2) % 3] = RKLD(((G) + 2) & 7);                                                             \
    UNROLL for (int e = 0; e < 4; ++e) sa[q][(G) >> 2][4 * ((G) & 3) + e] *= rkb[n_ % 3][e];                     \
    mxv = (G) == 0 ? sa[q][0][0] : __builtin_fmaxf(mxv, sa[q][(G) >> 2][4 * ((G) & 3)]);                         \
    mxv = __builtin_fmaxf(mxv, sa[q][(G) >> 2][4 * ((G) & 3) + 1]);                                              \
    mxv = __builtin_fmaxf(__builtin_fmaxf(mxv, sa[q][(G) >> 2][4 * ((G) & 3) + 2]), sa[q][(G) >> 2][4 * ((G) & 3) + 3]); \
  }
#define X1(q) { mxv = xhalf_max(mxv); ccv = -rq[q] * mxv; l0 = 0.f; l1 = 0.f; }
#define P2H(q, H)                                                                                                \
  {                                                                                                              \
    const int b_ = (H) >> 3, i_ = 2 * ((H) & 7), G_ = (H) >> 1, n_ = 16 * (q) + 8 + G_;                          \
    if (((H) & 1) == 0 && n_ < 30) { MEMB(); rkb[(n_ + 2) % 3] = RKLD((G_ + 2) & 7); }                           \
    const float ea = __builtin_amdgcn_exp2f(__builtin_fmaf(sa[q][b_][i_], rq[q], ccv));                          \
    const float eb = __builtin_amdgcn_exp2f(__builtin_fmaf(sa[q][b_][i_ + 1], rq[q], ccv));                      \
    l0 += ea; l1 += eb;                                                                                          \
    sa[q][b_][i_] = ea * rkb[n_ % 3][2 * ((H) & 1)]; sa[q][b_][i_ + 1] = eb * rkb[n_ % 3][2 * ((H) & 1) + 1];    \
  }
#define X2(q) { invv = __builtin_amdgcn_rcpf(xhalf_add(l0 + l1)); }
#define P3(q, F)                                                                                                 \
  {                                                                                                              \
    UNROLL for (int e = 0; e < 8; ++e) sa[q][(F) >> 1][8 * ((F) & 1) + e] *= invv;                               \
    pf[q][F] = PACKS(sa[q][(F) >> 1], (F) & 1);                                                                  \
  }
    {
      f32x4 sm0 = z4, sm1 = z4, g0 = z4, g1 = z4;
      UNROLL for (int ks = 0; ks < 6; ++ks) {
        UNROLL for (int c = 0; c < 2; ++c) {
          sm0 = mfma4(ONES, ONES, x1[0][ks][2 * c], x1[0][ks][2 * c + 1], sm0);
          sm1 = mfma4(ONES, ONES, x1[1][ks][2 * c], x1[1][ks][2 * c + 1], sm1);
          g0 = mfma4(x1[0][ks][2 * c], x1[0][ks][2 * c + 1], x1[0][ks][2 * c], x1[0][ks][2 * c + 1], g0);
          g1 = mfma4(x1[1][ks][2 * c], x1[1][ks][2 * c + 1], x1[1][ks][2 * c], x1[1][ks][2 * c + 1], g1);
        }
        SB();
      }
      float q0, q1;
      UNROLL for (int i = 0; i < 18; ++i) {
        U_STEP(0, i);
        if (i == 1) LNFIN1(g0, g1, q0, q1);
        if (i == 2) {
          float r2;
          LNFIN2(sm0, sm1, q0, q1, r2);
          *(float*)(smem + rbase + lane * 4) = r2;
          auto rb = __builtin_amdgcn_permlane32_swap(ubits(r2), ubits(r2), false, false);
          rq[0] = fbits(rb[0]); rq[1] = fbits(rb[1]);
          MEMB();
        }
        {
          const int n = i < 8 ? i - 3 : (i < 10 ? -1 : (i < 14 ? i - 5 : -1));
          if (n >= 0 && n < 6) dma1ntc(lds0 + slot + n * 1024, xb, PFULL(n >= 0 && n < 6 ? n : 0, m1in, RS_IN), pfon);
          if (n >= 6) dma1ntc(lds0 + slot + n * 1024, xb + ROWB, PFULL(n >= 6 ? n - 6 : 0, m1in, RS_IN), pfon);
        }
        if (i >= 16) pre[i - 16] = __builtin_amdgcn_raw_buffer_load_b128(nrs, PFULL(i >= 16 ? i - 13 : 0, m1in, RS_IN), 0, 2);
        SB();
      }
    }
    UNROLL for (int ks = 0; ks < 6; ++ks) {
      S_STEP(0, ks);
      if (ks == 3) pre[2] = __builtin_amdgcn_raw_buffer_load_b128(nrs, PFULL(5, m1in, RS_IN), 0, 2);
      if (ks == 4) { rkb[0] = RKLD(0); rkb[1] = RKLD(1); }
      SB();
    }
    STAMPL(7);
    PRIO_DENSE();
    STAGE(PB_DENSE, 30);
    UNROLL for (int i = 0; i < 18; ++i) {
      U_STEP(1, i);
      if (i >= 1 && i <= 4) { P1(0, 2 * (i - 1)); P1(0, 2 * (i - 1) + 1); }
      if (i == 5) X1(0);
      if (i >= 6) P2H(0, i - 6);
      SB();
    }
#undef SFILL
#define SFILL { if (ks == 0) P2H(0, 12); if (ks == 1) P2H(0, 14); if (ks == 3) P3(0, 0); if (ks == 4) P3(0, 2); }
    UNROLL for (int ks = 0; ks < 6; ++ks) {
      S_STEP(1, ks);
      if (ks == 0) P2H(0, 13);
      if (ks == 1) P2H(0, 15);
      if (ks == 2) X2(0);
      if (ks == 3) P3(0, 1);
      if (ks == 4) P3(0, 3);
      SB();
    }
    STAMPL(8);
    {
      f32x16 v0, v1;
#define R2_FILL(g)                                                                                               \
  {                                                                                                              \
    if ((g) >= 1 && (g) <= 8) P1(1, (g) >= 1 && (g) <= 8 ? (g) - 1 : 0);                                         \
    if ((g) == 9) X1(1);                                                                                         \
    { const int H_ = (g) == 10 ? 0 : ((g) >= 14 && (g) <= 22 ? (g) - 13 : ((g) >= 26 && (g) <= 31 ? (g) - 16 : -1)); \
      if (H_ >= 0) P2H(1, H_ >= 0 ? H_ : 0); }                                                                   \
    if ((g) == 32) X2(1);                                                                                        \
    if ((g) == 33) P3(1, 0);                                                                                     \
    if ((g) == 34) P3(1, 1);                                                                                     \
    if ((g) == 11 || (g) == 23 || (g) == 35) { PIN(v0); vp[(g) / 12][0] = PACKS(v0, 0); vp[(g) / 12][1] = PACKS(v0, 1); } \
    if ((g) == 12 || (g) == 24) { PIN(v1); vp[(g) / 12 - 1][2] = PACKS(v1, 0); vp[(g) / 12 - 1][3] = PACKS(v1, 1); } \
  }
      UNROLL for (int j = 0; j < 18; ++j) {
        const int ks = j % 6, k = 36 + j;
        v0 = mfma32(x1[0][ks], wb[k & 3], ks == 0 ? zz : v0);
        SB();
        wb[(k + 3) & 3] = WLD(SW(k + 3));
        R2_FILL(2 * j);
        SB();
        v1 = mfma32(x1[1][ks], wb[k & 3], ks == 0 ? zz : v1);
        SB();
        R2_FILL(2 * j + 1);
        SB();
      }
#undef R2_FILL
      STAMPL(9);
      PRIO_PURE();
      STAGE(PB_PURE, 10);
      f32x16 oc[2];
      UNROLL for (int n = 0; n < 6; ++n) {
        const int q = n / 3, tt = n % 3;
        f32x16 o;
        UNROLL for (int g = 0; g < 4; ++g) {
          f32x4 r4 = mfma4(id0, id1, x1[q][2 * tt + (g >> 1)][2 * (g & 1)], x1[q][2 * tt + (g >> 1)][2 * (g & 1) + 1], z4);
          UNROLL for (int e = 0; e < 4; ++e) o[4 * g + e] = r4[e];
        }
        SB();
        if (n == 0) { PIN(v1); vp[2][2] = PACKS(v1, 0); }
        o = mfma32(vp[tt][0], pf[q][0], o);
        SB();
        if (n == 0) { PIN(v1); vp[2][3] = PACKS(v1, 1); }
        if (n == 1) P3(1, 2);
        if (n == 2) P3(1, 3);
        SB();
        o = mfma32(vp[tt][1], pf[q][1], o);
        SB();
        if (n > 0) { PIN(oc[(n + 1) & 1]); x1[(n - 1) / 3][2 * ((n - 1) % 3)] = PACKS(oc[(n + 1) & 1], 0); }
        SB();
        o = mfma32(vp[tt][2], pf[q][2], o);
        SB();
        if (n > 0) { PIN(oc[(n + 1) & 1]); x1[(n - 1) / 3][2 * ((n - 1) % 3) + 1] = PACKS(oc[(n + 1) & 1], 1); }
        SB();
        o = mfma32(vp[tt][3], pf[q][3], o);
        oc[n & 1] = o;
        SB();
      }
      STAMPL(10);
      wait_vm0();
      UNROLL for (int ks = 0; ks < 6; ++ks) xf[0][ks] = lds_ld(smem, XA(ks));
      MEMB();
      UNROLL for (int i = 0; i < 3; ++i) lds_st(smem, sl16 + i * 1024, pre[i]);
      MEMB();
      UNROLL for (int ks = 0; ks < 6; ++ks) xf[1][ks] = lds_ld(smem, t1adj + XA(ks));
      MEMB();
      x1[1][4] = PACKS(oc[1], 0);
      x1[1][5] = PACKS(oc[1], 1);
    }
    STAMPL(11);
    { int wy = win / 28, wx = win - wy * 28; ob = (unsigned)w * RS_IN + (unsigned)((wy * 8) * 224 + wx * 8) * 192u; }
    if (!more) {
      UNROLL for (int a = 0; a < 2; ++a) {
        UNROLL for (int ks = 0; ks < 6; ++ks) lds_st(smem, XA(ks), x1[a][ks]);
        MEMB();
        UNROLL for (int i = 0; i < 6; ++i) x1[a][i] = lds_ld(smem, sl16 + i * 1024);
        MEMB();
      }
      const __amdgpu_buffer_rsrc_t ors0 = __builtin_amdgcn_make_buffer_rsrc(out + (size_t)ob, 0, 0x41000, 0x00020000);
      const __amdgpu_buffer_rsrc_t ors1 = __builtin_amdgcn_make_buffer_rsrc(out + (size_t)(ob + ROWB), 0, 0x41000, 0x00020000);
      UNROLL for (int n = 0; n < 12; ++n) OSTORE(n);
      STAMPL(12);
      break;
    }
    STAMPL(12);
    win = nwin;
    ++iter;
  }
  *(unsigned*)(smem + CNT_OFF + 128 + wave * 4) = 0u;
  STAMP_OUT;
  asm volatile(".Lswin_pf_end:");
}

extern "C" void kernel_launch(void* const* d_in, const int* in_sizes, int n_in,
                              void* d_out, int out_size, void* d_ws, size_t ws_size,
                              hipStream_t stream) {
  swin_kernel<<<NBLK + 16, 512, 0, stream>>>((const char*)d_in[0], (const char*)d_in[1], (const char*)d_in[2],
                                       (const char*)d_in[3], (const char*)d_in[4], (char*)d_out, (char*)d_ws + (1 << 20), (unsigned long long*)d_ws);
}
```

```cpp
#define PB_PURE 0
#define PB_DENSE 2
#define FPK_GAP 12
#define JB_PRIO 1
#define XB_BEFORE 1
#define NBLK 196
#define NOLOOPST 0
#define T2BASE 2
#define BPREP_PRIO 2
#define CODEPF
#define CODEPF_COND wave == 3
#include <hip/hip_runtime.h>
#include <hip/hip_bf16.h>
#include <stdint.h>

#if defined(__HIP_DEVICE_COMPILE__)
#define NOPK __attribute__((target("no-packed-fp32-ops")))
#else
#define NOPK
#endif
#define DI __device__ __forceinline__ NOPK

typedef float f32x16 __attribute__((ext_vector_type(16)));
typedef float f32x4 __attribute__((ext_vector_type(4)));
typedef unsigned u32x4 __attribute__((ext_vector_type(4)));
typedef unsigned u32x2 __attribute__((ext_vector_type(2)));
typedef __bf16 bf16x8 __attribute__((ext_vector_type(8)));
typedef __bf16 bf16x2 __attribute__((ext_vector_type(2)));
typedef short s16x4 __attribute__((ext_vector_type(4)));
#define LAS __attribute__((address_space(3)))

constexpr int WF_OFF = 0;
constexpr int SLOT_OFF = 73728;
constexpr int SLOT_B = 9216;
constexpr int R_OFF = SLOT_OFF + 8 * SLOT_B;
constexpr int RP_OFF = R_OFF + 2048;
constexpr int CNT_OFF = RP_OFF + 1024;
constexpr int DUM_OFF = CNT_OFF + 256;
constexpr int LDS_BYTES = DUM_OFF + 9216;

constexpr unsigned RS_IN = 224u * 224u * 192u;
constexpr unsigned ROWB = 224u * 192u;
constexpr unsigned RS_OUT = 2u * ROWB;

DI f32x16 mfma32(u32x4 a, u32x4 b, f32x16 c) {
  return __builtin_amdgcn_mfma_f32_32x32x16_bf16(__builtin_bit_cast(bf16x8, a), __builtin_bit_cast(bf16x8, b), c, 0, 0, 0);
}
DI f32x4 mfma4(unsigned a0, unsigned a1, unsigned b0, unsigned b1, f32x4 c) {
  u32x2 a = {a0, a1}, b = {b0, b1};
  return __builtin_amdgcn_mfma_f32_4x4x4bf16_1k(__builtin_bit_cast(s16x4, a), __builtin_bit_cast(s16x4, b), c, 0, 0, 0);
}
DI unsigned pk(float lo, float hi) {
  bf16x2 p = {(__bf16)lo, (__bf16)hi};
  return __builtin_bit_cast(unsigned, p);
}
#define PACKS(x, s) u32x4{pk((x)[8 * (s)], (x)[8 * (s) + 1]), pk((x)[8 * (s) + 2], (x)[8 * (s) + 3]), \
                          pk((x)[8 * (s) + 4], (x)[8 * (s) + 5]), pk((x)[8 * (s) + 6], (x)[8 * (s) + 7])}
DI float fbits(unsigned u) { return __builtin_bit_cast(float, u); }
DI unsigned ubits(float f) { return __builtin_bit_cast(unsigned, f); }
DI float merge_add(float a, float b) {
  auto r = __builtin_amdgcn_permlane32_swap(ubits(a), ubits(b), false, false);
  return fbits(r[0]) + fbits(r[1]);
}
DI float xhalf_add(float a) { return merge_add(a, a); }
DI float xhalf_max(float a) {
  auto r = __builtin_amdgcn_permlane32_swap(ubits(a), ubits(a), false, false);
  return __builtin_fmaxf(fbits(r[0]), fbits(r[1]));
}
template <int I> DI float quad_bcast(float v) {
  return fbits((unsigned)__builtin_amdgcn_mov_dpp((int)ubits(v), I * 0x55, 0xf, 0xf, true));
}
DI u32x4 lds_ld(const char* smem, unsigned off) { return *(const u32x4*)(smem + off); }
DI void lds_st(char* smem, unsigned off, u32x4 v) { *(u32x4*)(smem + off) = v; }

DI void dma16(unsigned lds_dst, const char* sbase, unsigned voff) {
  unsigned keep;
  asm volatile("s_mov_b32 %0, m0\n\ts_mov_b32 m0, %1\n\ts_nop 2\n\tglobal_load_lds_dwordx4 %2, %3\n\ts_mov_b32 m0, %0"
               : "=&s"(keep) : "s"(lds_dst), "v"(voff), "s"(sbase) : "memory");
}
DI void dma16nt(unsigned lds_dst, const char* sbase, unsigned voff) {
  unsigned keep;
  asm volatile("s_mov_b32 %0, m0\n\ts_mov_b32 m0, %1\n\ts_nop 2\n\tglobal_load_lds_dwordx4 %2, %3 nt\n\ts_mov_b32 m0, %0"
               : "=&s"(keep) : "s"(lds_dst), "v"(voff), "s"(sbase) : "memory");
}
DI void dma16ntc(unsigned lds_dst, const char* sbase, unsigned voff, unsigned on) {
  unsigned keep;
  asm volatile("s_cmp_eq_u32 %4, 0\n\ts_cbranch_scc1 1f\n\ts_mov_b32 %0, m0\n\ts_mov_b32 m0, %1\n\ts_nop 0\n\t"
               "global_load_lds_dwordx4 %2, %3 nt\n\ts_mov_b32 m0, %0\n1:"
               : "=&s"(keep) : "s"(lds_dst), "v"(voff), "s"(sbase), "s"(on) : "memory", "scc");
}
template <int K> DI unsigned xad(unsigned a, unsigned c) {
  unsigned r;
  asm volatile("v_xad_u32 %0, %1, %2, %3" : "=v"(r) : "v"(a), "i"(K), "s"(c));
  return r;
}
DI void dma1ntc(unsigned lds_dst, const char* sbase, unsigned v0, unsigned on) {
  asm volatile("s_cmp_eq_u32 %3, 0\n\ts_mov_b32 m0, %0\n\ts_cbranch_scc1 1f\n\ts_nop 1\n\tglobal_load_lds_dwordx4 %1, %2 nt\n1:"
               :: "s"(lds_dst), "v"(v0), "s"(sbase), "s"(on) : "memory", "scc");
}
DI void dma3ntc(unsigned lds_dst, const char* sbase, unsigned v0, unsigned v1, unsigned v2, unsigned on) {
  asm volatile("s_cmp_eq_u32 %5, 0\n\ts_cbranch_scc1 1f\n\t"
               "s_mov_b32 m0, %0\n\ts_nop 0\n\tglobal_load_lds_dwordx4 %1, %4 nt\n\t"
               "s_add_u32 m0, m0, 0x400\n\ts_nop 0\n\tglobal_load_lds_dwordx4 %2, %4 nt\n\t"
               "s_add_u32 m0, m0, 0x400\n\ts_nop 0\n\tglobal_load_lds_dwordx4 %3, %4 nt\n1:"
               :: "s"(lds_dst), "v"(v0), "v"(v1), "v"(v2), "s"(sbase), "s"(on) : "memory", "scc");
}
DI void flag_st(const char* p, unsigned v) {
  unsigned a, d;
  asm volatile("s_nop 4\n\tv_mov_b32 %0, 0\n\tv_mov_b32 %1, %3\n\ts_mov_b64 exec, 1\n\tglobal_store_dword %0, %1, %2 sc1\n\ts_mov_b64 exec, -1"
               : "=&v"(a), "=&v"(d) : "s"(p), "s"(v) : "memory");
}
DI void flag_wait(const char* p, unsigned v) {
  unsigned a, d, sv;
  asm volatile("s_nop 4\n\tv_mov_b32 %0, 0\n"
               "1:\n\tglobal_load_dword %1, %0, %3 sc1\n\ts_waitcnt vmcnt(0)\n\tv_readfirstlane_b32 %2, %1\n\ts_nop 1\n\ts_cmp_eq_u32 %2, %4\n\t"
               "s_cbranch_scc1 2f\n\ts_sleep 8\n\ts_branch 1b\n2:"
               : "=&v"(a), "=&v"(d), "=&s"(sv) : "s"(p), "s"(v) : "memory", "scc");
}
DI void flags_init(const char* p, unsigned voff, unsigned long long mask) {
  unsigned d;
  asm volatile("s_nop 4\n\tv_mov_b32 %0, 0\n\ts_mov_b64 exec, %3\n\tglobal_store_dword %1, %0, %2 offset:128 sc1\n\ts_mov_b64 exec, -1"
               : "=&v"(d) : "v"(voff), "s"(p), "s"(mask) : "memory");
}
DI void flags_publish(const char* p, unsigned voff, unsigned long long mask) {
  unsigned d, e;
  asm volatile("s_nop 4\n\tv_mov_b32 %1, 2\n\ts_mov_b64 exec, %4\n"
               "1:\n\tglobal_store_dword %2, %1, %3 sc1\n\tglobal_load_dword %0, %2, %3 offset:128 sc1\n\ts_waitcnt vmcnt(0)\n\t"
               "v_cmp_ne_u32 vcc, 3, %0\n\ts_nop 4\n\ts_cbranch_vccz 2f\n\ts_sleep 16\n\ts_branch 1b\n2:\n\ts_mov_b64 exec, -1"
               : "=&v"(d), "=&v"(e) : "v"(voff), "s"(p), "s"(mask) : "memory", "vcc");
}
DI void fetch_s(unsigned on, unsigned peek, const char* flag, const char* src, unsigned lds_dst, unsigned l16) {
  unsigned a, d, sv;
  asm volatile("s_cmp_eq_u32 %3, 0\n\ts_cbranch_scc1 9f\n\t"
               "v_readfirstlane_b32 %2, %4\n\tv_mov_b32 %0, 0\n\ts_nop 0\n\ts_cmp_eq_u32 %2, 2\n\ts_cbranch_scc1 2f\n"
               "1:\n\tglobal_load_dword %1, %0, %5 sc1\n\ts_waitcnt vmcnt(0)\n\tv_readfirstlane_b32 %2, %1\n\ts_nop 1\n\ts_cmp_eq_u32 %2, 2\n\t"
               "s_cbranch_scc1 2f\n\ts_sleep 8\n\ts_branch 1b\n"
               "2:\n\tv_mov_b32 %1, 3\n\ts_mov_b64 exec, 1\n\tglobal_store_dword %0, %1, %5 offset:128 sc1\n\ts_mov_b64 exec, -1\n\t"
               "s_mov_b32 m0, %7\n\ts_nop 0\n\t"
               "global_load_lds_dwordx4 %8, %6 sc1\n\tglobal_load_lds_dwordx4 %8, %6 offset:1024 sc1\n\tglobal_load_lds_dwordx4 %8, %6 offset:2048 sc1\n\t"
               "global_load_lds_dwordx4 %8, %6 offset:3072 sc1\n\t"
               "v_add_u32 %0, 0x1000, %8\n\ts_add_u32 m0, m0, 0x1000\n\ts_nop 0\n\t"
               "global_load_lds_dwordx4 %0, %6 sc1\n\tglobal_load_lds_dwordx4 %0, %6 offset:1024 sc1\n\tglobal_load_lds_dwordx4 %0, %6 offset:2048 sc1\n\t"
               "global_load_lds_dwordx4 %0, %6 offset:3072 sc1\n\t"
               "v_add_u32 %0, 0x2000, %8\n\ts_add_u32 m0, m0, 0x1000\n\ts_nop 0\n\t"
               "global_load_lds_dwordx4 %0, %6 sc1\n"
               "9:"
               : "=&v"(a), "=&v"(d), "=&s"(sv)
               : "s"(on), "v"(peek), "s"(flag), "s"(src), "s"(lds_dst), "v"(l16) : "memory", "scc");
}
DI void drain_arrive(unsigned on, unsigned lds_addr) {
  unsigned a, o;
  asm volatile("s_cmp_eq_u32 %3, 0\n\ts_cbranch_scc1 9f\n\tv_mov_b32 %0, %2\n\tv_mov_b32 %1, 1\n\ts_waitcnt vmcnt(0)\n\t"
               "s_mov_b64 exec, 1\n\tds_add_u32 %0, %1\n\ts_mov_b64 exec, -1\n9:"
               : "=&v"(a), "=&v"(o) : "s"(lds_addr), "s"(on) : "memory", "scc");
}
DI void wait_vm0() { asm volatile("s_waitcnt vmcnt(0)" ::: "memory"); }
DI void wait_lgkm0() { asm volatile("s_waitcnt lgkmcnt(0)" ::: "memory"); }
DI void wg_barrier() { asm volatile("s_waitcnt lgkmcnt(0)\n\ts_barrier" ::: "memory"); }

DI void grp_arrive(unsigned lds_addr) {
  unsigned a, o;
  asm volatile("v_mov_b32 %1, 1\n\ts_waitcnt lgkmcnt(0)\n\tv_mov_b32 %0, %2\n\ts_mov_b64 exec, 1\n\tds_add_u32 %0, %1\n\ts_mov_b64 exec, -1"
               : "=&v"(a), "=&v"(o) : "s"(lds_addr) : "memory");
}
DI void grp_wait(unsigned lds_addr, unsigned target) {
  unsigned a, v, sv;
  asm volatile("s_nop 1\n\tv_mov_b32 %0, %3\n"
               "1:\n\tds_read_b32 %1, %0\n\ts_waitcnt lgkmcnt(0)\n\tv_readfirstlane_b32 %2, %1\n\ts_nop 1\n\ts_cmp_ge_u32 %2, %4\n\t"
               "s_cbranch_scc1 2f\n\ts_sleep 1\n\ts_branch 1b\n2:"
               : "=&v"(a), "=&v"(v), "=&s"(sv) : "s"(lds_addr), "s"(target) : "memory", "scc");
}
DI void grp_wait_peeked(unsigned lds_addr, unsigned target, unsigned peeked) {
  unsigned a, v, sv;
  asm volatile("s_nop 0\n\tv_readfirstlane_b32 %2, %5\n\tv_mov_b32 %0, %3\n\ts_cmp_ge_u32 %2, %4\n\ts_cbranch_scc1 2f\n"
               "1:\n\tds_read_b32 %1, %0\n\ts_waitcnt lgkmcnt(0)\n\tv_readfirstlane_b32 %2, %1\n\ts_nop 1\n\ts_cmp_ge_u32 %2, %4\n\t"
               "s_cbranch_scc1 2f\n\ts_sleep 1\n\ts_branch 1b\n2:"
               : "=&v"(a), "=&v"(v), "=&s"(sv) : "s"(lds_addr), "s"(target), "v"(peeked) : "memory", "scc");
}
template <int BASE> DI void set_prio_rem(unsigned rem, unsigned partner) {
  unsigned sv;
  asm volatile("s_nop 0\n\tv_readfirstlane_b32 %0, %2\n\ts_nop 0\n\ts_cmp_gt_u32 %1, %0\n\ts_cbranch_scc1 1f\n\ts_setprio %3\n\ts_branch 2f\n1:\n\ts_setprio %4\n2:"
               : "=&s"(sv) : "s"(rem), "v"(partner), "i"(BASE), "i"(BASE + 1) : "scc");
}
template <int PA, int PB> DI void set_prio(int grp) {
  asm volatile("s_cmp_eq_u32 %0, 0\n\ts_cbranch_scc1 1f\n\ts_setprio %2\n\ts_branch 2f\n1:\n\ts_setprio %1\n2:"
               :: "s"(grp), "i"(PA), "i"(PB) : "scc");
}

#define STAMP0(i)
#define STAMPL(i)
#define STAMP_DECL
#define STAMP_OUT
DI int swap23(int n) { return (n & 0x13) | ((n & 4) << 1) | ((n & 8) >> 1); }

#define SB() __builtin_amdgcn_sched_barrier(0)
#define MEMB() asm volatile("" ::: "memory")
#define UNROLL _Pragma("unroll")
#define PMODE 6
#define PRIO_DENSE()
#define PRIO_PURE()

__global__ __launch_bounds__(512, 2) NOPK void swin_kernel(const char* __restrict__ x, const char* __restrict__ wqkv_t,
                                                           const char* __restrict__ wo_t, const char* __restrict__ wqkv_s,
                                                           const char* __restrict__ wo_s, char* __restrict__ out, char* ws, unsigned long long* dbg) {
  __shared__ __attribute__((aligned(16))) char smem[LDS_BYTES];
  const unsigned lds0 = (unsigned)(size_t)smem;
  const int lane = threadIdx.x & 63;
  const int wave = __builtin_amdgcn_readfirstlane(threadIdx.x >> 6);
  const int grp = wave >> 2, w = wave & 3;
  const int col = lane & 31, h = lane >> 5, p = col >> 2, t = col & 3;
  const unsigned l16 = (unsigned)lane * 16u;
  const bool prod = blockIdx.x < 8;
  const int bid = prod ? 0 : blockIdx.x - 8;
  char* const hflags = ws + (blockIdx.x & 7) * 8192;
  char* const hflag = hflags + ((blockIdx.x >> 3) - 1) * 256;
  char* const hdata = ws + 65536 + (blockIdx.x & 7) * 36864;
  const unsigned long long cmask = (1ull << ((NBLK + 7 - (blockIdx.x & 7)) >> 3)) - 1;
  const char* const wq = prod ? wqkv_s : wqkv_t;
  const char* const wo = prod ? wo_s : wo_t;

  STAMP_DECL
  STAMP0(0);
  {
    unsigned long long pc; unsigned len;
    asm volatile("s_getpc_b64 %0\n.Lswin_pf_start:\n\ts_mov_b32 %1, .Lswin_pf_end-.Lswin_pf_start" : "=s"(pc), "=s"(len));
    if (CODEPF_COND) {
      UNROLL for (int i = 0; i < 8; ++i) {
        unsigned o = (unsigned)lane * 64u + i * 4096u;
        o = o < len ? o : 0u;
        unsigned keep;
        asm volatile("s_mov_b32 %0, m0\n\ts_mov_b32 m0, %1\n\ts_nop 2\n\tglobal_load_lds_dword %2, %3\n\ts_mov_b32 m0, %0"
                     : "=&s"(keep) : "s"(lds0 + DUM_OFF + 1024), "v"(o), "s"(pc) : "memory");
      }
    }
  }
  const unsigned m1in = lane < 32 ? l16 + 1024u : ((l16 - 512u) ^ 16u) + RS_IN;
  const unsigned m1out = lane < 32 ? l16 + 1024u : ((l16 - 512u) ^ 16u) + RS_OUT;
#define POFF(i, lb, m1) ((i) == 0 ? (lb) : (i) == 1 ? (m1) : (i) == 2 ? ((lb) ^ 16u) : (i) == 3 ? ((lb) ^ 32u) : (i) == 4 ? ((m1) ^ 32u) : ((lb) ^ 48u))
#define PUNI(i, RS) ((i) == 0 ? 0u : (i) == 1 ? 0u : (i) == 2 ? (RS) + 512u : (i) == 3 ? 2u * (RS) : (i) == 4 ? 2u * (RS) : 3u * (RS) + 512u)
#define OPAQUE(v) asm volatile("" : "+v"(v))
#define PIN(v) asm volatile("" : "+v"(v))
#define PFULL(i, m1, RS) ((i) == 0 ? l16 : (i) == 1 ? (m1) : (i) == 2 ? xad<16>(l16, PUNI(2, RS)) : (i) == 3 ? xad<32>(l16, PUNI(3, RS)) : (i) == 4 ? xad<32>(m1, PUNI(4, RS)) : xad<48>(l16, PUNI(5, RS)))
  const __amdgpu_buffer_rsrc_t xrs = __builtin_amdgcn_make_buffer_rsrc((void*)x, 0, 38535168, 0x00020000);
  int win = bid + NBLK * grp;
  u32x4 xf[2][6];
  unsigned xb0;
  { int wy = win / 28, wx = win - wy * 28; xb0 = (unsigned)((wy * 8 + 2 * w) * 224 + wx * 8) * 192u; }
#define XLOAD(n) xf[(n) / 6][(n) % 6] = __builtin_amdgcn_raw_buffer_load_b128(xrs, POFF((n) % 6, l16, m1in), xb0 + ((n) / 6) * ROWB + PUNI((n) % 6, RS_IN), 2)

  if (threadIdx.x < 64) *(unsigned*)(smem + CNT_OFF + threadIdx.x * 4) = 0u;
#define CN(g, o) (lds0 + CNT_OFF + (g) * 64 + (o))
  const unsigned slot = SLOT_OFF + wave * SLOT_B;
  const unsigned cntA = CNT_OFF + grp * 64 + 32, cntB = cntA + 4;
  const float inv96 = 1.0f / 96.0f;

  const unsigned xa_e = slot + (unsigned)((t * 96 + 12 * p + (h ^ t)) * 16);
  const unsigned xa_o = slot + (unsigned)((t * 96 + 12 * p + ((2 + h) ^ t)) * 16);
  const unsigned t1adj = t < 2 ? 6144u : (unsigned)-3072;
  const unsigned sl16 = slot + l16;
#define XA(ks) ((((ks) & 1) ? xa_o : xa_e) + 64 * ((ks) >> 1))

  {
    constexpr unsigned QK = SLOT_OFF + 36864, VO = WF_OFF + 36864;
    const char* xb = x + xb0;
#define XFIRST()                                                                                                 \
  {                                                                                                              \
    UNROLL for (int i = 0; i < 6; ++i) dma16nt(lds0 + slot + i * 1024, xb + PUNI(i, RS_IN), POFF(i, l16, m1in)); \
    UNROLL for (int i = 0; i < 3; ++i) dma16nt(lds0 + slot + 6144 + i * 1024, xb + ROWB + PUNI(i, RS_IN), POFF(i, l16, m1in)); \
  }
    if (grp && w < 3) {
      unsigned qo[3], qf[3];
      UNROLL for (int m = 0; m < 3; ++m) {
        int idx = m * 64 + lane, row = idx / 24, qq = idx - row * 24;
        int c = qq ^ (row >> 1);
        qo[m] = (unsigned)(row * 576 + c * 16);
        qf[m] = (unsigned)(row * 576 + (c ^ 4) * 16);
      }
      UNROLL for (int i = 0; i < 4; ++i)
        UNROLL for (int m = 0; m < 3; ++m)
          dma16(lds0 + QK + (4 * w + i) * 3072 + m * 1024, wq + (4 * w + i) * 8 * 576, (i & 1) ? qf[m] : qo[m]);
    }
    if (wave == 0 && prod) flags_init(hflags, (unsigned)lane * 256u, cmask);
    if (wave == 3 && !prod) flag_st(hflag, 1u);
    wg_barrier();
    if (!grp && !prod) XFIRST();
    UNROLL for (int n = 9; n < 12; ++n) XLOAD(n);
    MEMB();
    if (wave == 3) {
      asm volatile("s_waitcnt vmcnt(12)" ::: "memory");
      grp_arrive(CN(0, 20));
    }
    const int pn = swap23(col);
    unsigned qb[4];
    UNROLL for (int e = 0; e < 4; ++e) qb[e] = QK + (unsigned)(pn * 384 + (((2 * e) | h) ^ ((pn >> 1) & 7)) * 16);
    UNROLL for (int e = 0; e < 4; ++e) OPAQUE(qb[e]);
    STAMP0(1);
#define QKF(rt, c2) lds_ld(smem, qb[(c2) & 3] + (rt) * 12288 + ((c2) >> 2) * 128)
#define QKR(ro, c2) lds_ld(smem, qb[(c2) & 3] + (ro) + ((c2) >> 2) * 128)
    if (wave == 7) {
      unsigned vv[3];
      UNROLL for (int m = 0; m < 3; ++m) {
        int idx = m * 64 + lane, row2 = idx / 12, q2 = idx - row2 * 12;
        vv[m] = (unsigned)(row2 * 576 + 384 + (q2 ^ (row2 >> 2)) * 16);
      }
      for (int i = 0; i < 6; ++i) {
        UNROLL for (int m = 0; m < 3; ++m) dma16(lds0 + VO + i * 3072 + m * 1024, wo + i * 3072 + m * 1024, l16);
        UNROLL for (int m = 0; m < 3; ++m) dma16(lds0 + VO + 18432 + i * 3072 + m * 1024, wq + i * 16 * 576, vv[m]);
      }
      wait_vm0();
      grp_arrive(CN(0, 4));
      if (!prod) {
        grp_wait(CN(0, 16), 6);
        XFIRST();
      }
    } else if (w < 3) {
      const unsigned cn = lds0 + CNT_OFF;
      if (grp) {
        asm volatile("s_waitcnt vmcnt(3)" ::: "memory");
        grp_arrive(cn + 0);
      }
      grp_wait(cn + 0, 3);
      if (grp) {
        __builtin_amdgcn_s_setprio(3);
        {
          u32x4 af[3][6], bb[6];
          UNROLL for (int kk = 0; kk < 6; ++kk) { bb[kk] = QKR(w * 12288, kk); af[0][kk] = QKF(0, 6 + kk); }
          UNROLL for (int ct = 1; ct < 3; ++ct)
            UNROLL for (int kk = 0; kk < 6; ++kk) af[ct][kk] = QKF(ct, 6 + kk);
          SB();
          f32x16 d = {};
          UNROLL for (int ct = 0; ct < 3; ++ct)
            UNROLL for (int kk = 0; kk < 6; ++kk) d = mfma32(af[ct][kk], bb[kk], d);
          float r0 = (d[0] + d[1]) + (d[2] + d[3]), r1 = (d[4] + d[5]) + (d[6] + d[7]);
          float r2_ = (d[8] + d[9]) + (d[10] + d[11]), r3 = (d[12] + d[13]) + (d[14] + d[15]);
          float rs = xhalf_add((r0 + r1) + (r2_ + r3));
          *(float*)(smem + RP_OFF + (32 * w + pn) * 4) = rs * -inv96;
          grp_arrive(cn + 8);
          grp_arrive(cn + 16);
        }
        __builtin_amdgcn_s_setprio(JB_PRIO);
        unsigned vb[2];
        UNROLL for (int e = 0; e < 2; ++e) vb[e] = VO + (unsigned)(18432 + pn * 192 + (((2 * e) | h) ^ ((pn >> 2) & 3)) * 16);
        const unsigned tb = VO + (unsigned)((8 * h + ((lane & 15) >> 2)) * 192 + (16 * ((lane >> 4) & 1) + 8 * (lane & 1) + 4 * ((lane >> 1) & 1)) * 2 + w * 64);
        if (!prod) {
          grp_wait(cn + 16, 6);
          XFIRST();
        }
        grp_wait(cn + 4, 1);
#define VF(rt, kk) lds_ld(smem, vb[(kk) & 1] + (rt) * 6144 + ((kk) >> 1) * 64)
        u32x4 bfr[6], av[3][6];
        UNROLL for (int kk = 0; kk < 6; ++kk) {
          s16x4 lo = __builtin_amdgcn_ds_read_tr16_b64_v4i16((LAS s16x4*)(smem + tb + kk * 3072));
          s16x4 hi = __builtin_amdgcn_ds_read_tr16_b64_v4i16((LAS s16x4*)(smem + tb + kk * 3072 + 768));
          u32x2 l2 = __builtin_bit_cast(u32x2, lo), h2 = __builtin_bit_cast(u32x2, hi);
          bfr[kk] = u32x4{l2[0], l2[1], h2[0], h2[1]};
          av[0][kk] = VF(0, kk);
        }
        UNROLL for (int rt = 1; rt < 3; ++rt)
          UNROLL for (int kk = 0; kk < 6; ++kk) av[rt][kk] = VF(rt, kk);
        SB();
        f32x16 DB[3];
        float c0 = 0.f, c1 = 0.f;
        UNROLL for (int rt = 0; rt < 3; ++rt) {
          f32x16 d = {};
          UNROLL for (int kk = 0; kk < 6; ++kk) d = mfma32(av[rt][kk], bfr[kk], d);
          DB[rt] = d;
          if (rt == 0) grp_arrive(cn + 12);
          if (rt > 0) { UNROLL for (int i = 0; i < 16; i += 2) { c0 += DB[rt - 1][i]; c1 += DB[rt - 1][i + 1]; } }
          SB();
        }
        UNROLL for (int i = 0; i < 16; i += 2) { c0 += DB[2][i]; c1 += DB[2][i + 1]; }
        const float cm = xhalf_add(c0 + c1) * inv96;
        UNROLL for (int rt = 0; rt < 3; ++rt) {
          UNROLL for (int i = 0; i < 16; ++i) DB[rt][i] -= cm;
          lds_st(smem, WF_OFF + 18432 + (w * 6 + 2 * rt) * 1024 + l16, PACKS(DB[rt], 0));
          lds_st(smem, WF_OFF + 18432 + (w * 6 + 2 * rt + 1) * 1024 + l16, PACKS(DB[rt], 1));
        }
        grp_arrive(cn + 24);
#undef VF
        __builtin_amdgcn_s_setprio(0);
      } else {
        __builtin_amdgcn_s_setprio(2);
        f32x16 DA[3];
        float a0 = 0.f, a1 = 0.f;
        {
          u32x4 bb[6], af[3][6];
          UNROLL for (int kk = 0; kk < 6; ++kk) { bb[kk] = QKR(w * 12288, 6 + kk); af[0][kk] = QKF(0, kk); }
          UNROLL for (int rt = 1; rt < 3; ++rt)
            UNROLL for (int kk = 0; kk < 6; ++kk) af[rt][kk] = QKF(rt, kk);
          SB();
          UNROLL for (int rt = 0; rt < 3; ++rt) {
            f32x16 d = {};
            UNROLL for (int kk = 0; kk < 6; ++kk) d = mfma32(af[rt][kk], bb[kk], d);
            DA[rt] = d;
            if (rt == 0) grp_arrive(cn + 16);
            if (rt > 0) { UNROLL for (int i = 0; i < 16; i += 2) { a0 += DA[rt - 1][i]; a1 += DA[rt - 1][i + 1]; } }
            SB();
          }
          UNROLL for (int i = 0; i < 16; i += 2) { a0 += DA[2][i]; a1 += DA[2][i + 1]; }
        }
        grp_wait(cn + 8, 3);
        f32x4 rm[12];
        UNROLL for (int n = 0; n < 12; ++n) rm[n] = *(const f32x4*)(smem + RP_OFF + (32 * (n >> 2) + 16 * ((n >> 1) & 1) + 8 * h + 4 * (n & 1)) * 4);
        UNROLL for (int n = 0; n < 12; ++n) {
          a0 += rm[n][0] + rm[n][2]; a1 += rm[n][1] + rm[n][3];
          UNROLL for (int i = 0; i < 4; ++i) DA[n >> 2][4 * (n & 3) + i] += rm[n][i];
        }
        const float fA = 0.10206207261596575f * 1.4426950408889634f;
        const float cadd = xhalf_add(a0 + a1) * (-fA * inv96);
        UNROLL for (int rt = 0; rt < 3; ++rt) {
          UNROLL for (int i = 0; i < 16; ++i) DA[rt][i] = __builtin_fmaf(DA[rt][i], fA, cadd);
          lds_st(smem, WF_OFF + (w * 6 + 2 * rt) * 1024 + l16, PACKS(DA[rt], 0));
          lds_st(smem, WF_OFF + (w * 6 + 2 * rt + 1) * 1024 + l16, PACKS(DA[rt], 1));
        }
        grp_arrive(cn + 20);
      }
    }
#undef QKF
#undef QKR
#undef XFIRST
    STAMP0(2);
    if (prod) {
      grp_wait(CN(0, 20), 4);
      grp_wait(CN(0, 24), 3);
      const __amdgpu_buffer_rsrc_t hrs = __builtin_amdgcn_make_buffer_rsrc(hdata, 0, 36864, 0x00020000);
      UNROLL for (int i = 0; i < 5; ++i) {
        const unsigned o = (unsigned)(wave + 8 * i) * 1024u + l16;
        __builtin_amdgcn_raw_buffer_store_b128(lds_ld(smem, WF_OFF + o), hrs, o, 0, 16);
      }
      wait_vm0();
      wg_barrier();
      if (wave == 0) flags_publish(hflags, (unsigned)lane * 256u, cmask);
      return;
    }
  }
  wait_vm0();
  UNROLL for (int ks = 0; ks < 6; ++ks) xf[0][ks] = lds_ld(smem, XA(ks));
  MEMB();
  UNROLL for (int i = 0; i < 3; ++i) lds_st(smem, sl16 + i * 1024, xf[1][3 + i]);
  MEMB();
  UNROLL for (int ks = 0; ks < 6; ++ks) xf[1][ks] = lds_ld(smem, t1adj + XA(ks));
  MEMB();

  const bool own = (t == w);
  const unsigned exo = (unsigned)(h * 384 + p * 48 + ((t ^ w) - 1) * 16);
  const unsigned ex_w = own ? (unsigned)(DUM_OFF + h * 128 + ((3 * (p & 6) + 6 + (p & 1)) & 7) * 16)
                            : SLOT_OFF + (grp * 4 + t) * SLOT_B + exo;
  const unsigned ex_r = slot + exo;
  const unsigned long long notown = ~(0x1111111111111111ull << w);
  const unsigned rbase = R_OFF + wave * 256;
  unsigned wS = l16 + 36864u;
  asm volatile("" : "+v"(wS));
#define WLD(i) lds_ld(smem, ((i) < 36 ? l16 + (i) * 1024 : wS + ((i) - 36) * 1024))

  const unsigned ONES = 0x3F803F80u;
  const unsigned id0 = t == 0 ? 0x00003F80u : (t == 1 ? 0x3F800000u : 0u);
  const unsigned id1 = t == 2 ? 0x00003F80u : (t == 3 ? 0x3F800000u : 0u);
  unsigned msk[4];
  UNROLL for (int g = 0; g < 4; ++g) msk[g] = ((col >> 3) == g && h == (p & 1)) ? 0xffffffffu : 0u;
  const f32x4 z4 = {0.f, 0.f, 0.f, 0.f};
  const f32x16 zz = {};

  grp_wait(CN(0, 20), 4);

  u32x4 wb[4];
  wb[2] = WLD(0); wb[3] = WLD(1); wb[0] = WLD(2);
  u32x4 x1[2][6];
  UNROLL for (int a = 0; a < 2; ++a) UNROLL for (int i = 0; i < 6; ++i) x1[a][i] = wb[0];
  unsigned iter = 1;

#define OSTORE(n_) __builtin_amdgcn_raw_buffer_store_b128(x1[(n_) / 6][(n_) % 6], (n_) < 6 ? ors0 : ors1, PFULL((n_) % 6, m1out, RS_OUT), 0, 2)
#define LNFIN1(g0, g1, q0, q1)                                                                                    \
  {                                                                                                              \
    PIN(g0); PIN(g1);                                                                                            \
    float q0a = (t & 1) ? g0[1] : g0[0], q0b = (t & 1) ? g0[3] : g0[2];                                          \
    float q1a = (t & 1) ? g1[1] : g1[0], q1b = (t & 1) ? g1[3] : g1[2];                                          \
    q0 = (t & 2) ? q0b : q0a; q1 = (t & 2) ? q1b : q1a;                                                          \
  }
#define LNFIN2(sm0, sm1, q0, q1, r)                                                                              \
  {                                                                                                              \
    PIN(q0);                                                                                                     \
    float mean = merge_add(sm0[0], sm1[0]) * inv96;                                                              \
    float var = merge_add(q0, q1) * inv96 - mean * mean;                                                         \
    var = __builtin_fmaxf(var, 0.f);                                                                             \
    r = __builtin_amdgcn_rsqf(var + 1e-5f);                                                                      \
  }
#define T1_FILL(g)                                                                                               \
  {                                                                                                              \
    if ((g) < 3) { MEMB(); x1[0][2 * (g)] = lds_ld(smem, sl16 + (2 * (g)) * 1024); x1[0][2 * (g) + 1] = lds_ld(smem, sl16 + (2 * (g) + 1) * 1024); MEMB(); } \
    if ((g) >= 3 && (g) < 9) lds_st(smem, XA((g) - 3), x1[1][(g) >= 3 && (g) < 9 ? (g) - 3 : 0]);                \
    if ((g) >= 9 && (g) < 12) { const int k_ = (g) >= 9 && (g) < 12 ? 2 * ((g) - 9) : 0; MEMB(); x1[1][k_] = lds_ld(smem, sl16 + k_ * 1024); x1[1][k_ + 1] = lds_ld(smem, sl16 + (k_ + 1) * 1024); MEMB(); } \
    if ((g) == 10) LNFIN1(g0, g1, q0, q1);                                                                       \
    if ((g) == 12) LNFIN2(sm0, sm1, q0, q1, rr);                                                                 \
    if ((g) >= 13 && ((g) & 1)) OSTORE(((g) - 13) / 2);                                                          \
    if (((g) >= 14 && (g) <= 20 && !((g) & 1)) || ((g) >= 26 && (g) <= 32 && !((g) & 1))) {                      \
      const int tp_ = (g) >= 26 ? 1 : 0, k_ = (((g) - 14) % 12) / 2, a_ = (k_ >> 1) & 1, s_ = k_ & 1;           \
      PIN(acc[tp_][a_]);                                                                                         \
      uf[a_][2 * tp_ + s_] = PACKS(acc[tp_][a_], s_);                                                            \
    }                                                                                                            \
    if ((g) == 22) grp_arrive(lds0 + cntB);                                                                      \
    if ((g) == 24) { MEMB(); b1v = *(const unsigned*)(smem + CNT_OFF + 24); }                                    \
    if ((g) == 28) grp_wait_peeked(CN(0, 24), 3, b1v);                                                           \
  }
#define T1_STAGE()                                                                                               \
  {                                                                                                              \
    f32x4 sm0 = z4, sm1 = z4, g0 = z4, g1 = z4;                                                                  \
    unsigned b1v;                                                                                                \
    float q0, q1;                                                                                                \
    UNROLL for (int ks = 0; ks < 6; ++ks) {                                                                      \
      lds_st(smem, XA(ks), x1[0][ks]);                                                                           \
      UNROLL for (int c = 0; c < 2; ++c) {                                                                       \
        sm0 = mfma4(ONES, ONES, xf[0][ks][2 * c], xf[0][ks][2 * c + 1], sm0);                                    \
        sm1 = mfma4(ONES, ONES, xf[1][ks][2 * c], xf[1][ks][2 * c + 1], sm1);                                    \
        g0 = mfma4(xf[0][ks][2 * c], xf[0][ks][2 * c + 1], xf[0][ks][2 * c], xf[0][ks][2 * c + 1], g0);          \
        g1 = mfma4(xf[1][ks][2 * c], xf[1][ks][2 * c + 1], xf[1][ks][2 * c], xf[1][ks][2 * c + 1], g1);          \
      }                                                                                                          \
      SB();                                                                                                      \
    }                                                                                                            \
    UNROLL for (int i = 0; i < 18; ++i) {                                                                        \
      const int tt = i / 6, ks = i % 6;                                                                          \
      acc[tt & 1][0] = mfma32(wb[(i + 2) & 3], xf[0][ks], ks == 0 ? zz : acc[tt & 1][0]);                        \
      SB();                                                                                                      \
      wb[(i + 5) & 3] = WLD(i + 3);                                                                              \
      T1_FILL(2 * i);                                                                                            \
      SB();                                                                                                      \
      acc[tt & 1][1] = mfma32(wb[(i + 2) & 3], xf[1][ks], ks == 0 ? zz : acc[tt & 1][1]);                        \
      SB();                                                                                                      \
      T1_FILL(2 * i + 1);                                                                                        \
      SB();                                                                                                      \
    }                                                                                                            \
  }

  unsigned rem = (unsigned)((784 - win + 2 * NBLK - 1) / (2 * NBLK)) * 90u + (unsigned)grp;
  unsigned prem = 0;
#define STAGE(BASE, WGT)                                                                                         \
  {                                                                                                              \
    set_prio_rem<BASE>(rem, prem);                                                                               \
    MEMB();                                                                                                      \
    *(unsigned*)(smem + CNT_OFF + 128 + wave * 4) = rem;                                                                \
    prem = *(const unsigned*)(smem + CNT_OFF + 128 + (wave ^ 4) * 4);                                                   \
    MEMB();                                                                                                      \
    rem -= (WGT);                                                                                                \
  }
  unsigned ob = 0;
  PRIO_PURE();
  for (;;) {
    STAMPL(3);
    float rr;
    u32x4 uf[2][6];
    f32x16 acc[2][2];
    STAGE(PB_PURE, 16);
    const __amdgpu_buffer_rsrc_t ors0 = __builtin_amdgcn_make_buffer_rsrc(out + (size_t)ob, 0, iter > 1 ? 0x41000 : 0, 0x00020000);
    const __amdgpu_buffer_rsrc_t ors1 = __builtin_amdgcn_make_buffer_rsrc(out + (size_t)(ob + ROWB), 0, iter > 1 ? 0x41000 : 0, 0x00020000);
    T1_STAGE();
    STAMPL(4);
    STAGE(T2BASE, 20);
    u32x4 pt[2][2];
    unsigned a2v, fpk;
    const unsigned fon = (unsigned)__builtin_amdgcn_readfirstlane(!grp && iter == 1);
    const __amdgpu_buffer_rsrc_t frs = __builtin_amdgcn_make_buffer_rsrc(hflag, 0, fon * 4, 0x00020000);
    const int nwin = win + 2 * NBLK;
    const bool more = nwin < 784;
    const char* xb;
    unsigned pfon;
#define NEXTADDR()                                                                                               \
  {                                                                                                              \
    int nw = more ? nwin : win; int wy = nw / 28, wx = nw - wy * 28;                                             \
    xb = x + (unsigned)((wy * 8 + 2 * w) * 224 + wx * 8) * 192u;                                                 \
    pfon = (unsigned)__builtin_amdgcn_readfirstlane(more ? 0x7fffffff : 0);                                      \
    asm volatile("" : "+s"(xb), "+s"(pfon));                                                                     \
  }
    {
      f32x4 s0, s1, s0b, s1b;
      f32x16 vac[2][2];
      f32x16 oac[2];
      u32x4 vpk[2][2];
      float rs0, rs1, rs2, rs3, y0, y1, y2, y3, e0, e1, e2, e3;
      unsigned cbv;
      unsigned pa0, pa1, pb0, pb1;
      float sinv;
#define O_INIT(tt)                                                                                               \
  UNROLL for (int a = 0; a < 2; ++a) {                                                                           \
    f32x16 o;                                                                                                    \
    UNROLL for (int g = 0; g < 4; ++g) {                                                                         \
      f32x4 r4 = mfma4(id0, id1, xf[a][2 * (tt) + (g >> 1)][2 * (g & 1)], xf[a][2 * (tt) + (g >> 1)][2 * (g & 1) + 1], z4); \
      UNROLL for (int e = 0; e < 4; ++e) o[4 * g + e] = r4[e];                                                   \
    }                                                                                                            \
    oac[a] = o;                                                                                                  \
  }
#define O_STEP(tt)                                                                                               \
  O_INIT(tt);                                                                                                    \
  SB();                                                                                                          \
  oac[0] = mfma32(vpk[0][0], pt[0][0], oac[0]);                                                                  \
  oac[0] = mfma32(vpk[0][1], pt[0][1], oac[0]);                                                                  \
  oac[1] = mfma32(vpk[1][0], pt[1][0], oac[1]);                                                                  \
  oac[1] = mfma32(vpk[1][1], pt[1][1], oac[1]);
#define S2_STEP(tt, a, s)                                                                                        \
  PIN(oac[a]);                                                                                                   \
  x1[a][2 * (tt) + (s)] = PACKS(oac[a], s);                                                                      \
  lds_st(smem, ex_w + ((a) * 6 + 2 * (tt) + (s)) * 768, x1[a][2 * (tt) + (s)]);
#define VP_STEP(tt, k) { PIN(vac[(tt) & 1][(k) >> 1]); vpk[(k) >> 1][(k) & 1] = PACKS(vac[(tt) & 1][(k) >> 1], (k) & 1); }
#define T2_FILL(g)                                                                                               \
  {                                                                                                              \
    if ((g) >= 1 && (g) <= 4) { const int k_ = (g) >= 1 && (g) <= 4 ? (g) - 1 : 0; PIN(acc[0][k_ >> 1]); uf[k_ >> 1][4 + (k_ & 1)] = PACKS(acc[0][k_ >> 1], k_ & 1); } \
    if ((g) == 5) {                                                                                              \
      s0 = z4; s1 = z4; s0b = z4; s1b = z4;                                                                      \
      UNROLL for (int k2 = 0; k2 < 6; ++k2) {                                                                    \
        s0 = mfma4(xf[0][k2][0], xf[0][k2][1], uf[0][k2][0], uf[0][k2][1], s0);                                  \
        s1 = mfma4(xf[1][k2][0], xf[1][k2][1], uf[1][k2][0], uf[1][k2][1], s1);                                  \
        s0b = mfma4(xf[0][k2][2], xf[0][k2][3], uf[0][k2][2], uf[0][k2][3], s0b);                                \
        s1b = mfma4(xf[1][k2][2], xf[1][k2][3], uf[1][k2][2], uf[1][k2][3], s1b);                                \
      }                                                                                                          \
      rs0 = quad_bcast<0>(rr); rs1 = quad_bcast<1>(rr); rs2 = quad_bcast<2>(rr); rs3 = quad_bcast<3>(rr);        \
    }                                                                                                            \
    if ((g) == 6) { PIN(s0); PIN(s1); UNROLL for (int e = 0; e < 4; ++e) { s0[e] += s0b[e]; } s1[0] += s1b[0]; s1[1] += s1b[1]; } \
    if ((g) == 7) { PIN(s1); s1[2] += s1b[2]; s1[3] += s1b[3]; y0 = merge_add(s0[0], s1[0]) * rs0; }             \
    if ((g) == 8) { PIN(s0); y1 = merge_add(s0[1], s1[1]) * rs1; y2 = merge_add(s0[2], s1[2]); }                 \
    if ((g) == 9) { PIN(s0); y2 *= rs2; y3 = merge_add(s0[3], s1[3]) * rs3; }                                    \
    if ((g) == 10) {                                                                                             \
      PIN(y3);                                                                                                   \
      float mx = __builtin_fmaxf(__builtin_fmaxf(y0, y1), __builtin_fmaxf(y2, y3));                              \
      float cc = -rr * mx;                                                                                       \
      y0 = __builtin_fmaf(y0, rr, cc); y1 = __builtin_fmaf(y1, rr, cc);                                          \
      y2 = __builtin_fmaf(y2, rr, cc); y3 = __builtin_fmaf(y3, rr, cc);                                          \
    }                                                                                                            \
    if ((g) == 11) { PIN(y0); e0 = __builtin_amdgcn_exp2f(y0); e1 = __builtin_amdgcn_exp2f(y1); e2 = __builtin_amdgcn_exp2f(y2); } \
    if ((g) == 12) { PIN(y3); e3 = __builtin_amdgcn_exp2f(y3); sinv = __builtin_amdgcn_rcpf((e0 + e1) + (e2 + e3)); } \
    if ((g) == 13) { PIN(e0); e0 *= rs0; e1 *= rs1; e2 *= rs2; e3 *= rs3; e0 *= sinv; e1 *= sinv; }              \
    if ((g) == 14) {                                                                                             \
      PIN(e2);                                                                                                   \
      unsigned p01 = pk(e0, e1), p23 = pk(e2 * sinv, e3 * sinv);                                                 \
      auto a_ = __builtin_amdgcn_permlane32_swap(p01, p01, false, false);                                        \
      auto b_ = __builtin_amdgcn_permlane32_swap(p23, p23, false, false);                                        \
      pa0 = a_[0]; pa1 = a_[1]; pb0 = b_[0]; pb1 = b_[1];                                                        \
    }                                                                                                            \
    if ((g) == 15) { PIN(pa0); pt[0][0] = u32x4{pa0 & msk[0], pb0 & msk[0], pa0 & msk[1], pb0 & msk[1]}; { MEMB(); cbv = *(const unsigned*)(smem + cntB); } } \
    if ((g) == 16) { PIN(pa0); pt[0][1] = u32x4{pa0 & msk[2], pb0 & msk[2], pa0 & msk[3], pb0 & msk[3]}; }       \
    if ((g) == 17) { PIN(pa1); pt[1][0] = u32x4{pa1 & msk[0], pb1 & msk[0], pa1 & msk[1], pb1 & msk[1]}; }       \
    if ((g) == 18) { PIN(pa1); pt[1][1] = u32x4{pa1 & msk[2], pb1 & msk[2], pa1 & msk[3], pb1 & msk[3]}; }       \
    if ((g) >= 19 && (g) <= 22) VP_STEP(0, (g) >= 19 && (g) <= 22 ? (g) - 19 : 0);                               \
    if ((g) == 23) { grp_wait_peeked(lds0 + cntB, 4 * iter, cbv); O_STEP(0); }                                   \
    if ((g) >= 25 && (g) <= 28) { const int k_ = (g) >= 25 && (g) <= 28 ? (g) - 25 : 0; S2_STEP(0, k_ >> 1, k_ & 1); } \
    if ((g) >= 29 && (g) <= 32) VP_STEP(1, (g) >= 29 && (g) <= 32 ? (g) - 29 : 0);                               \
    if ((g) == FPK_GAP) fpk = __builtin_amdgcn_raw_buffer_load_b32(frs, 0, 0, 16);                               \
    if ((g) == 24) fetch_s(fon, fpk, hflag, hdata + w * 9216, lds0 + WF_OFF + 36864 + w * 9216, l16);            \
    if ((g) == 30) { MEMB(); a2v = *(const unsigned*)(smem + CNT_OFF + 64 + 20); }                               \
    if ((g) == 33) { O_STEP(1); }                                                                                \
    if ((g) == 35) { S2_STEP(1, 0, 0); S2_STEP(1, 0, 1); }                                                       \
  }
      UNROLL for (int j = 0; j < 18; ++j) {
        const int tt = j / 6, ks = j % 6;
        vac[tt & 1][0] = mfma32(xf[0][ks], wb[(j + 20) & 3], ks == 0 ? zz : vac[tt & 1][0]);
        SB();
        if (j < 15) wb[(j + 23) & 3] = WLD(j + 18 + 3);
        T2_FILL(2 * j);
        SB();
        vac[tt & 1][1] = mfma32(xf[1][ks], wb[(j + 20) & 3], ks == 0 ? zz : vac[tt & 1][1]);
        SB();
        T2_FILL(2 * j + 1);
        SB();
      }
      S2_STEP(1, 1, 0); S2_STEP(1, 1, 1);
      SB();
      O_INIT(2);
      VP_STEP(2, 0); VP_STEP(2, 1);
      SB();
      oac[0] = mfma32(vpk[0][0], pt[0][0], oac[0]);
      oac[0] = mfma32(vpk[0][1], pt[0][1], oac[0]);
      SB();
      VP_STEP(2, 2); VP_STEP(2, 3);
      SB();
      oac[1] = mfma32(vpk[1][0], pt[1][0], oac[1]);
      oac[1] = mfma32(vpk[1][1], pt[1][1], oac[1]);
      SB();
      NEXTADDR();
      S2_STEP(2, 0, 0); S2_STEP(2, 0, 1);
      SB();
      S2_STEP(2, 1, 0); S2_STEP(2, 1, 1);
#undef O_INIT
#undef VP_STEP
#undef T2_FILL
#undef O_STEP
#undef S2_STEP
    }
    STAMPL(5);
    grp_arrive(lds0 + cntA);
    drain_arrive(fon, CN(1, 20));
    grp_wait_peeked(CN(1, 20), 4, a2v);
    wb[0] = WLD(36); wb[1] = WLD(37); wb[2] = WLD(38);
    grp_wait(lds0 + cntA, 4 * iter);
    asm volatile("s_mov_b64 exec, %13\n\t"
                 "ds_read_b128 %0, %12\n\tds_read_b128 %1, %12 offset:768\n\tds_read_b128 %2, %12 offset:1536\n\t"
                 "ds_read_b128 %3, %12 offset:2304\n\tds_read_b128 %4, %12 offset:3072\n\tds_read_b128 %5, %12 offset:3840\n\t"
                 "ds_read_b128 %6, %12 offset:4608\n\tds_read_b128 %7, %12 offset:5376\n\tds_read_b128 %8, %12 offset:6144\n\t"
                 "ds_read_b128 %9, %12 offset:6912\n\tds_read_b128 %10, %12 offset:7680\n\tds_read_b128 %11, %12 offset:8448\n\t"
                 "s_mov_b64 exec, -1\n\ts_waitcnt lgkmcnt(0)"
                 : "+v"(x1[0][0]), "+v"(x1[0][1]), "+v"(x1[0][2]), "+v"(x1[0][3]), "+v"(x1[0][4]), "+v"(x1[0][5]),
                   "+v"(x1[1][0]), "+v"(x1[1][1]), "+v"(x1[1][2]), "+v"(x1[1][3]), "+v"(x1[1][4]), "+v"(x1[1][5])
                 : "v"(lds0 + ex_r), "s"(notown) : "memory");
    float rq[2];
    const unsigned rka = rbase + h * 16;
#define RKLD(G) (*(const f32x4*)(smem + rka + (G) * 32))
    f32x4 rkb[3];
    STAMPL(6);
    STAGE(PB_PURE, 14);
    const __amdgpu_buffer_rsrc_t nrs = __builtin_amdgcn_make_buffer_rsrc((void*)(xb + ROWB), 0, pfon, 0x00020000);
    u32x4 pre[3];
    SB();
#define SW(k) ((k) < 18 ? 36 + (k) : ((k) < 54 ? 18 + (k) : (k) - 54))
    u32x4 pf[2][4];
    u32x4 vp[3][4];
    f32x16 sa[2][2];
    u32x4 uq[6];
    f32x16 ua[2];
    float mxv, ccv, l0, l1, invv;
#define U_STEP(q, i)                                                                                             \
  {                                                                                                              \
    const int tt_ = (i) / 6, ks_ = (i) % 6, k_ = 18 * (q) + (i);                                                 \
    ua[tt_ & 1] = mfma32(wb[k_ & 3], x1[q][ks_], ks_ == 0 ? zz : ua[tt_ & 1]);                                   \
    SB();                                                                                                        \
    wb[(k_ + 3) & 3] = WLD(SW(k_ + 3));                                                                          \
    if (tt_ > 0 && (ks_ == 2 || ks_ == 3)) { PIN(ua[(tt_ + 1) & 1]); uq[2 * (tt_ - 1) + (ks_ - 2)] = PACKS(ua[(tt_ + 1) & 1], ks_ - 2); } \
  }
#define S_STEP(q, ks_)                                                                                           \
  {                                                                                                              \
    sa[q][0] = mfma32(x1[0][ks_], uq[ks_], (ks_) == 0 ? zz : sa[q][0]);                                          \
    SB();                                                                                                        \
    if ((ks_) == 1) { PIN(ua[0]); uq[4] = PACKS(ua[0], 0); }                                                     \
    if ((ks_) == 2) { PIN(ua[0]); uq[5] = PACKS(ua[0], 1); }                                                     \
    SFILL;                                                                                                       \
    SB();                                                                                                        \
    sa[q][1] = mfma32(x1[1][ks_], uq[ks_], (ks_) == 0 ? zz : sa[q][1]);                                          \
    SB();                                                                                                        \
  }
#define SFILL
#define P1(q, G)                                                                                                 \
  {                                                                                                              \
    const int n_ = 16 * (q) + (G);                                                                               \
    MEMB(); rkb[(n_ + 2) % 3] = RKLD(((G) + 2) & 7);                                                             \
    UNROLL for (int e = 0; e < 4; ++e) sa[q][(G) >> 2][4 * ((G) & 3) + e] *= rkb[n_ % 3][e];                     \
    mxv = (G) == 0 ? sa[q][0][0] : __builtin_fmaxf(mxv, sa[q][(G) >> 2][4 * ((G) & 3)]);                         \
    mxv = __builtin_fmaxf(mxv, sa[q][(G) >> 2][4 * ((G) & 3) + 1]);                                              \
    mxv = __builtin_fmaxf(__builtin_fmaxf(mxv, sa[q][(G) >> 2][4 * ((G) & 3) + 2]), sa[q][(G) >> 2][4 * ((G) & 3) + 3]); \
  }
#define X1(q) { mxv = xhalf_max(mxv); ccv = -rq[q] * mxv; l0 = 0.f; l1 = 0.f; }
#define P2H(q, H)                                                                                                \
  {                                                                                                              \
    const int b_ = (H) >> 3, i_ = 2 * ((H) & 7), G_ = (H) >> 1, n_ = 16 * (q) + 8 + G_;                          \
    if (((H) & 1) == 0 && n_ < 30) { MEMB(); rkb[(n_ + 2) % 3] = RKLD((G_ + 2) & 7); }                           \
    const float ea = __builtin_amdgcn_exp2f(__builtin_fmaf(sa[q][b_][i_], rq[q], ccv));                          \
    const float eb = __builtin_amdgcn_exp2f(__builtin_fmaf(sa[q][b_][i_ + 1], rq[q], ccv));                      \
    l0 += ea; l1 += eb;                                                                                          \
    sa[q][b_][i_] = ea * rkb[n_ % 3][2 * ((H) & 1)]; sa[q][b_][i_ + 1] = eb * rkb[n_ % 3][2 * ((H) & 1) + 1];    \
  }
#define X2(q) { invv = __builtin_amdgcn_rcpf(xhalf_add(l0 + l1)); }
#define P3(q, F)                                                                                                 \
  {                                                                                                              \
    UNROLL for (int e = 0; e < 8; ++e) sa[q][(F) >> 1][8 * ((F) & 1) + e] *= invv;                               \
    pf[q][F] = PACKS(sa[q][(F) >> 1], (F) & 1);                                                                  \
  }
    {
      f32x4 sm0 = z4, sm1 = z4, g0 = z4, g1 = z4;
      UNROLL for (int ks = 0; ks < 6; ++ks) {
        UNROLL for (int c = 0; c < 2; ++c) {
          sm0 = mfma4(ONES, ONES, x1[0][ks][2 * c], x1[0][ks][2 * c + 1], sm0);
          sm1 = mfma4(ONES, ONES, x1[1][ks][2 * c], x1[1][ks][2 * c + 1], sm1);
          g0 = mfma4(x1[0][ks][2 * c], x1[0][ks][2 * c + 1], x1[0][ks][2 * c], x1[0][ks][2 * c + 1], g0);
          g1 = mfma4(x1[1][ks][2 * c], x1[1][ks][2 * c + 1], x1[1][ks][2 * c], x1[1][ks][2 * c + 1], g1);
        }
        SB();
      }
      float q0, q1;
      UNROLL for (int i = 0; i < 18; ++i) {
        U_STEP(0, i);
        if (i == 1) LNFIN1(g0, g1, q0, q1);
        if (i == 2) {
          float r2;
          LNFIN2(sm0, sm1, q0, q1, r2);
          *(float*)(smem + rbase + lane * 4) = r2;
          auto rb = __builtin_amdgcn_permlane32_swap(ubits(r2), ubits(r2), false, false);
          rq[0] = fbits(rb[0]); rq[1] = fbits(rb[1]);
          MEMB();
        }
        {
          const int n = i < 8 ? i - 3 : (i < 10 ? -1 : (i < 14 ? i - 5 : -1));
          if (n >= 0 && n < 6) dma1ntc(lds0 + slot + n * 1024, xb, PFULL(n >= 0 && n < 6 ? n : 0, m1in, RS_IN), pfon);
          if (n >= 6) dma1ntc(lds0 + slot + n * 1024, xb + ROWB, PFULL(n >= 6 ? n - 6 : 0, m1in, RS_IN), pfon);
        }
        if (i >= 16) pre[i - 16] = __builtin_amdgcn_raw_buffer_load_b128(nrs, PFULL(i >= 16 ? i - 13 : 0, m1in, RS_IN), 0, 2);
        SB();
      }
    }
    UNROLL for (int ks = 0; ks < 6; ++ks) {
      S_STEP(0, ks);
      if (ks == 3) pre[2] = __builtin_amdgcn_raw_buffer_load_b128(nrs, PFULL(5, m1in, RS_IN), 0, 2);
      if (ks == 4) { rkb[0] = RKLD(0); rkb[1] = RKLD(1); }
      SB();
    }
    STAMPL(7);
    PRIO_DENSE();
    STAGE(PB_DENSE, 30);
    UNROLL for (int i = 0; i < 18; ++i) {
      U_STEP(1, i);
      if (i >= 1 && i <= 4) { P1(0, 2 * (i - 1)); P1(0, 2 * (i - 1) + 1); }
      if (i == 5) X1(0);
      if (i >= 6) P2H(0, i - 6);
      SB();
    }
#undef SFILL
#define SFILL { if (ks == 0) P2H(0, 12); if (ks == 1) P2H(0, 14); if (ks == 3) P3(0, 0); if (ks == 4) P3(0, 2); }
    UNROLL for (int ks = 0; ks < 6; ++ks) {
      S_STEP(1, ks);
      if (ks == 0) P2H(0, 13);
      if (ks == 1) P2H(0, 15);
      if (ks == 2) X2(0);
      if (ks == 3) P3(0, 1);
      if (ks == 4) P3(0, 3);
      SB();
    }
    STAMPL(8);
    {
      f32x16 v0, v1;
#define R2_FILL(g)                                                                                               \
  {                                                                                                              \
    if ((g) >= 1 && (g) <= 8) P1(1, (g) >= 1 && (g) <= 8 ? (g) - 1 : 0);                                         \
    if ((g) == 9) X1(1);                                                                                         \
    { const int H_ = (g) == 10 ? 0 : ((g) >= 14 && (g) <= 22 ? (g) - 13 : ((g) >= 26 && (g) <= 31 ? (g) - 16 : -1)); \
      if (H_ >= 0) P2H(1, H_ >= 0 ? H_ : 0); }                                                                   \
    if ((g) == 32) X2(1);                                                                                        \
    if ((g) == 33) P3(1, 0);                                                                                     \
    if ((g) == 34) P3(1, 1);                                                                                     \
    if ((g) == 11 || (g) == 23 || (g) == 35) { PIN(v0); vp[(g) / 12][0] = PACKS(v0, 0); vp[(g) / 12][1] = PACKS(v0, 1); } \
    if ((g) == 12 || (g) == 24) { PIN(v1); vp[(g) / 12 - 1][2] = PACKS(v1, 0); vp[(g) / 12 - 1][3] = PACKS(v1, 1); } \
  }
      UNROLL for (int j = 0; j < 18; ++j) {
        const int ks = j % 6, k = 36 + j;
        v0 = mfma32(x1[0][ks], wb[k & 3], ks == 0 ? zz : v0);
        SB();
        wb[(k + 3) & 3] = WLD(SW(k + 3));
        R2_FILL(2 * j);
        SB();
        v1 = mfma32(x1[1][ks], wb[k & 3], ks == 0 ? zz : v1);
        SB();
        R2_FILL(2 * j + 1);
        SB();
      }
#undef R2_FILL
      STAMPL(9);
      PRIO_PURE();
      STAGE(PB_PURE, 10);
      f32x16 oc[2];
      UNROLL for (int n = 0; n < 6; ++n) {
        const int q = n / 3, tt = n % 3;
        f32x16 o;
        UNROLL for (int g = 0; g < 4; ++g) {
          f32x4 r4 = mfma4(id0, id1, x1[q][2 * tt + (g >> 1)][2 * (g & 1)], x1[q][2 * tt + (g >> 1)][2 * (g & 1) + 1], z4);
          UNROLL for (int e = 0; e < 4; ++e) o[4 * g + e] = r4[e];
        }
        SB();
        if (n == 0) { PIN(v1); vp[2][2] = PACKS(v1, 0); }
        o = mfma32(vp[tt][0], pf[q][0], o);
        SB();
        if (n == 0) { PIN(v1); vp[2][3] = PACKS(v1, 1); }
        if (n == 1) P3(1, 2);
        if (n == 2) P3(1, 3);
        SB();
        o = mfma32(vp[tt][1], pf[q][1], o);
        SB();
        if (n > 0) { PIN(oc[(n + 1) & 1]); x1[(n - 1) / 3][2 * ((n - 1) % 3)] = PACKS(oc[(n + 1) & 1], 0); }
        SB();
        o = mfma32(vp[tt][2], pf[q][2], o);
        SB();
        if (n > 0) { PIN(oc[(n + 1) & 1]); x1[(n - 1) / 3][2 * ((n - 1) % 3) + 1] = PACKS(oc[(n + 1) & 1], 1); }
        SB();
        o = mfma32(vp[tt][3], pf[q][3], o);
        oc[n & 1] = o;
        SB();
      }
      STAMPL(10);
      wait_vm0();
      UNROLL for (int ks = 0; ks < 6; ++ks) xf[0][ks] = lds_ld(smem, XA(ks));
      MEMB();
      UNROLL for (int i = 0; i < 3; ++i) lds_st(smem, sl16 + i * 1024, pre[i]);
      MEMB();
      UNROLL for (int ks = 0; ks < 6; ++ks) xf[1][ks] = lds_ld(smem, t1adj + XA(ks));
      MEMB();
      x1[1][4] = PACKS(oc[1], 0);
      x1[1][5] = PACKS(oc[1], 1);
    }
    STAMPL(11);
    { int wy = win / 28, wx = win - wy * 28; ob = (unsigned)w * RS_IN + (unsigned)((wy * 8) * 224 + wx * 8) * 192u; }
    if (!more) {
      UNROLL for (int a = 0; a < 2; ++a) {
        UNROLL for (int ks = 0; ks < 6; ++ks) lds_st(smem, XA(ks), x1[a][ks]);
        MEMB();
        UNROLL for (int i = 0; i < 6; ++i) x1[a][i] = lds_ld(smem, sl16 + i * 1024);
        MEMB();
      }
      const __amdgpu_buffer_rsrc_t ors0 = __builtin_amdgcn_make_buffer_rsrc(out + (size_t)ob, 0, 0x41000, 0x00020000);
      const __amdgpu_buffer_rsrc_t ors1 = __builtin_amdgcn_make_buffer_rsrc(out + (size_t)(ob + ROWB), 0, 0x41000, 0x00020000);
      UNROLL for (int n = 0; n < 12; ++n) OSTORE(n);
      STAMPL(12);
      break;
    }
    STAMPL(12);
    win = nwin;
    ++iter;
  }
  *(unsigned*)(smem + CNT_OFF + 128 + wave * 4) = 0u;
  STAMP_OUT;
  asm volatile(".Lswin_pf_end:");
}

extern "C" void kernel_launch(void* const* d_in, const int* in_sizes, int n_in,
                              void* d_out, int out_size, void* d_ws, size_t ws_size,
                              hipStream_t stream) {
  swin_kernel<<<NBLK + 8, 512, 0, stream>>>((const char*)d_in[0], (const char*)d_in[1], (const char*)d_in[2],
                                       (const char*)d_in[3], (const char*)d_in[4], (char*)d_out, (char*)d_ws + (1 << 20), (unsigned long long*)d_ws);
}
```

```cpp
#define PB_PURE 0
#define PB_DENSE 2
#define FPK_GAP 12
#define JB_PRIO 1
#define XB_BEFORE 1
#define NBLK 196
#define NOLOOPST 0
#define T2BASE 2
#define BPREP_PRIO 2
#define CODEPF_COND wave == 3
#include <hip/hip_runtime.h>
#include <hip/hip_bf16.h>
#include <stdint.h>

#if defined(__HIP_DEVICE_COMPILE__)
#define NOPK __attribute__((target("no-packed-fp32-ops")))
#else
#define NOPK
#endif
#define DI __device__ __forceinline__ NOPK

typedef float f32x16 __attribute__((ext_vector_type(16)));
typedef float f32x4 __attribute__((ext_vector_type(4)));
typedef unsigned u32x4 __attribute__((ext_vector_type(4)));
typedef unsigned u32x2 __attribute__((ext_vector_type(2)));
typedef __bf16 bf16x8 __attribute__((ext_vector_type(8)));
typedef __bf16 bf16x2 __attribute__((ext_vector_type(2)));
typedef short s16x4 __attribute__((ext_vector_type(4)));
#define LAS __attribute__((address_space(3)))

constexpr int WF_OFF = 0;
constexpr int SLOT_OFF = 73728;
constexpr int SLOT_B = 9216;
constexpr int R_OFF = SLOT_OFF + 8 * SLOT_B;
constexpr int RP_OFF = R_OFF + 2048;
constexpr int CNT_OFF = RP_OFF + 1024;
constexpr int DUM_OFF = CNT_OFF + 256;
constexpr int LDS_BYTES = DUM_OFF + 9216;

constexpr unsigned RS_IN = 224u * 224u * 192u;
constexpr unsigned ROWB = 224u * 192u;
constexpr unsigned RS_OUT = 2u * ROWB;

DI f32x16 mfma32(u32x4 a, u32x4 b, f32x16 c) {
  return __builtin_amdgcn_mfma_f32_32x32x16_bf16(__builtin_bit_cast(bf16x8, a), __builtin_bit_cast(bf16x8, b), c, 0, 0, 0);
}
DI f32x4 mfma4(unsigned a0, unsigned a1, unsigned b0, unsigned b1, f32x4 c) {
  u32x2 a = {a0, a1}, b = {b0, b1};
  return __builtin_amdgcn_mfma_f32_4x4x4bf16_1k(__builtin_bit_cast(s16x4, a), __builtin_bit_cast(s16x4, b), c, 0, 0, 0);
}
DI unsigned pk(float lo, float hi) {
  bf16x2 p = {(__bf16)lo, (__bf16)hi};
  return __builtin_bit_cast(unsigned, p);
}
#define PACKS(x, s) u32x4{pk((x)[8 * (s)], (x)[8 * (s) + 1]), pk((x)[8 * (s) + 2], (x)[8 * (s) + 3]), \
                          pk((x)[8 * (s) + 4], (x)[8 * (s) + 5]), pk((x)[8 * (s) + 6], (x)[8 * (s) + 7])}
DI float fbits(unsigned u) { return __builtin_bit_cast(float, u); }
DI unsigned ubits(float f) { return __builtin_bit_cast(unsigned, f); }
DI float merge_add(float a, float b) {
  auto r = __builtin_amdgcn_permlane32_swap(ubits(a), ubits(b), false, false);
  return fbits(r[0]) + fbits(r[1]);
}
DI float xhalf_add(float a) { return merge_add(a, a); }
DI float xhalf_max(float a) {
  auto r = __builtin_amdgcn_permlane32_swap(ubits(a), ubits(a), false, false);
  return __builtin_fmaxf(fbits(r[0]), fbits(r[1]));
}
template <int I> DI float quad_bcast(float v) {
  return fbits((unsigned)__builtin_amdgcn_mov_dpp((int)ubits(v), I * 0x55, 0xf, 0xf, true));
}
DI u32x4 lds_ld(const char* smem, unsigned off) { return *(const u32x4*)(smem + off); }
DI void lds_st(char* smem, unsigned off, u32x4 v) { *(u32x4*)(smem + off) = v; }

DI void dma16(unsigned lds_dst, const char* sbase, unsigned voff) {
  unsigned keep;
  asm volatile("s_mov_b32 %0, m0\n\ts_mov_b32 m0, %1\n\ts_nop 2\n\tglobal_load_lds_dwordx4 %2, %3\n\ts_mov_b32 m0, %0"
               : "=&s"(keep) : "s"(lds_dst), "v"(voff), "s"(sbase) : "memory");
}
DI void dma16nt(unsigned lds_dst, const char* sbase, unsigned voff) {
  unsigned keep;
  asm volatile("s_mov_b32 %0, m0\n\ts_mov_b32 m0, %1\n\ts_nop 2\n\tglobal_load_lds_dwordx4 %2, %3 nt\n\ts_mov_b32 m0, %0"
               : "=&s"(keep) : "s"(lds_dst), "v"(voff), "s"(sbase) : "memory");
}
DI void dma16ntc(unsigned lds_dst, const char* sbase, unsigned voff, unsigned on) {
  unsigned keep;
  asm volatile("s_cmp_eq_u32 %4, 0\n\ts_cbranch_scc1 1f\n\ts_mov_b32 %0, m0\n\ts_mov_b32 m0, %1\n\ts_nop 0\n\t"
               "global_load_lds_dwordx4 %2, %3 nt\n\ts_mov_b32 m0, %0\n1:"
               : "=&s"(keep) : "s"(lds_dst), "v"(voff), "s"(sbase), "s"(on) : "memory", "scc");
}
template <int K> DI unsigned xad(unsigned a, unsigned c) {
  unsigned r;
  asm volatile("v_xad_u32 %0, %1, %2, %3" : "=v"(r) : "v"(a), "i"(K), "s"(c));
  return r;
}
DI void dma1ntc(unsigned lds_dst, const char* sbase, unsigned v0, unsigned on) {
  asm volatile("s_cmp_eq_u32 %3, 0\n\ts_mov_b32 m0, %0\n\ts_cbranch_scc1 1f\n\ts_nop 1\n\tglobal_load_lds_dwordx4 %1, %2 nt\n1:"
               :: "s"(lds_dst), "v"(v0), "s"(sbase), "s"(on) : "memory", "scc");
}
DI void dma3ntc(unsigned lds_dst, const char* sbase, unsigned v0, unsigned v1, unsigned v2, unsigned on) {
  asm volatile("s_cmp_eq_u32 %5, 0\n\ts_cbranch_scc1 1f\n\t"
               "s_mov_b32 m0, %0\n\ts_nop 0\n\tglobal_load_lds_dwordx4 %1, %4 nt\n\t"
               "s_add_u32 m0, m0, 0x400\n\ts_nop 0\n\tglobal_load_lds_dwordx4 %2, %4 nt\n\t"
               "s_add_u32 m0, m0, 0x400\n\ts_nop 0\n\tglobal_load_lds_dwordx4 %3, %4 nt\n1:"
               :: "s"(lds_dst), "v"(v0), "v"(v1), "v"(v2), "s"(sbase), "s"(on) : "memory", "scc");
}
DI void flag_st(const char* p, unsigned v) {
  unsigned a, d;
  asm volatile("s_nop 4\n\tv_mov_b32 %0, 0\n\tv_mov_b32 %1, %3\n\ts_mov_b64 exec, 1\n\tglobal_store_dword %0, %1, %2 sc1\n\ts_mov_b64 exec, -1"
               : "=&v"(a), "=&v"(d) : "s"(p), "s"(v) : "memory");
}
DI void flag_wait(const char* p, unsigned v) {
  unsigned a, d, sv;
  asm volatile("s_nop 4\n\tv_mov_b32 %0, 0\n"
               "1:\n\tglobal_load_dword %1, %0, %3 sc1\n\ts_waitcnt vmcnt(0)\n\tv_readfirstlane_b32 %2, %1\n\ts_nop 1\n\ts_cmp_eq_u32 %2, %4\n\t"
               "s_cbranch_scc1 2f\n\ts_sleep 8\n\ts_branch 1b\n2:"
               : "=&v"(a), "=&v"(d), "=&s"(sv) : "s"(p), "s"(v) : "memory", "scc");
}
DI void flags_init(const char* p, unsigned voff, unsigned long long mask) {
  unsigned d;
  asm volatile("s_nop 4\n\tv_mov_b32 %0, 0\n\ts_mov_b64 exec, %3\n\tglobal_store_dword %1, %0, %2 offset:128 sc1\n\ts_mov_b64 exec, -1"
               : "=&v"(d) : "v"(voff), "s"(p), "s"(mask) : "memory");
}
DI void flags_publish(const char* p, unsigned voff, unsigned long long mask) {
  unsigned d, e;
  asm volatile("s_nop 4\n\tv_mov_b32 %1, 2\n\ts_mov_b64 exec, %4\n"
               "1:\n\tglobal_store_dword %2, %1, %3 sc1\n\tglobal_load_dword %0, %2, %3 offset:128 sc1\n\ts_waitcnt vmcnt(0)\n\t"
               "v_cmp_ne_u32 vcc, 3, %0\n\ts_nop 4\n\ts_cbranch_vccz 2f\n\ts_sleep 16\n\ts_branch 1b\n2:\n\ts_mov_b64 exec, -1"
               : "=&v"(d), "=&v"(e) : "v"(voff), "s"(p), "s"(mask) : "memory", "vcc");
}
DI void fetch_s(unsigned on, unsigned peek, const char* flag, const char* src, unsigned lds_dst, unsigned l16) {
  unsigned a, d, sv;
  asm volatile("s_cmp_eq_u32 %3, 0\n\ts_cbranch_scc1 9f\n\t"
               "v_readfirstlane_b32 %2, %4\n\tv_mov_b32 %0, 0\n\ts_nop 0\n\ts_cmp_eq_u32 %2, 2\n\ts_cbranch_scc1 2f\n"
               "1:\n\tglobal_load_dword %1, %0, %5 sc1\n\ts_waitcnt vmcnt(0)\n\tv_readfirstlane_b32 %2, %1\n\ts_nop 1\n\ts_cmp_eq_u32 %2, 2\n\t"
               "s_cbranch_scc1 2f\n\ts_sleep 8\n\ts_branch 1b\n"
               "2:\n\tv_mov_b32 %1, 3\n\ts_mov_b64 exec, 1\n\tglobal_store_dword %0, %1, %5 offset:128 sc1\n\ts_mov_b64 exec, -1\n\t"
               "s_mov_b32 m0, %7\n\ts_nop 0\n\t"
               "global_load_lds_dwordx4 %8, %6 sc1\n\tglobal_load_lds_dwordx4 %8, %6 offset:1024 sc1\n\tglobal_load_lds_dwordx4 %8, %6 offset:2048 sc1\n\t"
               "global_load_lds_dwordx4 %8, %6 offset:3072 sc1\n\t"
               "v_add_u32 %0, 0x1000, %8\n\ts_add_u32 m0, m0, 0x1000\n\ts_nop 0\n\t"
               "global_load_lds_dwordx4 %0, %6 sc1\n\tglobal_load_lds_dwordx4 %0, %6 offset:1024 sc1\n\tglobal_load_lds_dwordx4 %0, %6 offset:2048 sc1\n\t"
               "global_load_lds_dwordx4 %0, %6 offset:3072 sc1\n\t"
               "v_add_u32 %0, 0x2000, %8\n\ts_add_u32 m0, m0, 0x1000\n\ts_nop 0\n\t"
               "global_load_lds_dwordx4 %0, %6 sc1\n"
               "9:"
               : "=&v"(a), "=&v"(d), "=&s"(sv)
               : "s"(on), "v"(peek), "s"(flag), "s"(src), "s"(lds_dst), "v"(l16) : "memory", "scc");
}
DI void drain_arrive(unsigned on, unsigned lds_addr) {
  unsigned a, o;
  asm volatile("s_cmp_eq_u32 %3, 0\n\ts_cbranch_scc1 9f\n\tv_mov_b32 %0, %2\n\tv_mov_b32 %1, 1\n\ts_waitcnt vmcnt(0)\n\t"
               "s_mov_b64 exec, 1\n\tds_add_u32 %0, %1\n\ts_mov_b64 exec, -1\n9:"
               : "=&v"(a), "=&v"(o) : "s"(lds_addr), "s"(on) : "memory", "scc");
}
DI void wait_vm0() { asm volatile("s_waitcnt vmcnt(0)" ::: "memory"); }
DI void wait_lgkm0() { asm volatile("s_waitcnt lgkmcnt(0)" ::: "memory"); }
DI void wg_barrier() { asm volatile("s_waitcnt lgkmcnt(0)\n\ts_barrier" ::: "memory"); }

DI void grp_arrive(unsigned lds_addr) {
  unsigned a, o;
  asm volatile("v_mov_b32 %1, 1\n\ts_waitcnt lgkmcnt(0)\n\tv_mov_b32 %0, %2\n\ts_mov_b64 exec, 1\n\tds_add_u32 %0, %1\n\ts_mov_b64 exec, -1"
               : "=&v"(a), "=&v"(o) : "s"(lds_addr) : "memory");
}
DI void grp_wait(unsigned lds_addr, unsigned target) {
  unsigned a, v, sv;
  asm volatile("s_nop 1\n\tv_mov_b32 %0, %3\n"
               "1:\n\tds_read_b32 %1, %0\n\ts_waitcnt lgkmcnt(0)\n\tv_readfirstlane_b32 %2, %1\n\ts_nop 1\n\ts_cmp_ge_u32 %2, %4\n\t"
               "s_cbranch_scc1 2f\n\ts_sleep 1\n\ts_branch 1b\n2:"
               : "=&v"(a), "=&v"(v), "=&s"(sv) : "s"(lds_addr), "s"(target) : "memory", "scc");
}
DI void grp_wait_peeked(unsigned lds_addr, unsigned target, unsigned peeked) {
  unsigned a, v, sv;
  asm volatile("s_nop 0\n\tv_readfirstlane_b32 %2, %5\n\tv_mov_b32 %0, %3\n\ts_cmp_ge_u32 %2, %4\n\ts_cbranch_scc1 2f\n"
               "1:\n\tds_read_b32 %1, %0\n\ts_waitcnt lgkmcnt(0)\n\tv_readfirstlane_b32 %2, %1\n\ts_nop 1\n\ts_cmp_ge_u32 %2, %4\n\t"
               "s_cbranch_scc1 2f\n\ts_sleep 1\n\ts_branch 1b\n2:"
               : "=&v"(a), "=&v"(v), "=&s"(sv) : "s"(lds_addr), "s"(target), "v"(peeked) : "memory", "scc");
}
template <int BASE> DI void set_prio_rem(unsigned rem, unsigned partner) {
  unsigned sv;
  asm volatile("s_nop 0\n\tv_readfirstlane_b32 %0, %2\n\ts_nop 0\n\ts_cmp_gt_u32 %1, %0\n\ts_cbranch_scc1 1f\n\ts_setprio %3\n\ts_branch 2f\n1:\n\ts_setprio %4\n2:"
               : "=&s"(sv) : "s"(rem), "v"(partner), "i"(BASE), "i"(BASE + 1) : "scc");
}
template <int PA, int PB> DI void set_prio(int grp) {
  asm volatile("s_cmp_eq_u32 %0, 0\n\ts_cbranch_scc1 1f\n\ts_setprio %2\n\ts_branch 2f\n1:\n\ts_setprio %1\n2:"
               :: "s"(grp), "i"(PA), "i"(PB) : "scc");
}

#define STAMP0(i)
#define STAMPL(i)
#define STAMP_DECL
#define STAMP_OUT
DI int swap23(int n) { return (n & 0x13) | ((n & 4) << 1) | ((n & 8) >> 1); }

#define SB() __builtin_amdgcn_sched_barrier(0)
#define MEMB() asm volatile("" ::: "memory")
#define UNROLL _Pragma("unroll")
#define PMODE 6
#define PRIO_DENSE()
#define PRIO_PURE()

__global__ __launch_bounds__(512, 2) NOPK void swin_kernel(const char* __restrict__ x, const char* __restrict__ wqkv_t,
                                                           const char* __restrict__ wo_t, const char* __restrict__ wqkv_s,
                                                           const char* __restrict__ wo_s, char* __restrict__ out, char* ws, unsigned long long* dbg) {
  __shared__ __attribute__((aligned(16))) char smem[LDS_BYTES];
  const unsigned lds0 = (unsigned)(size_t)smem;
  const int lane = threadIdx.x & 63;
  const int wave = __builtin_amdgcn_readfirstlane(threadIdx.x >> 6);
  const int grp = wave >> 2, w = wave & 3;
  const int col = lane & 31, h = lane >> 5, p = col >> 2, t = col & 3;
  const unsigned l16 = (unsigned)lane * 16u;
  const bool prod = blockIdx.x < 8;
  const int bid = prod ? 0 : blockIdx.x - 8;
  char* const hflags = ws + (blockIdx.x & 7) * 8192;
  char* const hflag = hflags + ((blockIdx.x >> 3) - 1) * 256;
  char* const hdata = ws + 65536 + (blockIdx.x & 7) * 36864;
  const unsigned long long cmask = (1ull << ((NBLK + 7 - (blockIdx.x & 7)) >> 3)) - 1;
  const char* const wq = prod ? wqkv_s : wqkv_t;
  const char* const wo = prod ? wo_s : wo_t;

  STAMP_DECL
  STAMP0(0);
  const unsigned m1in = lane < 32 ? l16 + 1024u : ((l16 - 512u) ^ 16u) + RS_IN;
  const unsigned m1out = lane < 32 ? l16 + 1024u : ((l16 - 512u) ^ 16u) + RS_OUT;
#define POFF(i, lb, m1) ((i) == 0 ? (lb) : (i) == 1 ? (m1) : (i) == 2 ? ((lb) ^ 16u) : (i) == 3 ? ((lb) ^ 32u) : (i) == 4 ? ((m1) ^ 32u) : ((lb) ^ 48u))
#define PUNI(i, RS) ((i) == 0 ? 0u : (i) == 1 ? 0u : (i) == 2 ? (RS) + 512u : (i) == 3 ? 2u * (RS) : (i) == 4 ? 2u * (RS) : 3u * (RS) + 512u)
#define OPAQUE(v) asm volatile("" : "+v"(v))
#define PIN(v) asm volatile("" : "+v"(v))
#define PFULL(i, m1, RS) ((i) == 0 ? l16 : (i) == 1 ? (m1) : (i) == 2 ? xad<16>(l16, PUNI(2, RS)) : (i) == 3 ? xad<32>(l16, PUNI(3, RS)) : (i) == 4 ? xad<32>(m1, PUNI(4, RS)) : xad<48>(l16, PUNI(5, RS)))
  const __amdgpu_buffer_rsrc_t xrs = __builtin_amdgcn_make_buffer_rsrc((void*)x, 0, 38535168, 0x00020000);
  int win = bid + NBLK * grp;
  u32x4 xf[2][6];
  unsigned xb0;
  { int wy = win / 28, wx = win - wy * 28; xb0 = (unsigned)((wy * 8 + 2 * w) * 224 + wx * 8) * 192u; }
#define XLOAD(n) xf[(n) / 6][(n) % 6] = __builtin_amdgcn_raw_buffer_load_b128(xrs, POFF((n) % 6, l16, m1in), xb0 + ((n) / 6) * ROWB + PUNI((n) % 6, RS_IN), 2)

  if (threadIdx.x < 64) *(unsigned*)(smem + CNT_OFF + threadIdx.x * 4) = 0u;
#define CN(g, o) (lds0 + CNT_OFF + (g) * 64 + (o))
  const unsigned slot = SLOT_OFF + wave * SLOT_B;
  const unsigned cntA = CNT_OFF + grp * 64 + 32, cntB = cntA + 4;
  const float inv96 = 1.0f / 96.0f;

  const unsigned xa_e = slot + (unsigned)((t * 96 + 12 * p + (h ^ t)) * 16);
  const unsigned xa_o = slot + (unsigned)((t * 96 + 12 * p + ((2 + h) ^ t)) * 16);
  const unsigned t1adj = t < 2 ? 6144u : (unsigned)-3072;
  const unsigned sl16 = slot + l16;
#define XA(ks) ((((ks) & 1) ? xa_o : xa_e) + 64 * ((ks) >> 1))

  {
    constexpr unsigned QK = SLOT_OFF + 36864, VO = WF_OFF + 36864;
    const char* xb = x + xb0;
#define XFIRST()                                                                                                 \
  {                                                                                                              \
    UNROLL for (int i = 0; i < 6; ++i) dma16nt(lds0 + slot + i * 1024, xb + PUNI(i, RS_IN), POFF(i, l16, m1in)); \
    UNROLL for (int i = 0; i < 3; ++i) dma16nt(lds0 + slot + 6144 + i * 1024, xb + ROWB + PUNI(i, RS_IN), POFF(i, l16, m1in)); \
  }
    if (grp && w < 3) {
      unsigned qo[3], qf[3];
      UNROLL for (int m = 0; m < 3; ++m) {
        int idx = m * 64 + lane, row = idx / 24, qq = idx - row * 24;
        int c = qq ^ (row >> 1);
        qo[m] = (unsigned)(row * 576 + c * 16);
        qf[m] = (unsigned)(row * 576 + (c ^ 4) * 16);
      }
      UNROLL for (int i = 0; i < 4; ++i)
        UNROLL for (int m = 0; m < 3; ++m)
          dma16(lds0 + QK + (4 * w + i) * 3072 + m * 1024, wq + (4 * w + i) * 8 * 576, (i & 1) ? qf[m] : qo[m]);
    }
    if (wave == 0 && prod) flags_init(hflags, (unsigned)lane * 256u, cmask);
    if (wave == 3 && !prod) flag_st(hflag, 1u);
    wg_barrier();
    if (!grp && !prod) XFIRST();
    UNROLL for (int n = 9; n < 12; ++n) XLOAD(n);
    MEMB();
    if (wave == 3) {
      asm volatile("s_waitcnt vmcnt(12)" ::: "memory");
      grp_arrive(CN(0, 20));
    }
    const int pn = swap23(col);
    unsigned qb[4];
    UNROLL for (int e = 0; e < 4; ++e) qb[e] = QK + (unsigned)(pn * 384 + (((2 * e) | h) ^ ((pn >> 1) & 7)) * 16);
    UNROLL for (int e = 0; e < 4; ++e) OPAQUE(qb[e]);
    STAMP0(1);
#define QKF(rt, c2) lds_ld(smem, qb[(c2) & 3] + (rt) * 12288 + ((c2) >> 2) * 128)
#define QKR(ro, c2) lds_ld(smem, qb[(c2) & 3] + (ro) + ((c2) >> 2) * 128)
    if (wave == 7) {
      unsigned vv[3];
      UNROLL for (int m = 0; m < 3; ++m) {
        int idx = m * 64 + lane, row2 = idx / 12, q2 = idx - row2 * 12;
        vv[m] = (unsigned)(row2 * 576 + 384 + (q2 ^ (row2 >> 2)) * 16);
      }
      for (int i = 0; i < 6; ++i) {
        UNROLL for (int m = 0; m < 3; ++m) dma16(lds0 + VO + i * 3072 + m * 1024, wo + i * 3072 + m * 1024, l16);
        UNROLL for (int m = 0; m < 3; ++m) dma16(lds0 + VO + 18432 + i * 3072 + m * 1024, wq + i * 16 * 576, vv[m]);
      }
      wait_vm0();
      grp_arrive(CN(0, 4));
      if (!prod) {
        grp_wait(CN(0, 16), 6);
        XFIRST();
      }
    } else if (w < 3) {
      const unsigned cn = lds0 + CNT_OFF;
      if (grp) {
        asm volatile("s_waitcnt vmcnt(3)" ::: "memory");
        grp_arrive(cn + 0);
      }
      grp_wait(cn + 0, 3);
      if (grp) {
        __builtin_amdgcn_s_setprio(3);
        {
          u32x4 af[3][6], bb[6];
          UNROLL for (int kk = 0; kk < 6; ++kk) { bb[kk] = QKR(w * 12288, kk); af[0][kk] = QKF(0, 6 + kk); }
          UNROLL for (int ct = 1; ct < 3; ++ct)
            UNROLL for (int kk = 0; kk < 6; ++kk) af[ct][kk] = QKF(ct, 6 + kk);
          SB();
          f32x16 d = {};
          UNROLL for (int ct = 0; ct < 3; ++ct)
            UNROLL for (int kk = 0; kk < 6; ++kk) d = mfma32(af[ct][kk], bb[kk], d);
          float r0 = (d[0] + d[1]) + (d[2] + d[3]), r1 = (d[4] + d[5]) + (d[6] + d[7]);
          float r2_ = (d[8] + d[9]) + (d[10] + d[11]), r3 = (d[12] + d[13]) + (d[14] + d[15]);
          float rs = xhalf_add((r0 + r1) + (r2_ + r3));
          *(float*)(smem + RP_OFF + (32 * w + pn) * 4) = rs * -inv96;
          grp_arrive(cn + 8);
          grp_arrive(cn + 16);
        }
        __builtin_amdgcn_s_setprio(JB_PRIO);
        unsigned vb[2];
        UNROLL for (int e = 0; e < 2; ++e) vb[e] = VO + (unsigned)(18432 + pn * 192 + (((2 * e) | h) ^ ((pn >> 2) & 3)) * 16);
        const unsigned tb = VO + (unsigned)((8 * h + ((lane & 15) >> 2)) * 192 + (16 * ((lane >> 4) & 1) + 8 * (lane & 1) + 4 * ((lane >> 1) & 1)) * 2 + w * 64);
        if (!prod) {
          grp_wait(cn + 16, 6);
          XFIRST();
        }
        grp_wait(cn + 4, 1);
#define VF(rt, kk) lds_ld(smem, vb[(kk) & 1] + (rt) * 6144 + ((kk) >> 1) * 64)
        u32x4 bfr[6], av[3][6];
        UNROLL for (int kk = 0; kk < 6; ++kk) {
          s16x4 lo = __builtin_amdgcn_ds_read_tr16_b64_v4i16((LAS s16x4*)(smem + tb + kk * 3072));
          s16x4 hi = __builtin_amdgcn_ds_read_tr16_b64_v4i16((LAS s16x4*)(smem + tb + kk * 3072 + 768));
          u32x2 l2 = __builtin_bit_cast(u32x2, lo), h2 = __builtin_bit_cast(u32x2, hi);
          bfr[kk] = u32x4{l2[0], l2[1], h2[0], h2[1]};
          av[0][kk] = VF(0, kk);
        }
        UNROLL for (int rt = 1; rt < 3; ++rt)
          UNROLL for (int kk = 0; kk < 6; ++kk) av[rt][kk] = VF(rt, kk);
        SB();
        f32x16 DB[3];
        float c0 = 0.f, c1 = 0.f;
        UNROLL for (int rt = 0; rt < 3; ++rt) {
          f32x16 d = {};
          UNROLL for (int kk = 0; kk < 6; ++kk) d = mfma32(av[rt][kk], bfr[kk], d);
          DB[rt] = d;
          if (rt == 0) grp_arrive(cn + 12);
          if (rt > 0) { UNROLL for (int i = 0; i < 16; i += 2) { c0 += DB[rt - 1][i]; c1 += DB[rt - 1][i + 1]; } }
          SB();
        }
        UNROLL for (int i = 0; i < 16; i += 2) { c0 += DB[2][i]; c1 += DB[2][i + 1]; }
        const float cm = xhalf_add(c0 + c1) * inv96;
        UNROLL for (int rt = 0; rt < 3; ++rt) {
          UNROLL for (int i = 0; i < 16; ++i) DB[rt][i] -= cm;
          lds_st(smem, WF_OFF + 18432 + (w * 6 + 2 * rt) * 1024 + l16, PACKS(DB[rt], 0));
          lds_st(smem, WF_OFF + 18432 + (w * 6 + 2 * rt + 1) * 1024 + l16, PACKS(DB[rt], 1));
        }
        grp_arrive(cn + 24);
#undef VF
        __builtin_amdgcn_s_setprio(0);
      } else {
        __builtin_amdgcn_s_setprio(2);
        f32x16 DA[3];
        float a0 = 0.f, a1 = 0.f;
        {
          u32x4 bb[6], af[3][6];
          UNROLL for (int kk = 0; kk < 6; ++kk) { bb[kk] = QKR(w * 12288, 6 + kk); af[0][kk] = QKF(0, kk); }
          UNROLL for (int rt = 1; rt < 3; ++rt)
            UNROLL for (int kk = 0; kk < 6; ++kk) af[rt][kk] = QKF(rt, kk);
          SB();
          UNROLL for (int rt = 0; rt < 3; ++rt) {
            f32x16 d = {};
            UNROLL for (int kk = 0; kk < 6; ++kk) d = mfma32(af[rt][kk], bb[kk], d);
            DA[rt] = d;
            if (rt == 0) grp_arrive(cn + 16);
            if (rt > 0) { UNROLL for (int i = 0; i < 16; i += 2) { a0 += DA[rt - 1][i]; a1 += DA[rt - 1][i + 1]; } }
            SB();
          }
          UNROLL for (int i = 0; i < 16; i += 2) { a0 += DA[2][i]; a1 += DA[2][i + 1]; }
        }
        grp_wait(cn + 8, 3);
        f32x4 rm[12];
        UNROLL for (int n = 0; n < 12; ++n) rm[n] = *(const f32x4*)(smem + RP_OFF + (32 * (n >> 2) + 16 * ((n >> 1) & 1) + 8 * h + 4 * (n & 1)) * 4);
        UNROLL for (int n = 0; n < 12; ++n) {
          a0 += rm[n][0] + rm[n][2]; a1 += rm[n][1] + rm[n][3];
          UNROLL for (int i = 0; i < 4; ++i) DA[n >> 2][4 * (n & 3) + i] += rm[n][i];
        }
        const float fA = 0.10206207261596575f * 1.4426950408889634f;
        const float cadd = xhalf_add(a0 + a1) * (-fA * inv96);
        UNROLL for (int rt = 0; rt < 3; ++rt) {
          UNROLL for (int i = 0; i < 16; ++i) DA[rt][i] = __builtin_fmaf(DA[rt][i], fA, cadd);
          lds_st(smem, WF_OFF + (w * 6 + 2 * rt) * 1024 + l16, PACKS(DA[rt], 0));
          lds_st(smem, WF_OFF + (w * 6 + 2 * rt + 1) * 1024 + l16, PACKS(DA[rt], 1));
        }
        grp_arrive(cn + 20);
      }
    }
#undef QKF
#undef QKR
#undef XFIRST
    STAMP0(2);
    if (prod) {
      grp_wait(CN(0, 20), 4);
      grp_wait(CN(0, 24), 3);
      const __amdgpu_buffer_rsrc_t hrs = __builtin_amdgcn_make_buffer_rsrc(hdata, 0, 36864, 0x00020000);
      UNROLL for (int i = 0; i < 5; ++i) {
        const unsigned o = (unsigned)(wave + 8 * i) * 1024u + l16;
        __builtin_amdgcn_raw_buffer_store_b128(lds_ld(smem, WF_OFF + o), hrs, o, 0, 16);
      }
      wait_vm0();
      wg_barrier();
      if (wave == 0) flags_publish(hflags, (unsigned)lane * 256u, cmask);
      return;
    }
  }
  wait_vm0();
  UNROLL for (int ks = 0; ks < 6; ++ks) xf[0][ks] = lds_ld(smem, XA(ks));
  MEMB();
  UNROLL for (int i = 0; i < 3; ++i) lds_st(smem, sl16 + i * 1024, xf[1][3 + i]);
  MEMB();
  UNROLL for (int ks = 0; ks < 6; ++ks) xf[1][ks] = lds_ld(smem, t1adj + XA(ks));
  MEMB();

  const bool own = (t == w);
  const unsigned exo = (unsigned)(h * 384 + p * 48 + ((t ^ w) - 1) * 16);
  const unsigned ex_w = own ? (unsigned)(DUM_OFF + h * 128 + ((3 * (p & 6) + 6 + (p & 1)) & 7) * 16)
                            : SLOT_OFF + (grp * 4 + t) * SLOT_B + exo;
  const unsigned ex_r = slot + exo;
  const unsigned long long notown = ~(0x1111111111111111ull << w);
  const unsigned rbase = R_OFF + wave * 256;
  unsigned wS = l16 + 36864u;
  asm volatile("" : "+v"(wS));
#define WLD(i) lds_ld(smem, ((i) < 36 ? l16 + (i) * 1024 : wS + ((i) - 36) * 1024))

  const unsigned ONES = 0x3F803F80u;
  const unsigned id0 = t == 0 ? 0x00003F80u : (t == 1 ? 0x3F800000u : 0u);
  const unsigned id1 = t == 2 ? 0x00003F80u : (t == 3 ? 0x3F800000u : 0u);
  unsigned msk[4];
  UNROLL for (int g = 0; g < 4; ++g) msk[g] = ((col >> 3) == g && h == (p & 1)) ? 0xffffffffu : 0u;
  const f32x4 z4 = {0.f, 0.f, 0.f, 0.f};
  const f32x16 zz = {};

  grp_wait(CN(0, 20), 4);

  u32x4 wb[4];
  wb[2] = WLD(0); wb[3] = WLD(1); wb[0] = WLD(2);
  u32x4 x1[2][6];
  UNROLL for (int a = 0; a < 2; ++a) UNROLL for (int i = 0; i < 6; ++i) x1[a][i] = wb[0];
  unsigned iter = 1;

#define OSTORE(n_) __builtin_amdgcn_raw_buffer_store_b128(x1[(n_) / 6][(n_) % 6], (n_) < 6 ? ors0 : ors1, PFULL((n_) % 6, m1out, RS_OUT), 0, 18)
#define LNFIN1(g0, g1, q0, q1)                                                                                    \
  {                                                                                                              \
    PIN(g0); PIN(g1);                                                                                            \
    float q0a = (t & 1) ? g0[1] : g0[0], q0b = (t & 1) ? g0[3] : g0[2];                                          \
    float q1a = (t & 1) ? g1[1] : g1[0], q1b = (t & 1) ? g1[3] : g1[2];                                          \
    q0 = (t & 2) ? q0b : q0a; q1 = (t & 2) ? q1b : q1a;                                                          \
  }
#define LNFIN2(sm0, sm1, q0, q1, r)                                                                              \
  {                                                                                                              \
    PIN(q0);                                                                                                     \
    float mean = merge_add(sm0[0], sm1[0]) * inv96;                                                              \
    float var = merge_add(q0, q1) * inv96 - mean * mean;                                                         \
    var = __builtin_fmaxf(var, 0.f);                                                                             \
    r = __builtin_amdgcn_rsqf(var + 1e-5f);                                                                      \
  }
#define T1_FILL(g)                                                                                               \
  {                                                                                                              \
    if ((g) < 3) { MEMB(); x1[0][2 * (g)] = lds_ld(smem, sl16 + (2 * (g)) * 1024); x1[0][2 * (g) + 1] = lds_ld(smem, sl16 + (2 * (g) + 1) * 1024); MEMB(); } \
    if ((g) >= 3 && (g) < 9) lds_st(smem, XA((g) - 3), x1[1][(g) >= 3 && (g) < 9 ? (g) - 3 : 0]);                \
    if ((g) >= 9 && (g) < 12) { const int k_ = (g) >= 9 && (g) < 12 ? 2 * ((g) - 9) : 0; MEMB(); x1[1][k_] = lds_ld(smem, sl16 + k_ * 1024); x1[1][k_ + 1] = lds_ld(smem, sl16 + (k_ + 1) * 1024); MEMB(); } \
    if ((g) == 10) LNFIN1(g0, g1, q0, q1);                                                                       \
    if ((g) == 12) LNFIN2(sm0, sm1, q0, q1, rr);                                                                 \
    if ((g) >= 13 && ((g) & 1)) OSTORE(((g) - 13) / 2);                                                          \
    if (((g) >= 14 && (g) <= 20 && !((g) & 1)) || ((g) >= 26 && (g) <= 32 && !((g) & 1))) {                      \
      const int tp_ = (g) >= 26 ? 1 : 0, k_ = (((g) - 14) % 12) / 2, a_ = (k_ >> 1) & 1, s_ = k_ & 1;           \
      PIN(acc[tp_][a_]);                                                                                         \
      uf[a_][2 * tp_ + s_] = PACKS(acc[tp_][a_], s_);                                                            \
    }                                                                                                            \
    if ((g) == 22) grp_arrive(lds0 + cntB);                                                                      \
    if ((g) == 24) { MEMB(); b1v = *(const unsigned*)(smem + CNT_OFF + 24); }                                    \
    if ((g) == 28) grp_wait_peeked(CN(0, 24), 3, b1v);                                                           \
  }
#define T1_STAGE()                                                                                               \
  {                                                                                                              \
    f32x4 sm0 = z4, sm1 = z4, g0 = z4, g1 = z4;                                                                  \
    unsigned b1v;                                                                                                \
    float q0, q1;                                                                                                \
    UNROLL for (int ks = 0; ks < 6; ++ks) {                                                                      \
      lds_st(smem, XA(ks), x1[0][ks]);                                                                           \
      UNROLL for (int c = 0; c < 2; ++c) {                                                                       \
        sm0 = mfma4(ONES, ONES, xf[0][ks][2 * c], xf[0][ks][2 * c + 1], sm0);                                    \
        sm1 = mfma4(ONES, ONES, xf[1][ks][2 * c], xf[1][ks][2 * c + 1], sm1);                                    \
        g0 = mfma4(xf[0][ks][2 * c], xf[0][ks][2 * c + 1], xf[0][ks][2 * c], xf[0][ks][2 * c + 1], g0);          \
        g1 = mfma4(xf[1][ks][2 * c], xf[1][ks][2 * c + 1], xf[1][ks][2 * c], xf[1][ks][2 * c + 1], g1);          \
      }                                                                                                          \
      SB();                                                                                                      \
    }                                                                                                            \
    UNROLL for (int i = 0; i < 18; ++i) {                                                                        \
      const int tt = i / 6, ks = i % 6;                                                                          \
      acc[tt & 1][0] = mfma32(wb[(i + 2) & 3], xf[0][ks], ks == 0 ? zz : acc[tt & 1][0]);                        \
      SB();                                                                                                      \
      wb[(i + 5) & 3] = WLD(i + 3);                                                                              \
      T1_FILL(2 * i);                                                                                            \
      SB();                                                                                                      \
      acc[tt & 1][1] = mfma32(wb[(i + 2) & 3], xf[1][ks], ks == 0 ? zz : acc[tt & 1][1]);                        \
      SB();                                                                                                      \
      T1_FILL(2 * i + 1);                                                                                        \
      SB();                                                                                                      \
    }                                                                                                            \
  }

  unsigned rem = (unsigned)((784 - win + 2 * NBLK - 1) / (2 * NBLK)) * 90u + (unsigned)grp;
  unsigned prem = 0;
#define STAGE(BASE, WGT)                                                                                         \
  {                                                                                                              \
    set_prio_rem<BASE>(rem, prem);                                                                               \
    MEMB();                                                                                                      \
    *(unsigned*)(smem + CNT_OFF + 128 + wave * 4) = rem;                                                                \
    prem = *(const unsigned*)(smem + CNT_OFF + 128 + (wave ^ 4) * 4);                                                   \
    MEMB();                                                                                                      \
    rem -= (WGT);                                                                                                \
  }
  unsigned ob = 0;
  PRIO_PURE();
  for (;;) {
    STAMPL(3);
    float rr;
    u32x4 uf[2][6];
    f32x16 acc[2][2];
    STAGE(PB_PURE, 16);
    const __amdgpu_buffer_rsrc_t ors0 = __builtin_amdgcn_make_buffer_rsrc(out + (size_t)ob, 0, iter > 1 ? 0x41000 : 0, 0x00020000);
    const __amdgpu_buffer_rsrc_t ors1 = __builtin_amdgcn_make_buffer_rsrc(out + (size_t)(ob + ROWB), 0, iter > 1 ? 0x41000 : 0, 0x00020000);
    T1_STAGE();
    STAMPL(4);
    STAGE(T2BASE, 20);
    u32x4 pt[2][2];
    unsigned a2v, fpk;
    const unsigned fon = (unsigned)__builtin_amdgcn_readfirstlane(!grp && iter == 1);
    const __amdgpu_buffer_rsrc_t frs = __builtin_amdgcn_make_buffer_rsrc(hflag, 0, fon * 4, 0x00020000);
    const int nwin = win + 2 * NBLK;
    const bool more = nwin < 784;
    const char* xb;
    unsigned pfon;
#define NEXTADDR()                                                                                               \
  {                                                                                                              \
    int nw = more ? nwin : win; int wy = nw / 28, wx = nw - wy * 28;                                             \
    xb = x + (unsigned)((wy * 8 + 2 * w) * 224 + wx * 8) * 192u;                                                 \
    pfon = (unsigned)__builtin_amdgcn_readfirstlane(more ? 0x7fffffff : 0);                                      \
    asm volatile("" : "+s"(xb), "+s"(pfon));                                                                     \
  }
    {
      f32x4 s0, s1, s0b, s1b;
      f32x16 vac[2][2];
      f32x16 oac[2];
      u32x4 vpk[2][2];
      float rs0, rs1, rs2, rs3, y0, y1, y2, y3, e0, e1, e2, e3;
      unsigned cbv;
      unsigned pa0, pa1, pb0, pb1;
      float sinv;
#define O_INIT(tt)                                                                                               \
  UNROLL for (int a = 0; a < 2; ++a) {                                                                           \
    f32x16 o;                                                                                                    \
    UNROLL for (int g = 0; g < 4; ++g) {                                                                         \
      f32x4 r4 = mfma4(id0, id1, xf[a][2 * (tt) + (g >> 1)][2 * (g & 1)], xf[a][2 * (tt) + (g >> 1)][2 * (g & 1) + 1], z4); \
      UNROLL for (int e = 0; e < 4; ++e) o[4 * g + e] = r4[e];                                                   \
    }                                                                                                            \
    oac[a] = o;                                                                                                  \
  }
#define O_STEP(tt)                                                                                               \
  O_INIT(tt);                                                                                                    \
  SB();                                                                                                          \
  oac[0] = mfma32(vpk[0][0], pt[0][0], oac[0]);                                                                  \
  oac[0] = mfma32(vpk[0][1], pt[0][1], oac[0]);                                                                  \
  oac[1] = mfma32(vpk[1][0], pt[1][0], oac[1]);                                                                  \
  oac[1] = mfma32(vpk[1][1], pt[1][1], oac[1]);
#define S2_STEP(tt, a, s)                                                                                        \
  PIN(oac[a]);                                                                                                   \
  x1[a][2 * (tt) + (s)] = PACKS(oac[a], s);                                                                      \
  lds_st(smem, ex_w + ((a) * 6 + 2 * (tt) + (s)) * 768, x1[a][2 * (tt) + (s)]);
#define VP_STEP(tt, k) { PIN(vac[(tt) & 1][(k) >> 1]); vpk[(k) >> 1][(k) & 1] = PACKS(vac[(tt) & 1][(k) >> 1], (k) & 1); }
#define T2_FILL(g)                                                                                               \
  {                                                                                                              \
    if ((g) >= 1 && (g) <= 4) { const int k_ = (g) >= 1 && (g) <= 4 ? (g) - 1 : 0; PIN(acc[0][k_ >> 1]); uf[k_ >> 1][4 + (k_ & 1)] = PACKS(acc[0][k_ >> 1], k_ & 1); } \
    if ((g) == 5) {                                                                                              \
      s0 = z4; s1 = z4; s0b = z4; s1b = z4;                                                                      \
      UNROLL for (int k2 = 0; k2 < 6; ++k2) {                                                                    \
        s0 = mfma4(xf[0][k2][0], xf[0][k2][1], uf[0][k2][0], uf[0][k2][1], s0);                                  \
        s1 = mfma4(xf[1][k2][0], xf[1][k2][1], uf[1][k2][0], uf[1][k2][1], s1);                                  \
        s0b = mfma4(xf[0][k2][2], xf[0][k2][3], uf[0][k2][2], uf[0][k2][3], s0b);                                \
        s1b = mfma4(xf[1][k2][2], xf[1][k2][3], uf[1][k2][2], uf[1][k2][3], s1b);                                \
      }                                                                                                          \
      rs0 = quad_bcast<0>(rr); rs1 = quad_bcast<1>(rr); rs2 = quad_bcast<2>(rr); rs3 = quad_bcast<3>(rr);        \
    }                                                                                                            \
    if ((g) == 6) { PIN(s0); PIN(s1); UNROLL for (int e = 0; e < 4; ++e) { s0[e] += s0b[e]; } s1[0] += s1b[0]; s1[1] += s1b[1]; } \
    if ((g) == 7) { PIN(s1); s1[2] += s1b[2]; s1[3] += s1b[3]; y0 = merge_add(s0[0], s1[0]) * rs0; }             \
    if ((g) == 8) { PIN(s0); y1 = merge_add(s0[1], s1[1]) * rs1; y2 = merge_add(s0[2], s1[2]); }                 \
    if ((g) == 9) { PIN(s0); y2 *= rs2; y3 = merge_add(s0[3], s1[3]) * rs3; }                                    \
    if ((g) == 10) {                                                                                             \
      PIN(y3);                                                                                                   \
      float mx = __builtin_fmaxf(__builtin_fmaxf(y0, y1), __builtin_fmaxf(y2, y3));                              \
      float cc = -rr * mx;                                                                                       \
      y0 = __builtin_fmaf(y0, rr, cc); y1 = __builtin_fmaf(y1, rr, cc);                                          \
      y2 = __builtin_fmaf(y2, rr, cc); y3 = __builtin_fmaf(y3, rr, cc);                                          \
    }                                                                                                            \
    if ((g) == 11) { PIN(y0); e0 = __builtin_amdgcn_exp2f(y0); e1 = __builtin_amdgcn_exp2f(y1); e2 = __builtin_amdgcn_exp2f(y2); } \
    if ((g) == 12) { PIN(y3); e3 = __builtin_amdgcn_exp2f(y3); sinv = __builtin_amdgcn_rcpf((e0 + e1) + (e2 + e3)); } \
    if ((g) == 13) { PIN(e0); e0 *= rs0; e1 *= rs1; e2 *= rs2; e3 *= rs3; e0 *= sinv; e1 *= sinv; }              \
    if ((g) == 14) {                                                                                             \
      PIN(e2);                                                                                                   \
      unsigned p01 = pk(e0, e1), p23 = pk(e2 * sinv, e3 * sinv);                                                 \
      auto a_ = __builtin_amdgcn_permlane32_swap(p01, p01, false, false);                                        \
      auto b_ = __builtin_amdgcn_permlane32_swap(p23, p23, false, false);                                        \
      pa0 = a_[0]; pa1 = a_[1]; pb0 = b_[0]; pb1 = b_[1];                                                        \
    }                                                                                                            \
    if ((g) == 15) { PIN(pa0); pt[0][0] = u32x4{pa0 & msk[0], pb0 & msk[0], pa0 & msk[1], pb0 & msk[1]}; { MEMB(); cbv = *(const unsigned*)(smem + cntB); } } \
    if ((g) == 16) { PIN(pa0); pt[0][1] = u32x4{pa0 & msk[2], pb0 & msk[2], pa0 & msk[3], pb0 & msk[3]}; }       \
    if ((g) == 17) { PIN(pa1); pt[1][0] = u32x4{pa1 & msk[0], pb1 & msk[0], pa1 & msk[1], pb1 & msk[1]}; }       \
    if ((g) == 18) { PIN(pa1); pt[1][1] = u32x4{pa1 & msk[2], pb1 & msk[2], pa1 & msk[3], pb1 & msk[3]}; }       \
    if ((g) >= 19 && (g) <= 22) VP_STEP(0, (g) >= 19 && (g) <= 22 ? (g) - 19 : 0);                               \
    if ((g) == 23) { grp_wait_peeked(lds0 + cntB, 4 * iter, cbv); O_STEP(0); }                                   \
    if ((g) >= 25 && (g) <= 28) { const int k_ = (g) >= 25 && (g) <= 28 ? (g) - 25 : 0; S2_STEP(0, k_ >> 1, k_ & 1); } \
    if ((g) >= 29 && (g) <= 32) VP_STEP(1, (g) >= 29 && (g) <= 32 ? (g) - 29 : 0);                               \
    if ((g) == FPK_GAP) fpk = __builtin_amdgcn_raw_buffer_load_b32(frs, 0, 0, 16);                               \
    if ((g) == 24) fetch_s(fon, fpk, hflag, hdata + w * 9216, lds0 + WF_OFF + 36864 + w * 9216, l16);            \
    if ((g) == 30) { MEMB(); a2v = *(const unsigned*)(smem + CNT_OFF + 64 + 20); }                               \
    if ((g) == 33) { O_STEP(1); }                                                                                \
    if ((g) == 35) { S2_STEP(1, 0, 0); S2_STEP(1, 0, 1); }                                                       \
  }
      UNROLL for (int j = 0; j < 18; ++j) {
        const int tt = j / 6, ks = j % 6;
        vac[tt & 1][0] = mfma32(xf[0][ks], wb[(j + 20) & 3], ks == 0 ? zz : vac[tt & 1][0]);
        SB();
        if (j < 15) wb[(j + 23) & 3] = WLD(j + 18 + 3);
        T2_FILL(2 * j);
        SB();
        vac[tt & 1][1] = mfma32(xf[1][ks], wb[(j + 20) & 3], ks == 0 ? zz : vac[tt & 1][1]);
        SB();
        T2_FILL(2 * j + 1);
        SB();
      }
      S2_STEP(1, 1, 0); S2_STEP(1, 1, 1);
      SB();
      O_INIT(2);
      VP_STEP(2, 0); VP_STEP(2, 1);
      SB();
      oac[0] = mfma32(vpk[0][0], pt[0][0], oac[0]);
      oac[0] = mfma32(vpk[0][1], pt[0][1], oac[0]);
      SB();
      VP_STEP(2, 2); VP_STEP(2, 3);
      SB();
      oac[1] = mfma32(vpk[1][0], pt[1][0], oac[1]);
      oac[1] = mfma32(vpk[1][1], pt[1][1], oac[1]);
      SB();
      NEXTADDR();
      S2_STEP(2, 0, 0); S2_STEP(2, 0, 1);
      SB();
      S2_STEP(2, 1, 0); S2_STEP(2, 1, 1);
#undef O_INIT
#undef VP_STEP
#undef T2_FILL
#undef O_STEP
#undef S2_STEP
    }
    STAMPL(5);
    grp_arrive(lds0 + cntA);
    drain_arrive(fon, CN(1, 20));
    grp_wait_peeked(CN(1, 20), 4, a2v);
    wb[0] = WLD(36); wb[1] = WLD(37); wb[2] = WLD(38);
    grp_wait(lds0 + cntA, 4 * iter);
    asm volatile("s_mov_b64 exec, %13\n\t"
                 "ds_read_b128 %0, %12\n\tds_read_b128 %1, %12 offset:768\n\tds_read_b128 %2, %12 offset:1536\n\t"
                 "ds_read_b128 %3, %12 offset:2304\n\tds_read_b128 %4, %12 offset:3072\n\tds_read_b128 %5, %12 offset:3840\n\t"
                 "ds_read_b128 %6, %12 offset:4608\n\tds_read_b128 %7, %12 offset:5376\n\tds_read_b128 %8, %12 offset:6144\n\t"
                 "ds_read_b128 %9, %12 offset:6912\n\tds_read_b128 %10, %12 offset:7680\n\tds_read_b128 %11, %12 offset:8448\n\t"
                 "s_mov_b64 exec, -1\n\ts_waitcnt lgkmcnt(0)"
                 : "+v"(x1[0][0]), "+v"(x1[0][1]), "+v"(x1[0][2]), "+v"(x1[0][3]), "+v"(x1[0][4]), "+v"(x1[0][5]),
                   "+v"(x1[1][0]), "+v"(x1[1][1]), "+v"(x1[1][2]), "+v"(x1[1][3]), "+v"(x1[1][4]), "+v"(x1[1][5])
                 : "v"(lds0 + ex_r), "s"(notown) : "memory");
    float rq[2];
    const unsigned rka = rbase + h * 16;
#define RKLD(G) (*(const f32x4*)(smem + rka + (G) * 32))
    f32x4 rkb[3];
    STAMPL(6);
    STAGE(PB_PURE, 14);
    const __amdgpu_buffer_rsrc_t nrs = __builtin_amdgcn_make_buffer_rsrc((void*)(xb + ROWB), 0, pfon, 0x00020000);
    u32x4 pre[3];
    SB();
#define SW(k) ((k) < 18 ? 36 + (k) : ((k) < 54 ? 18 + (k) : (k) - 54))
    u32x4 pf[2][4];
    u32x4 vp[3][4];
    f32x16 sa[2][2];
    u32x4 uq[6];
    f32x16 ua[2];
    float mxv, ccv, l0, l1, invv;
#define U_STEP(q, i)                                                                                             \
  {                                                                                                              \
    const int tt_ = (i) / 6, ks_ = (i) % 6, k_ = 18 * (q) + (i);                                                 \
    ua[tt_ & 1] = mfma32(wb[k_ & 3], x1[q][ks_], ks_ == 0 ? zz : ua[tt_ & 1]);                                   \
    SB();                                                                                                        \
    wb[(k_ + 3) & 3] = WLD(SW(k_ + 3));                                                                          \
    if (tt_ > 0 && (ks_ == 2 || ks_ == 3)) { PIN(ua[(tt_ + 1) & 1]); uq[2 * (tt_ - 1) + (ks_ - 2)] = PACKS(ua[(tt_ + 1) & 1], ks_ - 2); } \
  }
#define S_STEP(q, ks_)                                                                                           \
  {                                                                                                              \
    sa[q][0] = mfma32(x1[0][ks_], uq[ks_], (ks_) == 0 ? zz : sa[q][0]);                                          \
    SB();                                                                                                        \
    if ((ks_) == 1) { PIN(ua[0]); uq[4] = PACKS(ua[0], 0); }                                                     \
    if ((ks_) == 2) { PIN(ua[0]); uq[5] = PACKS(ua[0], 1); }                                                     \
    SFILL;                                                                                                       \
    SB();                                                                                                        \
    sa[q][1] = mfma32(x1[1][ks_], uq[ks_], (ks_) == 0 ? zz : sa[q][1]);                                          \
    SB();                                                                                                        \
  }
#define SFILL
#define P1(q, G)                                                                                                 \
  {                                                                                                              \
    const int n_ = 16 * (q) + (G);                                                                               \
    MEMB(); rkb[(n_ + 2) % 3] = RKLD(((G) + 2) & 7);                                                             \
    UNROLL for (int e = 0; e < 4; ++e) sa[q][(G) >> 2][4 * ((G) & 3) + e] *= rkb[n_ % 3][e];                     \
    mxv = (G) == 0 ? sa[q][0][0] : __builtin_fmaxf(mxv, sa[q][(G) >> 2][4 * ((G) & 3)]);                         \
    mxv = __builtin_fmaxf(mxv, sa[q][(G) >> 2][4 * ((G) & 3) + 1]);                                              \
    mxv = __builtin_fmaxf(__builtin_fmaxf(mxv, sa[q][(G) >> 2][4 * ((G) & 3) + 2]), sa[q][(G) >> 2][4 * ((G) & 3) + 3]); \
  }
#define X1(q) { mxv = xhalf_max(mxv); ccv = -rq[q] * mxv; l0 = 0.f; l1 = 0.f; }
#define P2H(q, H)                                                                                                \
  {                                                                                                              \
    const int b_ = (H) >> 3, i_ = 2 * ((H) & 7), G_ = (H) >> 1, n_ = 16 * (q) + 8 + G_;                          \
    if (((H) & 1) == 0 && n_ < 30) { MEMB(); rkb[(n_ + 2) % 3] = RKLD((G_ + 2) & 7); }                           \
    const float ea = __builtin_amdgcn_exp2f(__builtin_fmaf(sa[q][b_][i_], rq[q], ccv));                          \
    const float eb = __builtin_amdgcn_exp2f(__builtin_fmaf(sa[q][b_][i_ + 1], rq[q], ccv));                      \
    l0 += ea; l1 += eb;                                                                                          \
    sa[q][b_][i_] = ea * rkb[n_ % 3][2 * ((H) & 1)]; sa[q][b_][i_ + 1] = eb * rkb[n_ % 3][2 * ((H) & 1) + 1];    \
  }
#define X2(q) { invv = __builtin_amdgcn_rcpf(xhalf_add(l0 + l1)); }
#define P3(q, F)                                                                                                 \
  {                                                                                                              \
    UNROLL for (int e = 0; e < 8; ++e) sa[q][(F) >> 1][8 * ((F) & 1) + e] *= invv;                               \
    pf[q][F] = PACKS(sa[q][(F) >> 1], (F) & 1);                                                                  \
  }
    {
      f32x4 sm0 = z4, sm1 = z4, g0 = z4, g1 = z4;
      UNROLL for (int ks = 0; ks < 6; ++ks) {
        UNROLL for (int c = 0; c < 2; ++c) {
          sm0 = mfma4(ONES, ONES, x1[0][ks][2 * c], x1[0][ks][2 * c + 1], sm0);
          sm1 = mfma4(ONES, ONES, x1[1][ks][2 * c], x1[1][ks][2 * c + 1], sm1);
          g0 = mfma4(x1[0][ks][2 * c], x1[0][ks][2 * c + 1], x1[0][ks][2 * c], x1[0][ks][2 * c + 1], g0);
          g1 = mfma4(x1[1][ks][2 * c], x1[1][ks][2 * c + 1], x1[1][ks][2 * c], x1[1][ks][2 * c + 1], g1);
        }
        SB();
      }
      float q0, q1;
      UNROLL for (int i = 0; i < 18; ++i) {
        U_STEP(0, i);
        if (i == 1) LNFIN1(g0, g1, q0, q1);
        if (i == 2) {
          float r2;
          LNFIN2(sm0, sm1, q0, q1, r2);
          *(float*)(smem + rbase + lane * 4) = r2;
          auto rb = __builtin_amdgcn_permlane32_swap(ubits(r2), ubits(r2), false, false);
          rq[0] = fbits(rb[0]); rq[1] = fbits(rb[1]);
          MEMB();
        }
        {
          const int n = i < 8 ? i - 3 : (i < 10 ? -1 : (i < 14 ? i - 5 : -1));
          if (n >= 0 && n < 6) dma1ntc(lds0 + slot + n * 1024, xb, PFULL(n >= 0 && n < 6 ? n : 0, m1in, RS_IN), pfon);
          if (n >= 6) dma1ntc(lds0 + slot + n * 1024, xb + ROWB, PFULL(n >= 6 ? n - 6 : 0, m1in, RS_IN), pfon);
        }
        if (i >= 16) pre[i - 16] = __builtin_amdgcn_raw_buffer_load_b128(nrs, PFULL(i >= 16 ? i - 13 : 0, m1in, RS_IN), 0, 2);
        SB();
      }
    }
    UNROLL for (int ks = 0; ks < 6; ++ks) {
      S_STEP(0, ks);
      if (ks == 3) pre[2] = __builtin_amdgcn_raw_buffer_load_b128(nrs, PFULL(5, m1in, RS_IN), 0, 2);
      if (ks == 4) { rkb[0] = RKLD(0); rkb[1] = RKLD(1); }
      SB();
    }
    STAMPL(7);
    PRIO_DENSE();
    STAGE(PB_DENSE, 30);
    UNROLL for (int i = 0; i < 18; ++i) {
      U_STEP(1, i);
      if (i >= 1 && i <= 4) { P1(0, 2 * (i - 1)); P1(0, 2 * (i - 1) + 1); }
      if (i == 5) X1(0);
      if (i >= 6) P2H(0, i - 6);
      SB();
    }
#undef SFILL
#define SFILL { if (ks == 0) P2H(0, 12); if (ks == 1) P2H(0, 14); if (ks == 3) P3(0, 0); if (ks == 4) P3(0, 2); }
    UNROLL for (int ks = 0; ks < 6; ++ks) {
      S_STEP(1, ks);
      if (ks == 0) P2H(0, 13);
      if (ks == 1) P2H(0, 15);
      if (ks == 2) X2(0);
      if (ks == 3) P3(0, 1);
      if (ks == 4) P3(0, 3);
      SB();
    }
    STAMPL(8);
    {
      f32x16 v0, v1;
#define R2_FILL(g)                                                                                               \
  {                                                                                                              \
    if ((g) >= 1 && (g) <= 8) P1(1, (g) >= 1 && (g) <= 8 ? (g) - 1 : 0);                                         \
    if ((g) == 9) X1(1);                                                                                         \
    { const int H_ = (g) == 10 ? 0 : ((g) >= 14 && (g) <= 22 ? (g) - 13 : ((g) >= 26 && (g) <= 31 ? (g) - 16 : -1)); \
      if (H_ >= 0) P2H(1, H_ >= 0 ? H_ : 0); }                                                                   \
    if ((g) == 32) X2(1);                                                                                        \
    if ((g) == 33) P3(1, 0);                                                                                     \
    if ((g) == 34) P3(1, 1);                                                                                     \
    if ((g) == 11 || (g) == 23 || (g) == 35) { PIN(v0); vp[(g) / 12][0] = PACKS(v0, 0); vp[(g) / 12][1] = PACKS(v0, 1); } \
    if ((g) == 12 || (g) == 24) { PIN(v1); vp[(g) / 12 - 1][2] = PACKS(v1, 0); vp[(g) / 12 - 1][3] = PACKS(v1, 1); } \
  }
      UNROLL for (int j = 0; j < 18; ++j) {
        const int ks = j % 6, k = 36 + j;
        v0 = mfma32(x1[0][ks], wb[k & 3], ks == 0 ? zz : v0);
        SB();
        wb[(k + 3) & 3] = WLD(SW(k + 3));
        R2_FILL(2 * j);
        SB();
        v1 = mfma32(x1[1][ks], wb[k & 3], ks == 0 ? zz : v1);
        SB();
        R2_FILL(2 * j + 1);
        SB();
      }
#undef R2_FILL
      STAMPL(9);
      PRIO_PURE();
      STAGE(PB_PURE, 10);
      f32x16 oc[2];
      UNROLL for (int n = 0; n < 6; ++n) {
        const int q = n / 3, tt = n % 3;
        f32x16 o;
        UNROLL for (int g = 0; g < 4; ++g) {
          f32x4 r4 = mfma4(id0, id1, x1[q][2 * tt + (g >> 1)][2 * (g & 1)], x1[q][2 * tt + (g >> 1)][2 * (g & 1) + 1], z4);
          UNROLL for (int e = 0; e < 4; ++e) o[4 * g + e] = r4[e];
        }
        SB();
        if (n == 0) { PIN(v1); vp[2][2] = PACKS(v1, 0); }
        o = mfma32(vp[tt][0], pf[q][0], o);
        SB();
        if (n == 0) { PIN(v1); vp[2][3] = PACKS(v1, 1); }
        if (n == 1) P3(1, 2);
        if (n == 2) P3(1, 3);
        SB();
        o = mfma32(vp[tt][1], pf[q][1], o);
        SB();
        if (n > 0) { PIN(oc[(n + 1) & 1]); x1[(n - 1) / 3][2 * ((n - 1) % 3)] = PACKS(oc[(n + 1) & 1], 0); }
        SB();
        o = mfma32(vp[tt][2], pf[q][2], o);
        SB();
        if (n > 0) { PIN(oc[(n + 1) & 1]); x1[(n - 1) / 3][2 * ((n - 1) % 3) + 1] = PACKS(oc[(n + 1) & 1], 1); }
        SB();
        o = mfma32(vp[tt][3], pf[q][3], o);
        oc[n & 1] = o;
        SB();
      }
      STAMPL(10);
      wait_vm0();
      UNROLL for (int ks = 0; ks < 6; ++ks) xf[0][ks] = lds_ld(smem, XA(ks));
      MEMB();
      UNROLL for (int i = 0; i < 3; ++i) lds_st(smem, sl16 + i * 1024, pre[i]);
      MEMB();
      UNROLL for (int ks = 0; ks < 6; ++ks) xf[1][ks] = lds_ld(smem, t1adj + XA(ks));
      MEMB();
      x1[1][4] = PACKS(oc[1], 0);
      x1[1][5] = PACKS(oc[1], 1);
    }
    STAMPL(11);
    { int wy = win / 28, wx = win - wy * 28; ob = (unsigned)w * RS_IN + (unsigned)((wy * 8) * 224 + wx * 8) * 192u; }
    if (!more) {
      UNROLL for (int a = 0; a < 2; ++a) {
        UNROLL for (int ks = 0; ks < 6; ++ks) lds_st(smem, XA(ks), x1[a][ks]);
        MEMB();
        UNROLL for (int i = 0; i < 6; ++i) x1[a][i] = lds_ld(smem, sl16 + i * 1024);
        MEMB();
      }
      const __amdgpu_buffer_rsrc_t ors0 = __builtin_amdgcn_make_buffer_rsrc(out + (size_t)ob, 0, 0x41000, 0x00020000);
      const __amdgpu_buffer_rsrc_t ors1 = __builtin_amdgcn_make_buffer_rsrc(out + (size_t)(ob + ROWB), 0, 0x41000, 0x00020000);
      UNROLL for (int n = 0; n < 12; ++n) OSTORE(n);
      STAMPL(12);
      break;
    }
    STAMPL(12);
    win = nwin;
    ++iter;
  }
  *(unsigned*)(smem + CNT_OFF + 128 + wave * 4) = 0u;
  STAMP_OUT;
}

extern "C" void kernel_launch(void* const* d_in, const int* in_sizes, int n_in,
                              void* d_out, int out_size, void* d_ws, size_t ws_size,
                              hipStream_t stream) {
  swin_kernel<<<NBLK + 8, 512, 0, stream>>>((const char*)d_in[0], (const char*)d_in[1], (const char*)d_in[2],
                                       (const char*)d_in[3], (const char*)d_in[4], (char*)d_out, (char*)d_ws + (1 << 20), (unsigned long long*)d_ws);
}
```

```cpp
#include <hip/hip_runtime.h>
#include <hip/hip_bf16.h>
#include <stdint.h>

#if defined(__HIP_DEVICE_COMPILE__)
#define NOPK __attribute__((target("no-packed-fp32-ops")))
#else
#define NOPK
#endif
#define DI __device__ __forceinline__ NOPK

typedef float f32x16 __attribute__((ext_vector_type(16)));
typedef float f32x4 __attribute__((ext_vector_type(4)));
typedef unsigned u32x4 __attribute__((ext_vector_type(4)));
typedef unsigned u32x2 __attribute__((ext_vector_type(2)));
typedef __bf16 bf16x8 __attribute__((ext_vector_type(8)));
typedef __bf16 bf16x2 __attribute__((ext_vector_type(2)));
typedef short s16x4 __attribute__((ext_vector_type(4)));
#define LAS __attribute__((address_space(3)))

constexpr int WF_OFF = 0;
constexpr int SLOT_OFF = 73728;
constexpr int SLOT_B = 9216;
constexpr int R_OFF = SLOT_OFF + 8 * SLOT_B;
constexpr int RP_OFF = R_OFF + 2048;
constexpr int CNT_OFF = RP_OFF + 1024;
constexpr int DUM_OFF = CNT_OFF + 256;
constexpr int LDS_BYTES = DUM_OFF + 9216;

constexpr int NBLK = 196;
constexpr unsigned RS_IN = 224u * 224u * 192u;
constexpr unsigned ROWB = 224u * 192u;
constexpr unsigned RS_OUT = 2u * ROWB;

DI f32x16 mfma32(u32x4 a, u32x4 b, f32x16 c) {
  return __builtin_amdgcn_mfma_f32_32x32x16_bf16(__builtin_bit_cast(bf16x8, a), __builtin_bit_cast(bf16x8, b), c, 0, 0, 0);
}
DI f32x4 mfma4(unsigned a0, unsigned a1, unsigned b0, unsigned b1, f32x4 c) {
  u32x2 a = {a0, a1}, b = {b0, b1};
  return __builtin_amdgcn_mfma_f32_4x4x4bf16_1k(__builtin_bit_cast(s16x4, a), __builtin_bit_cast(s16x4, b), c, 0, 0, 0);
}
DI unsigned pk(float lo, float hi) {
  bf16x2 p = {(__bf16)lo, (__bf16)hi};
  return __builtin_bit_cast(unsigned, p);
}
#define PACKS(x, s) u32x4{pk((x)[8 * (s)], (x)[8 * (s) + 1]), pk((x)[8 * (s) + 2], (x)[8 * (s) + 3]), \
                          pk((x)[8 * (s) + 4], (x)[8 * (s) + 5]), pk((x)[8 * (s) + 6], (x)[8 * (s) + 7])}
DI float fbits(unsigned u) { return __builtin_bit_cast(float, u); }
DI unsigned ubits(float f) { return __builtin_bit_cast(unsigned, f); }
DI float merge_add(float a, float b) {
  auto r = __builtin_amdgcn_permlane32_swap(ubits(a), ubits(b), false, false);
  return fbits(r[0]) + fbits(r[1]);
}
DI float xhalf_add(float a) { return merge_add(a, a); }
DI float xhalf_max(float a) {
  auto r = __builtin_amdgcn_permlane32_swap(ubits(a), ubits(a), false, false);
  return __builtin_fmaxf(fbits(r[0]), fbits(r[1]));
}
template <int I> DI float quad_bcast(float v) {
  return fbits((unsigned)__builtin_amdgcn_mov_dpp((int)ubits(v), I * 0x55, 0xf, 0xf, true));
}
DI u32x4 lds_ld(const char* smem, unsigned off) { return *(const u32x4*)(smem + off); }
DI void lds_st(char* smem, unsigned off, u32x4 v) { *(u32x4*)(smem + off) = v; }

DI void dma16(unsigned lds_dst, const char* sbase, unsigned voff) {
  unsigned keep;
  asm volatile("s_mov_b32 %0, m0\n\ts_mov_b32 m0, %1\n\ts_nop 2\n\tglobal_load_lds_dwordx4 %2, %3\n\ts_mov_b32 m0, %0"
               : "=&s"(keep) : "s"(lds_dst), "v"(voff), "s"(sbase) : "memory");
}
DI void dma16nt(unsigned lds_dst, const char* sbase, unsigned voff) {
  unsigned keep;
  asm volatile("s_mov_b32 %0, m0\n\ts_mov_b32 m0, %1\n\ts_nop 2\n\tglobal_load_lds_dwordx4 %2, %3 nt\n\ts_mov_b32 m0, %0"
               : "=&s"(keep) : "s"(lds_dst), "v"(voff), "s"(sbase) : "memory");
}
template <int K> DI unsigned xad(unsigned a, unsigned c) {
  unsigned r;
  asm volatile("v_xad_u32 %0, %1, %2, %3" : "=v"(r) : "v"(a), "i"(K), "s"(c));
  return r;
}
DI void dma1ntc(unsigned lds_dst, const char* sbase, unsigned v0, unsigned on) {
  asm volatile("s_cmp_eq_u32 %3, 0\n\ts_mov_b32 m0, %0\n\ts_cbranch_scc1 1f\n\ts_nop 1\n\tglobal_load_lds_dwordx4 %1, %2 nt\n1:"
               :: "s"(lds_dst), "v"(v0), "s"(sbase), "s"(on) : "memory", "scc");
}
DI void flag_st(const char* p, unsigned v) {
  unsigned a, d;
  asm volatile("s_nop 4\n\tv_mov_b32 %0, 0\n\tv_mov_b32 %1, %3\n\ts_mov_b64 exec, 1\n\tglobal_store_dword %0, %1, %2 sc1\n\ts_mov_b64 exec, -1"
               : "=&v"(a), "=&v"(d) : "s"(p), "s"(v) : "memory");
}
DI void flags_init(const char* p, unsigned voff, unsigned long long mask) {
  unsigned d;
  asm volatile("s_nop 4\n\tv_mov_b32 %0, 0\n\ts_mov_b64 exec, %3\n\tglobal_store_dword %1, %0, %2 offset:128 sc1\n\ts_mov_b64 exec, -1"
               : "=&v"(d) : "v"(voff), "s"(p), "s"(mask) : "memory");
}
DI void flags_publish(const char* p, unsigned voff, unsigned long long mask) {
  unsigned d, e;
  asm volatile("s_nop 4\n\tv_mov_b32 %1, 2\n\ts_mov_b64 exec, %4\n"
               "1:\n\tglobal_store_dword %2, %1, %3 sc1\n\tglobal_load_dword %0, %2, %3 offset:128 sc1\n\ts_waitcnt vmcnt(0)\n\t"
               "v_cmp_ne_u32 vcc, 3, %0\n\ts_nop 4\n\ts_cbranch_vccz 2f\n\ts_sleep 16\n\ts_branch 1b\n2:\n\ts_mov_b64 exec, -1"
               : "=&v"(d), "=&v"(e) : "v"(voff), "s"(p), "s"(mask) : "memory", "vcc");
}
DI void fetch_s(unsigned on, unsigned peek, const char* flag, const char* src, unsigned lds_dst, unsigned l16) {
  unsigned a, d, sv;
  asm volatile("s_cmp_eq_u32 %3, 0\n\ts_cbranch_scc1 9f\n\t"
               "v_readfirstlane_b32 %2, %4\n\tv_mov_b32 %0, 0\n\ts_nop 0\n\ts_cmp_eq_u32 %2, 2\n\ts_cbranch_scc1 2f\n"
               "1:\n\tglobal_load_dword %1, %0, %5 sc1\n\ts_waitcnt vmcnt(0)\n\tv_readfirstlane_b32 %2, %1\n\ts_nop 1\n\ts_cmp_eq_u32 %2, 2\n\t"
               "s_cbranch_scc1 2f\n\ts_sleep 8\n\ts_branch 1b\n"
               "2:\n\tv_mov_b32 %1, 3\n\ts_mov_b64 exec, 1\n\tglobal_store_dword %0, %1, %5 offset:128 sc1\n\ts_mov_b64 exec, -1\n\t"
               "s_mov_b32 m0, %7\n\ts_nop 0\n\t"
               "global_load_lds_dwordx4 %8, %6 sc1\n\tglobal_load_lds_dwordx4 %8, %6 offset:1024 sc1\n\tglobal_load_lds_dwordx4 %8, %6 offset:2048 sc1\n\t"
               "global_load_lds_dwordx4 %8, %6 offset:3072 sc1\n\t"
               "v_add_u32 %0, 0x1000, %8\n\ts_add_u32 m0, m0, 0x1000\n\ts_nop 0\n\t"
               "global_load_lds_dwordx4 %0, %6 sc1\n\tglobal_load_lds_dwordx4 %0, %6 offset:1024 sc1\n\tglobal_load_lds_dwordx4 %0, %6 offset:2048 sc1\n\t"
               "global_load_lds_dwordx4 %0, %6 offset:3072 sc1\n\t"
               "v_add_u32 %0, 0x2000, %8\n\ts_add_u32 m0, m0, 0x1000\n\ts_nop 0\n\t"
               "global_load_lds_dwordx4 %0, %6 sc1\n"
               "9:"
               : "=&v"(a), "=&v"(d), "=&s"(sv)
               : "s"(on), "v"(peek), "s"(flag), "s"(src), "s"(lds_dst), "v"(l16) : "memory", "scc");
}
DI void drain_arrive(unsigned on, unsigned lds_addr) {
  unsigned a, o;
  asm volatile("s_cmp_eq_u32 %3, 0\n\ts_cbranch_scc1 9f\n\tv_mov_b32 %0, %2\n\tv_mov_b32 %1, 1\n\ts_waitcnt vmcnt(0)\n\t"
               "s_mov_b64 exec, 1\n\tds_add_u32 %0, %1\n\ts_mov_b64 exec, -1\n9:"
               : "=&v"(a), "=&v"(o) : "s"(lds_addr), "s"(on) : "memory", "scc");
}
DI void wait_vm0() { asm volatile("s_waitcnt vmcnt(0)" ::: "memory"); }
DI void wait_lgkm0() { asm volatile("s_waitcnt lgkmcnt(0)" ::: "memory"); }
DI void wg_barrier() { asm volatile("s_waitcnt lgkmcnt(0)\n\ts_barrier" ::: "memory"); }

DI void grp_arrive(unsigned lds_addr) {
  unsigned a, o;
  asm volatile("v_mov_b32 %1, 1\n\ts_waitcnt lgkmcnt(0)\n\tv_mov_b32 %0, %2\n\ts_mov_b64 exec, 1\n\tds_add_u32 %0, %1\n\ts_mov_b64 exec, -1"
               : "=&v"(a), "=&v"(o) : "s"(lds_addr) : "memory");
}
DI void grp_wait(unsigned lds_addr, unsigned target) {
  unsigned a, v, sv;
  asm volatile("s_nop 1\n\tv_mov_b32 %0, %3\n"
               "1:\n\tds_read_b32 %1, %0\n\ts_waitcnt lgkmcnt(0)\n\tv_readfirstlane_b32 %2, %1\n\ts_nop 1\n\ts_cmp_ge_u32 %2, %4\n\t"
               "s_cbranch_scc1 2f\n\ts_sleep 1\n\ts_branch 1b\n2:"
               : "=&v"(a), "=&v"(v), "=&s"(sv) : "s"(lds_addr), "s"(target) : "memory", "scc");
}
DI void grp_wait_peeked(unsigned lds_addr, unsigned target, unsigned peeked) {
  unsigned a, v, sv;
  asm volatile("s_nop 0\n\tv_readfirstlane_b32 %2, %5\n\tv_mov_b32 %0, %3\n\ts_cmp_ge_u32 %2, %4\n\ts_cbranch_scc1 2f\n"
               "1:\n\tds_read_b32 %1, %0\n\ts_waitcnt lgkmcnt(0)\n\tv_readfirstlane_b32 %2, %1\n\ts_nop 1\n\ts_cmp_ge_u32 %2, %4\n\t"
               "s_cbranch_scc1 2f\n\ts_sleep 1\n\ts_branch 1b\n2:"
               : "=&v"(a), "=&v"(v), "=&s"(sv) : "s"(lds_addr), "s"(target), "v"(peeked) : "memory", "scc");
}
template <int BASE> DI void set_prio_rem(unsigned rem, unsigned partner) {
  unsigned sv;
  asm volatile("s_nop 0\n\tv_readfirstlane_b32 %0, %2\n\ts_nop 0\n\ts_cmp_gt_u32 %1, %0\n\ts_cbranch_scc1 1f\n\ts_setprio %3\n\ts_branch 2f\n1:\n\ts_setprio %4\n2:"
               : "=&s"(sv) : "s"(rem), "v"(partner), "i"(BASE), "i"(BASE + 1) : "scc");
}
DI int swap23(int n) { return (n & 0x13) | ((n & 4) << 1) | ((n & 8) >> 1); }

#define SB() __builtin_amdgcn_sched_barrier(0)
#define MEMB() asm volatile("" ::: "memory")
#define UNROLL _Pragma("unroll")

__global__ __launch_bounds__(512, 2) NOPK void swin_kernel(const char* __restrict__ x, const char* __restrict__ wqkv_t,
                                                           const char* __restrict__ wo_t, const char* __restrict__ wqkv_s,
                                                           const char* __restrict__ wo_s, char* __restrict__ out, char* ws) {
  __shared__ __attribute__((aligned(16))) char smem[LDS_BYTES];
  const unsigned lds0 = (unsigned)(size_t)smem;
  const int lane = threadIdx.x & 63;
  const int wave = __builtin_amdgcn_readfirstlane(threadIdx.x >> 6);
  const int grp = wave >> 2, w = wave & 3;
  const int col = lane & 31, h = lane >> 5, p = col >> 2, t = col & 3;
  const unsigned l16 = (unsigned)lane * 16u;
  const bool prod = blockIdx.x < 8;
  const int bid = prod ? 0 : blockIdx.x - 8;
  char* const hflags = ws + (blockIdx.x & 7) * 8192;
  char* const hflag = hflags + ((blockIdx.x >> 3) - 1) * 256;
  char* const hdata = ws + 65536 + (blockIdx.x & 7) * 36864;
  const unsigned long long cmask = (1ull << ((NBLK + 7 - (blockIdx.x & 7)) >> 3)) - 1;
  const char* const wq = prod ? wqkv_s : wqkv_t;
  const char* const wo = prod ? wo_s : wo_t;
  const unsigned m1in = lane < 32 ? l16 + 1024u : ((l16 - 512u) ^ 16u) + RS_IN;
  const unsigned m1out = lane < 32 ? l16 + 1024u : ((l16 - 512u) ^ 16u) + RS_OUT;
#define POFF(i, lb, m1) ((i) == 0 ? (lb) : (i) == 1 ? (m1) : (i) == 2 ? ((lb) ^ 16u) : (i) == 3 ? ((lb) ^ 32u) : (i) == 4 ? ((m1) ^ 32u) : ((lb) ^ 48u))
#define PUNI(i, RS) ((i) == 0 ? 0u : (i) == 1 ? 0u : (i) == 2 ? (RS) + 512u : (i) == 3 ? 2u * (RS) : (i) == 4 ? 2u * (RS) : 3u * (RS) + 512u)
#define OPAQUE(v) asm volatile("" : "+v"(v))
#define PIN(v) asm volatile("" : "+v"(v))
#define PFULL(i, m1, RS) ((i) == 0 ? l16 : (i) == 1 ? (m1) : (i) == 2 ? xad<16>(l16, PUNI(2, RS)) : (i) == 3 ? xad<32>(l16, PUNI(3, RS)) : (i) == 4 ? xad<32>(m1, PUNI(4, RS)) : xad<48>(l16, PUNI(5, RS)))
  const __amdgpu_buffer_rsrc_t xrs = __builtin_amdgcn_make_buffer_rsrc((void*)x, 0, 38535168, 0x00020000);
  int win = bid + NBLK * grp;
  u32x4 xf[2][6];
  unsigned xb0;
  { int wy = win / 28, wx = win - wy * 28; xb0 = (unsigned)((wy * 8 + 2 * w) * 224 + wx * 8) * 192u; }
#define XLOAD(n) xf[(n) / 6][(n) % 6] = __builtin_amdgcn_raw_buffer_load_b128(xrs, POFF((n) % 6, l16, m1in), xb0 + ((n) / 6) * ROWB + PUNI((n) % 6, RS_IN), 2)

  if (threadIdx.x < 64) *(unsigned*)(smem + CNT_OFF + threadIdx.x * 4) = 0u;
#define CN(g, o) (lds0 + CNT_OFF + (g) * 64 + (o))
  const unsigned slot = SLOT_OFF + wave * SLOT_B;
  const unsigned cntA = CNT_OFF + grp * 64 + 32, cntB = cntA + 4;
  const float inv96 = 1.0f / 96.0f;

  const unsigned xa_e = slot + (unsigned)((t * 96 + 12 * p + (h ^ t)) * 16);
  const unsigned xa_o = slot + (unsigned)((t * 96 + 12 * p + ((2 + h) ^ t)) * 16);
  const unsigned t1adj = t < 2 ? 6144u : (unsigned)-3072;
  const unsigned sl16 = slot + l16;
#define XA(ks) ((((ks) & 1) ? xa_o : xa_e) + 64 * ((ks) >> 1))

  {
    constexpr unsigned QK = SLOT_OFF + 36864, VO = WF_OFF + 36864;
    const char* xb = x + xb0;
#define XFIRST()                                                                                                 \
  {                                                                                                              \
    UNROLL for (int i = 0; i < 6; ++i) dma16nt(lds0 + slot + i * 1024, xb + PUNI(i, RS_IN), POFF(i, l16, m1in)); \
    UNROLL for (int i = 0; i < 3; ++i) dma16nt(lds0 + slot + 6144 + i * 1024, xb + ROWB + PUNI(i, RS_IN), POFF(i, l16, m1in)); \
  }
    if (grp && w < 3) {
      unsigned qo[3], qf[3];
      UNROLL for (int m = 0; m < 3; ++m) {
        int idx = m * 64 + lane, row = idx / 24, qq = idx - row * 24;
        int c = qq ^ (row >> 1);
        qo[m] = (unsigned)(row * 576 + c * 16);
        qf[m] = (unsigned)(row * 576 + (c ^ 4) * 16);
      }
      UNROLL for (int i = 0; i < 4; ++i)
        UNROLL for (int m = 0; m < 3; ++m)
          dma16(lds0 + QK + (4 * w + i) * 3072 + m * 1024, wq + (4 * w + i) * 8 * 576, (i & 1) ? qf[m] : qo[m]);
    }
    if (wave == 0 && prod) flags_init(hflags, (unsigned)lane * 256u, cmask);
    if (wave == 3 && !prod) flag_st(hflag, 1u);
    wg_barrier();
    if (!grp && !prod) XFIRST();
    UNROLL for (int n = 9; n < 12; ++n) XLOAD(n);
    MEMB();
    if (wave == 3) {
      asm volatile("s_waitcnt vmcnt(12)" ::: "memory");
      grp_arrive(CN(0, 20));
    }
    const int pn = swap23(col);
    unsigned qb[4];
    UNROLL for (int e = 0; e < 4; ++e) qb[e] = QK + (unsigned)(pn * 384 + (((2 * e) | h) ^ ((pn >> 1) & 7)) * 16);
    UNROLL for (int e = 0; e < 4; ++e) OPAQUE(qb[e]);
#define FINISH(D, base)                                                                                          \
  {                                                                                                              \
    u32x4 pw[6];                                                                                                 \
    f32x4 cs4 = {0.f, 0.f, 0.f, 0.f};                                                                            \
    UNROLL for (int k = 0; k < 6; ++k) {                                                                         \
      pw[k] = PACKS(D[k >> 1], k & 1);                                                                           \
      cs4 = mfma4(0x3F803F80u, 0x3F803F80u, pw[k][0], pw[k][1], cs4);                                            \
      cs4 = mfma4(0x3F803F80u, 0x3F803F80u, pw[k][2], pw[k][3], cs4);                                            \
      if (k > 0) lds_st(smem, (base) + (w * 6 + k) * 1024 + l16, pw[k]);                                         \
    }                                                                                                            \
    const float res = xhalf_add(cs4[0]) * 0.5f;                                                                  \
    pw[0][0] = pk(fbits(pw[0][0] << 16) - res, D[0][1]);                                                         \
    lds_st(smem, (base) + (w * 6) * 1024 + l16, pw[0]);                                                          \
  }
#define QKF(rt, c2) lds_ld(smem, qb[(c2) & 3] + (rt) * 12288 + ((c2) >> 2) * 128)
#define QKR(ro, c2) lds_ld(smem, qb[(c2) & 3] + (ro) + ((c2) >> 2) * 128)
    if (wave == 7) {
      unsigned vv[3];
      UNROLL for (int m = 0; m < 3; ++m) {
        int idx = m * 64 + lane, row2 = idx / 12, q2 = idx - row2 * 12;
        vv[m] = (unsigned)(row2 * 576 + 384 + (q2 ^ (row2 >> 2)) * 16);
      }
      for (int i = 0; i < 6; ++i) {
        UNROLL for (int m = 0; m < 3; ++m) dma16(lds0 + VO + i * 3072 + m * 1024, wo + i * 3072 + m * 1024, l16);
        UNROLL for (int m = 0; m < 3; ++m) dma16(lds0 + VO + 18432 + i * 3072 + m * 1024, wq + i * 16 * 576, vv[m]);
      }
      wait_vm0();
      grp_arrive(CN(0, 4));
      if (!prod) {
        grp_wait(CN(0, 16), 6);
        XFIRST();
      }
    } else if (w < 3) {
      const unsigned cn = lds0 + CNT_OFF;
      if (grp) {
        asm volatile("s_waitcnt vmcnt(3)" ::: "memory");
        grp_arrive(cn + 0);
      }
      grp_wait(cn + 0, 3);
      if (grp) {
        __builtin_amdgcn_s_setprio(3);
        {
          u32x4 af[3][6], bb[6];
          UNROLL for (int kk = 0; kk < 6; ++kk) { bb[kk] = QKR(w * 12288, kk); af[0][kk] = QKF(0, 6 + kk); }
          UNROLL for (int ct = 1; ct < 3; ++ct)
            UNROLL for (int kk = 0; kk < 6; ++kk) af[ct][kk] = QKF(ct, 6 + kk);
          SB();
          f32x16 d = {};
          UNROLL for (int ct = 0; ct < 3; ++ct)
            UNROLL for (int kk = 0; kk < 6; ++kk) d = mfma32(af[ct][kk], bb[kk], d);
          float r0 = (d[0] + d[1]) + (d[2] + d[3]), r1 = (d[4] + d[5]) + (d[6] + d[7]);
          float r2_ = (d[8] + d[9]) + (d[10] + d[11]), r3 = (d[12] + d[13]) + (d[14] + d[15]);
          float rs = xhalf_add((r0 + r1) + (r2_ + r3));
          *(float*)(smem + RP_OFF + (32 * w + pn) * 4) = rs * -inv96;
          grp_arrive(cn + 8);
          grp_arrive(cn + 16);
        }
        __builtin_amdgcn_s_setprio(1);
        unsigned vb[2];
        UNROLL for (int e = 0; e < 2; ++e) vb[e] = VO + (unsigned)(18432 + pn * 192 + (((2 * e) | h) ^ ((pn >> 2) & 3)) * 16);
        const unsigned tb = VO + (unsigned)((8 * h + ((lane & 15) >> 2)) * 192 + (16 * ((lane >> 4) & 1) + 8 * (lane & 1) + 4 * ((lane >> 1) & 1)) * 2 + w * 64);
        if (!prod) {
          grp_wait(cn + 16, 6);
          XFIRST();
        }
        grp_wait(cn + 4, 1);
#define VF(rt, kk) lds_ld(smem, vb[(kk) & 1] + (rt) * 6144 + ((kk) >> 1) * 64)
        u32x4 bfr[6], av[3][6];
        UNROLL for (int kk = 0; kk < 6; ++kk) {
          s16x4 lo = __builtin_amdgcn_ds_read_tr16_b64_v4i16((LAS s16x4*)(smem + tb + kk * 3072));
          s16x4 hi = __builtin_amdgcn_ds_read_tr16_b64_v4i16((LAS s16x4*)(smem + tb + kk * 3072 + 768));
          u32x2 l2 = __builtin_bit_cast(u32x2, lo), h2 = __builtin_bit_cast(u32x2, hi);
          bfr[kk] = u32x4{l2[0], l2[1], h2[0], h2[1]};
          av[0][kk] = VF(0, kk);
        }
        UNROLL for (int rt = 1; rt < 3; ++rt)
          UNROLL for (int kk = 0; kk < 6; ++kk) av[rt][kk] = VF(rt, kk);
        SB();
        f32x16 DB[3];
        float c0 = 0.f, c1 = 0.f;
        UNROLL for (int rt = 0; rt < 3; ++rt) {
          f32x16 d = {};
          UNROLL for (int kk = 0; kk < 6; ++kk) d = mfma32(av[rt][kk], bfr[kk], d);
          DB[rt] = d;
          if (rt == 0) grp_arrive(cn + 12);
          if (rt > 0) { UNROLL for (int i = 0; i < 16; i += 2) { c0 += DB[rt - 1][i]; c1 += DB[rt - 1][i + 1]; } }
          SB();
        }
        UNROLL for (int i = 0; i < 16; i += 2) { c0 += DB[2][i]; c1 += DB[2][i + 1]; }
        const float cm = xhalf_add(c0 + c1) * inv96;
        UNROLL for (int rt = 0; rt < 3; ++rt)
          UNROLL for (int i = 0; i < 16; ++i) DB[rt][i] -= cm;
        FINISH(DB, WF_OFF + 18432);
        grp_arrive(cn + 24);
#undef VF
        __builtin_amdgcn_s_setprio(0);
      } else {
        __builtin_amdgcn_s_setprio(2);
        f32x16 DA[3];
        float a0 = 0.f, a1 = 0.f;
        {
          u32x4 bb[6], af[3][6];
          UNROLL for (int kk = 0; kk < 6; ++kk) { bb[kk] = QKR(w * 12288, 6 + kk); af[0][kk] = QKF(0, kk); }
          UNROLL for (int rt = 1; rt < 3; ++rt)
            UNROLL for (int kk = 0; kk < 6; ++kk) af[rt][kk] = QKF(rt, kk);
          SB();
          UNROLL for (int rt = 0; rt < 3; ++rt) {
            f32x16 d = {};
            UNROLL for (int kk = 0; kk < 6; ++kk) d = mfma32(af[rt][kk], bb[kk], d);
            DA[rt] = d;
            if (rt == 0) grp_arrive(cn + 16);
            if (rt > 0) { UNROLL for (int i = 0; i < 16; i += 2) { a0 += DA[rt - 1][i]; a1 += DA[rt - 1][i + 1]; } }
            SB();
          }
          UNROLL for (int i = 0; i < 16; i += 2) { a0 += DA[2][i]; a1 += DA[2][i + 1]; }
        }
        grp_wait(cn + 8, 3);
        f32x4 rm[12];
        UNROLL for (int n = 0; n < 12; ++n) rm[n] = *(const f32x4*)(smem + RP_OFF + (32 * (n >> 2) + 16 * ((n >> 1) & 1) + 8 * h + 4 * (n & 1)) * 4);
        UNROLL for (int n = 0; n < 12; ++n) {
          a0 += rm[n][0] + rm[n][2]; a1 += rm[n][1] + rm[n][3];
          UNROLL for (int i = 0; i < 4; ++i) DA[n >> 2][4 * (n & 3) + i] += rm[n][i];
        }
        const float fA = 0.10206207261596575f * 1.4426950408889634f;
        const float cadd = xhalf_add(a0 + a1) * (-fA * inv96);
        UNROLL for (int rt = 0; rt < 3; ++rt)
          UNROLL for (int i = 0; i < 16; ++i) DA[rt][i] = __builtin_fmaf(DA[rt][i], fA, cadd);
        FINISH(DA, WF_OFF);
        grp_arrive(cn + 20);
      }
    }
#undef FINISH
#undef QKF
#undef QKR
#undef XFIRST
    if (prod) {
      grp_wait(CN(0, 20), 4);
      grp_wait(CN(0, 24), 3);
      const __amdgpu_buffer_rsrc_t hrs = __builtin_amdgcn_make_buffer_rsrc(hdata, 0, 36864, 0x00020000);
      UNROLL for (int i = 0; i < 5; ++i) {
        const unsigned o = (unsigned)(wave + 8 * i) * 1024u + l16;
        __builtin_amdgcn_raw_buffer_store_b128(lds_ld(smem, WF_OFF + o), hrs, o, 0, 16);
      }
      wait_vm0();
      wg_barrier();
      if (wave == 0) flags_publish(hflags, (unsigned)lane * 256u, cmask);
      return;
    }
  }
  wait_vm0();
  UNROLL for (int ks = 0; ks < 6; ++ks) xf[0][ks] = lds_ld(smem, XA(ks));
  MEMB();
  UNROLL for (int i = 0; i < 3; ++i) lds_st(smem, sl16 + i * 1024, xf[1][3 + i]);
  MEMB();
  UNROLL for (int ks = 0; ks < 6; ++ks) xf[1][ks] = lds_ld(smem, t1adj + XA(ks));
  MEMB();

  const bool own = (t == w);
  const unsigned exo = (unsigned)(h * 384 + p * 48 + ((t ^ w) - 1) * 16);
  const unsigned ex_w = own ? (unsigned)(DUM_OFF + h * 128 + ((3 * (p & 6) + 6 + (p & 1)) & 7) * 16)
                            : SLOT_OFF + (grp * 4 + t) * SLOT_B + exo;
  const unsigned ex_r = slot + exo;
  const unsigned long long notown = ~(0x1111111111111111ull << w);
  const unsigned rbase = R_OFF + wave * 256;
  unsigned wS = l16 + 36864u;
  asm volatile("" : "+v"(wS));
#define WLD(i) lds_ld(smem, ((i) < 36 ? l16 + (i) * 1024 : wS + ((i) - 36) * 1024))

  const unsigned ONES = 0x3F803F80u;
  const unsigned id0 = t == 0 ? 0x00003F80u : (t == 1 ? 0x3F800000u : 0u);
  const unsigned id1 = t == 2 ? 0x00003F80u : (t == 3 ? 0x3F800000u : 0u);
  unsigned msk[4];
  UNROLL for (int g = 0; g < 4; ++g) msk[g] = ((col >> 3) == g && h == (p & 1)) ? 0xffffffffu : 0u;
  const f32x4 z4 = {0.f, 0.f, 0.f, 0.f};
  const f32x16 zz = {};

  grp_wait(CN(0, 20), 4);

  u32x4 wb[4];
  wb[2] = WLD(0); wb[3] = WLD(1); wb[0] = WLD(2);
  u32x4 x1[2][6];
  UNROLL for (int a = 0; a < 2; ++a) UNROLL for (int i = 0; i < 6; ++i) x1[a][i] = wb[0];
  unsigned iter = 1;

#define OSTORE(n_) __builtin_amdgcn_raw_buffer_store_b128(x1[(n_) / 6][(n_) % 6], (n_) < 6 ? ors0 : ors1, PFULL((n_) % 6, m1out, RS_OUT), 0, 18)
#define LNFIN1(g0, g1, q0, q1)                                                                                    \
  {                                                                                                              \
    PIN(g0); PIN(g1);                                                                                            \
    float q0a = (t & 1) ? g0[1] : g0[0], q0b = (t & 1) ? g0[3] : g0[2];                                          \
    float q1a = (t & 1) ? g1[1] : g1[0], q1b = (t & 1) ? g1[3] : g1[2];                                          \
    q0 = (t & 2) ? q0b : q0a; q1 = (t & 2) ? q1b : q1a;                                                          \
  }
#define LNFIN2(sm0, sm1, q0, q1, r)                                                                              \
  {                                                                                                              \
    PIN(q0);                                                                                                     \
    float mean = merge_add(sm0[0], sm1[0]) * inv96;                                                              \
    float var = merge_add(q0, q1) * inv96 - mean * mean;                                                         \
    var = __builtin_fmaxf(var, 0.f);                                                                             \
    r = __builtin_amdgcn_rsqf(var + 1e-5f);                                                                      \
  }
#define T1_FILL(g)                                                                                               \
  {                                                                                                              \
    if ((g) < 3) { MEMB(); x1[0][2 * (g)] = lds_ld(smem, sl16 + (2 * (g)) * 1024); x1[0][2 * (g) + 1] = lds_ld(smem, sl16 + (2 * (g) + 1) * 1024); MEMB(); } \
    if ((g) >= 3 && (g) < 9) lds_st(smem, XA((g) - 3), x1[1][(g) >= 3 && (g) < 9 ? (g) - 3 : 0]);                \
    if ((g) >= 9 && (g) < 12) { const int k_ = (g) >= 9 && (g) < 12 ? 2 * ((g) - 9) : 0; MEMB(); x1[1][k_] = lds_ld(smem, sl16 + k_ * 1024); x1[1][k_ + 1] = lds_ld(smem, sl16 + (k_ + 1) * 1024); MEMB(); } \
    if ((g) == 10) LNFIN1(g0, g1, q0, q1);                                                                       \
    if ((g) == 12) LNFIN2(sm0, sm1, q0, q1, rr);                                                                 \
    if ((g) >= 13 && ((g) & 1)) OSTORE(((g) - 13) / 2);                                                          \
    if (((g) >= 14 && (g) <= 20 && !((g) & 1)) || ((g) >= 26 && (g) <= 32 && !((g) & 1))) {                      \
      const int tp_ = (g) >= 26 ? 1 : 0, k_ = (((g) - 14) % 12) / 2, a_ = (k_ >> 1) & 1, s_ = k_ & 1;           \
      PIN(acc[tp_][a_]);                                                                                         \
      uf[a_][2 * tp_ + s_] = PACKS(acc[tp_][a_], s_);                                                            \
    }                                                                                                            \
    if ((g) == 22) grp_arrive(lds0 + cntB);                                                                      \
    if ((g) == 24) { MEMB(); b1v = *(const unsigned*)(smem + CNT_OFF + 24); }                                    \
    if ((g) == 28) grp_wait_peeked(CN(0, 24), 3, b1v);                                                           \
  }
#define T1_STAGE()                                                                                               \
  {                                                                                                              \
    f32x4 sm0 = z4, sm1 = z4, g0 = z4, g1 = z4;                                                                  \
    unsigned b1v;                                                                                                \
    float q0, q1;                                                                                                \
    UNROLL for (int ks = 0; ks < 6; ++ks) {                                                                      \
      lds_st(smem, XA(ks), x1[0][ks]);                                                                           \
      UNROLL for (int c = 0; c < 2; ++c) {                                                                       \
        sm0 = mfma4(ONES, ONES, xf[0][ks][2 * c], xf[0][ks][2 * c + 1], sm0);                                    \
        sm1 = mfma4(ONES, ONES, xf[1][ks][2 * c], xf[1][ks][2 * c + 1], sm1);                                    \
        g0 = mfma4(xf[0][ks][2 * c], xf[0][ks][2 * c + 1], xf[0][ks][2 * c], xf[0][ks][2 * c + 1], g0);          \
        g1 = mfma4(xf[1][ks][2 * c], xf[1][ks][2 * c + 1], xf[1][ks][2 * c], xf[1][ks][2 * c + 1], g1);          \
      }                                                                                                          \
      SB();                                                                                                      \
    }                                                                                                            \
    UNROLL for (int i = 0; i < 18; ++i) {                                                                        \
      const int tt = i / 6, ks = i % 6;                                                                          \
      acc[tt & 1][0] = mfma32(wb[(i + 2) & 3], xf[0][ks], ks == 0 ? zz : acc[tt & 1][0]);                        \
      SB();                                                                                                      \
      wb[(i + 5) & 3] = WLD(i + 3);                                                                              \
      T1_FILL(2 * i);                                                                                            \
      SB();                                                                                                      \
      acc[tt & 1][1] = mfma32(wb[(i + 2) & 3], xf[1][ks], ks == 0 ? zz : acc[tt & 1][1]);                        \
      SB();                                                                                                      \
      T1_FILL(2 * i + 1);                                                                                        \
      SB();                                                                                                      \
    }                                                                                                            \
  }

  unsigned rem = (unsigned)((784 - win + 2 * NBLK - 1) / (2 * NBLK)) * 90u + (unsigned)grp;
  unsigned prem = 0;
#define STAGE(BASE, WGT)                                                                                         \
  {                                                                                                              \
    set_prio_rem<BASE>(rem, prem);                                                                               \
    MEMB();                                                                                                      \
    *(unsigned*)(smem + CNT_OFF + 128 + wave * 4) = rem;                                                                \
    prem = *(const unsigned*)(smem + CNT_OFF + 128 + (wave ^ 4) * 4);                                                   \
    MEMB();                                                                                                      \
    rem -= (WGT);                                                                                                \
  }
  unsigned ob = 0;
  for (;;) {
    float rr;
    u32x4 uf[2][6];
    f32x16 acc[2][2];
    STAGE(0, 16);
    const __amdgpu_buffer_rsrc_t ors0 = __builtin_amdgcn_make_buffer_rsrc(out + (size_t)ob, 0, iter > 1 ? 0x41000 : 0, 0x00020000);
    const __amdgpu_buffer_rsrc_t ors1 = __builtin_amdgcn_make_buffer_rsrc(out + (size_t)(ob + ROWB), 0, iter > 1 ? 0x41000 : 0, 0x00020000);
    T1_STAGE();
    STAGE(2, 20);
    u32x4 pt[2][2];
    unsigned a2v, fpk;
    const unsigned fon = (unsigned)__builtin_amdgcn_readfirstlane(!grp && iter == 1);
    const __amdgpu_buffer_rsrc_t frs = __builtin_amdgcn_make_buffer_rsrc(hflag, 0, fon * 4, 0x00020000);
    const int nwin = win + 2 * NBLK;
    const bool more = nwin < 784;
    const char* xb;
    unsigned pfon;
#define NEXTADDR()                                                                                               \
  {                                                                                                              \
    int nw = more ? nwin : win; int wy = nw / 28, wx = nw - wy * 28;                                             \
    xb = x + (unsigned)((wy * 8 + 2 * w) * 224 + wx * 8) * 192u;                                                 \
    pfon = (unsigned)__builtin_amdgcn_readfirstlane(more ? 0x7fffffff : 0);                                      \
    asm volatile("" : "+s"(xb), "+s"(pfon));                                                                     \
  }
    {
      f32x4 s0, s1, s0b, s1b;
      f32x16 vac[2][2];
      f32x16 oac[2];
      u32x4 vpk[2][2];
      float rs0, rs1, rs2, rs3, y0, y1, y2, y3, e0, e1, e2, e3;
      unsigned cbv;
      unsigned pa0, pa1, pb0, pb1;
      float sinv;
#define O_INIT(tt)                                                                                               \
  UNROLL for (int a = 0; a < 2; ++a) {                                                                           \
    f32x16 o;                                                                                                    \
    UNROLL for (int g = 0; g < 4; ++g) {                                                                         \
      f32x4 r4 = mfma4(id0, id1, xf[a][2 * (tt) + (g >> 1)][2 * (g & 1)], xf[a][2 * (tt) + (g >> 1)][2 * (g & 1) + 1], z4); \
      UNROLL for (int e = 0; e < 4; ++e) o[4 * g + e] = r4[e];                                                   \
    }                                                                                                            \
    oac[a] = o;                                                                                                  \
  }
#define O_STEP(tt)                                                                                               \
  O_INIT(tt);                                                                                                    \
  SB();                                                                                                          \
  oac[0] = mfma32(vpk[0][0], pt[0][0], oac[0]);                                                                  \
  oac[0] = mfma32(vpk[0][1], pt[0][1], oac[0]);                                                                  \
  oac[1] = mfma32(vpk[1][0], pt[1][0], oac[1]);                                                                  \
  oac[1] = mfma32(vpk[1][1], pt[1][1], oac[1]);
#define S2_STEP(tt, a, s)                                                                                        \
  PIN(oac[a]);                                                                                                   \
  x1[a][2 * (tt) + (s)] = PACKS(oac[a], s);                                                                      \
  lds_st(smem, ex_w + ((a) * 6 + 2 * (tt) + (s)) * 768, x1[a][2 * (tt) + (s)]);
#define VP_STEP(tt, k) { PIN(vac[(tt) & 1][(k) >> 1]); vpk[(k) >> 1][(k) & 1] = PACKS(vac[(tt) & 1][(k) >> 1], (k) & 1); }
#define T2_FILL(g)                                                                                               \
  {                                                                                                              \
    if ((g) >= 1 && (g) <= 4) { const int k_ = (g) >= 1 && (g) <= 4 ? (g) - 1 : 0; PIN(acc[0][k_ >> 1]); uf[k_ >> 1][4 + (k_ & 1)] = PACKS(acc[0][k_ >> 1], k_ & 1); } \
    if ((g) == 5) {                                                                                              \
      s0 = z4; s1 = z4; s0b = z4; s1b = z4;                                                                      \
      UNROLL for (int k2 = 0; k2 < 6; ++k2) {                                                                    \
        s0 = mfma4(xf[0][k2][0], xf[0][k2][1], uf[0][k2][0], uf[0][k2][1], s0);                                  \
        s1 = mfma4(xf[1][k2][0], xf[1][k2][1], uf[1][k2][0], uf[1][k2][1], s1);                                  \
        s0b = mfma4(xf[0][k2][2], xf[0][k2][3], uf[0][k2][2], uf[0][k2][3], s0b);                                \
        s1b = mfma4(xf[1][k2][2], xf[1][k2][3], uf[1][k2][2], uf[1][k2][3], s1b);                                \
      }                                                                                                          \
      rs0 = quad_bcast<0>(rr); rs1 = quad_bcast<1>(rr); rs2 = quad_bcast<2>(rr); rs3 = quad_bcast<3>(rr);        \
    }                                                                                                            \
    if ((g) == 6) { PIN(s0); PIN(s1); UNROLL for (int e = 0; e < 4; ++e) { s0[e] += s0b[e]; } s1[0] += s1b[0]; s1[1] += s1b[1]; } \
    if ((g) == 7) { PIN(s1); s1[2] += s1b[2]; s1[3] += s1b[3]; y0 = merge_add(s0[0], s1[0]) * rs0; }             \
    if ((g) == 8) { PIN(s0); y1 = merge_add(s0[1], s1[1]) * rs1; y2 = merge_add(s0[2], s1[2]); }                 \
    if ((g) == 9) { PIN(s0); y2 *= rs2; y3 = merge_add(s0[3], s1[3]) * rs3; }                                    \
    if ((g) == 10) {                                                                                             \
      PIN(y3);                                                                                                   \
      float mx = __builtin_fmaxf(__builtin_fmaxf(y0, y1), __builtin_fmaxf(y2, y3));                              \
      float cc = -rr * mx;                                                                                       \
      y0 = __builtin_fmaf(y0, rr, cc); y1 = __builtin_fmaf(y1, rr, cc);                                          \
      y2 = __builtin_fmaf(y2, rr, cc); y3 = __builtin_fmaf(y3, rr, cc);                                          \
    }                                                                                                            \
    if ((g) == 11) { PIN(y0); e0 = __builtin_amdgcn_exp2f(y0); e1 = __builtin_amdgcn_exp2f(y1); e2 = __builtin_amdgcn_exp2f(y2); } \
    if ((g) == 12) { PIN(y3); e3 = __builtin_amdgcn_exp2f(y3); sinv = __builtin_amdgcn_rcpf((e0 + e1) + (e2 + e3)); } \
    if ((g) == 13) { PIN(e0); e0 *= rs0; e1 *= rs1; e2 *= rs2; e3 *= rs3; e0 *= sinv; e1 *= sinv; }              \
    if ((g) == 14) {                                                                                             \
      PIN(e2);                                                                                                   \
      unsigned p01 = pk(e0, e1), p23 = pk(e2 * sinv, e3 * sinv);                                                 \
      auto a_ = __builtin_amdgcn_permlane32_swap(p01, p01, false, false);                                        \
      auto b_ = __builtin_amdgcn_permlane32_swap(p23, p23, false, false);                                        \
      pa0 = a_[0]; pa1 = a_[1]; pb0 = b_[0]; pb1 = b_[1];                                                        \
    }                                                                                                            \
    if ((g) == 15) { PIN(pa0); pt[0][0] = u32x4{pa0 & msk[0], pb0 & msk[0], pa0 & msk[1], pb0 & msk[1]}; { MEMB(); cbv = *(const unsigned*)(smem + cntB); } } \
    if ((g) == 16) { PIN(pa0); pt[0][1] = u32x4{pa0 & msk[2], pb0 & msk[2], pa0 & msk[3], pb0 & msk[3]}; }       \
    if ((g) == 17) { PIN(pa1); pt[1][0] = u32x4{pa1 & msk[0], pb1 & msk[0], pa1 & msk[1], pb1 & msk[1]}; }       \
    if ((g) == 18) { PIN(pa1); pt[1][1] = u32x4{pa1 & msk[2], pb1 & msk[2], pa1 & msk[3], pb1 & msk[3]}; }       \
    if ((g) >= 19 && (g) <= 22) VP_STEP(0, (g) >= 19 && (g) <= 22 ? (g) - 19 : 0);                               \
    if ((g) == 23) { grp_wait_peeked(lds0 + cntB, 4 * iter, cbv); O_STEP(0); }                                   \
    if ((g) >= 25 && (g) <= 28) { const int k_ = (g) >= 25 && (g) <= 28 ? (g) - 25 : 0; S2_STEP(0, k_ >> 1, k_ & 1); } \
    if ((g) >= 29 && (g) <= 32) VP_STEP(1, (g) >= 29 && (g) <= 32 ? (g) - 29 : 0);                               \
    if ((g) == 12) fpk = __builtin_amdgcn_raw_buffer_load_b32(frs, 0, 0, 16);                               \
    if ((g) == 24) fetch_s(fon, fpk, hflag, hdata + w * 9216, lds0 + WF_OFF + 36864 + w * 9216, l16);            \
    if ((g) == 30) { MEMB(); a2v = *(const unsigned*)(smem + CNT_OFF + 64 + 20); }                               \
    if ((g) == 33) { O_STEP(1); }                                                                                \
    if ((g) == 35) { S2_STEP(1, 0, 0); S2_STEP(1, 0, 1); }                                                       \
  }
      UNROLL for (int j = 0; j < 18; ++j) {
        const int tt = j / 6, ks = j % 6;
        vac[tt & 1][0] = mfma32(xf[0][ks], wb[(j + 20) & 3], ks == 0 ? zz : vac[tt & 1][0]);
        SB();
        if (j < 15) wb[(j + 23) & 3] = WLD(j + 18 + 3);
        T2_FILL(2 * j);
        SB();
        vac[tt & 1][1] = mfma32(xf[1][ks], wb[(j + 20) & 3], ks == 0 ? zz : vac[tt & 1][1]);
        SB();
        T2_FILL(2 * j + 1);
        SB();
      }
      S2_STEP(1, 1, 0); S2_STEP(1, 1, 1);
      SB();
      O_INIT(2);
      VP_STEP(2, 0); VP_STEP(2, 1);
      SB();
      oac[0] = mfma32(vpk[0][0], pt[0][0], oac[0]);
      oac[0] = mfma32(vpk[0][1], pt[0][1], oac[0]);
      SB();
      VP_STEP(2, 2); VP_STEP(2, 3);
      SB();
      oac[1] = mfma32(vpk[1][0], pt[1][0], oac[1]);
      oac[1] = mfma32(vpk[1][1], pt[1][1], oac[1]);
      SB();
      NEXTADDR();
      S2_STEP(2, 0, 0); S2_STEP(2, 0, 1);
      SB();
      S2_STEP(2, 1, 0); S2_STEP(2, 1, 1);
#undef O_INIT
#undef VP_STEP
#undef T2_FILL
#undef O_STEP
#undef S2_STEP
    }
    grp_arrive(lds0 + cntA);
    drain_arrive(fon, CN(1, 20));
    grp_wait_peeked(CN(1, 20), 4, a2v);
    wb[0] = WLD(36); wb[1] = WLD(37); wb[2] = WLD(38);
    grp_wait(lds0 + cntA, 4 * iter);
    asm volatile("s_mov_b64 exec, %13\n\t"
                 "ds_read_b128 %0, %12\n\tds_read_b128 %1, %12 offset:768\n\tds_read_b128 %2, %12 offset:1536\n\t"
                 "ds_read_b128 %3, %12 offset:2304\n\tds_read_b128 %4, %12 offset:3072\n\tds_read_b128 %5, %12 offset:3840\n\t"
                 "ds_read_b128 %6, %12 offset:4608\n\tds_read_b128 %7, %12 offset:5376\n\tds_read_b128 %8, %12 offset:6144\n\t"
                 "ds_read_b128 %9, %12 offset:6912\n\tds_read_b128 %10, %12 offset:7680\n\tds_read_b128 %11, %12 offset:8448\n\t"
                 "s_mov_b64 exec, -1\n\ts_waitcnt lgkmcnt(0)"
                 : "+v"(x1[0][0]), "+v"(x1[0][1]), "+v"(x1[0][2]), "+v"(x1[0][3]), "+v"(x1[0][4]), "+v"(x1[0][5]),
                   "+v"(x1[1][0]), "+v"(x1[1][1]), "+v"(x1[1][2]), "+v"(x1[1][3]), "+v"(x1[1][4]), "+v"(x1[1][5])
                 : "v"(lds0 + ex_r), "s"(notown) : "memory");
    float rq[2];
    const unsigned rka = rbase + h * 16;
#define RKLD(G) (*(const f32x4*)(smem + rka + (G) * 32))
    f32x4 rkb[3];
    STAGE(0, 14);
    const __amdgpu_buffer_rsrc_t nrs = __builtin_amdgcn_make_buffer_rsrc((void*)(xb + ROWB), 0, pfon, 0x00020000);
    u32x4 pre[3];
    SB();
#define SW(k) ((k) < 18 ? 36 + (k) : ((k) < 54 ? 18 + (k) : (k) - 54))
    u32x4 pf[2][4];
    u32x4 vp[3][4];
    f32x16 sa[2][2];
    u32x4 uq[6];
    f32x16 ua[2];
    float mxv, ccv, l0, l1, invv;
#define U_STEP(q, i)                                                                                             \
  {                                                                                                              \
    const int tt_ = (i) / 6, ks_ = (i) % 6, k_ = 18 * (q) + (i);                                                 \
    ua[tt_ & 1] = mfma32(wb[k_ & 3], x1[q][ks_], ks_ == 0 ? zz : ua[tt_ & 1]);                                   \
    SB();                                                                                                        \
    wb[(k_ + 3) & 3] = WLD(SW(k_ + 3));                                                                          \
    if (tt_ > 0 && (ks_ == 2 || ks_ == 3)) { PIN(ua[(tt_ + 1) & 1]); uq[2 * (tt_ - 1) + (ks_ - 2)] = PACKS(ua[(tt_ + 1) & 1], ks_ - 2); } \
  }
#define S_STEP(q, ks_)                                                                                           \
  {                                                                                                              \
    sa[q][0] = mfma32(x1[0][ks_], uq[ks_], (ks_) == 0 ? zz : sa[q][0]);                                          \
    SB();                                                                                                        \
    if ((ks_) == 1) { PIN(ua[0]); uq[4] = PACKS(ua[0], 0); }                                                     \
    if ((ks_) == 2) { PIN(ua[0]); uq[5] = PACKS(ua[0], 1); }                                                     \
    SFILL;                                                                                                       \
    SB();                                                                                                        \
    sa[q][1] = mfma32(x1[1][ks_], uq[ks_], (ks_) == 0 ? zz : sa[q][1]);                                          \
    SB();                                                                                                        \
  }
#define SFILL
#define P1(q, G)                                                                                                 \
  {                                                                                                              \
    const int n_ = 16 * (q) + (G);                                                                               \
    MEMB(); rkb[(n_ + 2) % 3] = RKLD(((G) + 2) & 7);                                                             \
    UNROLL for (int e = 0; e < 4; ++e) sa[q][(G) >> 2][4 * ((G) & 3) + e] *= rkb[n_ % 3][e];                     \
    mxv = (G) == 0 ? sa[q][0][0] : __builtin_fmaxf(mxv, sa[q][(G) >> 2][4 * ((G) & 3)]);                         \
    mxv = __builtin_fmaxf(mxv, sa[q][(G) >> 2][4 * ((G) & 3) + 1]);                                              \
    mxv = __builtin_fmaxf(__builtin_fmaxf(mxv, sa[q][(G) >> 2][4 * ((G) & 3) + 2]), sa[q][(G) >> 2][4 * ((G) & 3) + 3]); \
  }
#define X1(q) { mxv = xhalf_max(mxv); ccv = -rq[q] * mxv; l0 = 0.f; l1 = 0.f; }
#define P2H(q, H)                                                                                                \
  {                                                                                                              \
    const int b_ = (H) >> 3, i_ = 2 * ((H) & 7), G_ = (H) >> 1, n_ = 16 * (q) + 8 + G_;                          \
    if (((H) & 1) == 0 && n_ < 30) { MEMB(); rkb[(n_ + 2) % 3] = RKLD((G_ + 2) & 7); }                           \
    const float ea = __builtin_amdgcn_exp2f(__builtin_fmaf(sa[q][b_][i_], rq[q], ccv));                          \
    const float eb = __builtin_amdgcn_exp2f(__builtin_fmaf(sa[q][b_][i_ + 1], rq[q], ccv));                      \
    l0 += ea; l1 += eb;                                                                                          \
    sa[q][b_][i_] = ea * rkb[n_ % 3][2 * ((H) & 1)]; sa[q][b_][i_ + 1] = eb * rkb[n_ % 3][2 * ((H) & 1) + 1];    \
  }
#define X2(q) { invv = __builtin_amdgcn_rcpf(xhalf_add(l0 + l1)); }
#define P3(q, F)                                                                                                 \
  {                                                                                                              \
    UNROLL for (int e = 0; e < 8; ++e) sa[q][(F) >> 1][8 * ((F) & 1) + e] *= invv;                               \
    pf[q][F] = PACKS(sa[q][(F) >> 1], (F) & 1);                                                                  \
  }
    {
      f32x4 sm0 = z4, sm1 = z4, g0 = z4, g1 = z4;
      UNROLL for (int ks = 0; ks < 6; ++ks) {
        UNROLL for (int c = 0; c < 2; ++c) {
          sm0 = mfma4(ONES, ONES, x1[0][ks][2 * c], x1[0][ks][2 * c + 1], sm0);
          sm1 = mfma4(ONES, ONES, x1[1][ks][2 * c], x1[1][ks][2 * c + 1], sm1);
          g0 = mfma4(x1[0][ks][2 * c], x1[0][ks][2 * c + 1], x1[0][ks][2 * c], x1[0][ks][2 * c + 1], g0);
          g1 = mfma4(x1[1][ks][2 * c], x1[1][ks][2 * c + 1], x1[1][ks][2 * c], x1[1][ks][2 * c + 1], g1);
        }
        SB();
      }
      float q0, q1;
      UNROLL for (int i = 0; i < 18; ++i) {
        U_STEP(0, i);
        if (i == 1) LNFIN1(g0, g1, q0, q1);
        if (i == 2) {
          float r2;
          LNFIN2(sm0, sm1, q0, q1, r2);
          *(float*)(smem + rbase + lane * 4) = r2;
          auto rb = __builtin_amdgcn_permlane32_swap(ubits(r2), ubits(r2), false, false);
          rq[0] = fbits(rb[0]); rq[1] = fbits(rb[1]);
          MEMB();
        }
        {
          const int n = i < 8 ? i - 3 : (i < 10 ? -1 : (i < 14 ? i - 5 : -1));
          if (n >= 0 && n < 6) dma1ntc(lds0 + slot + n * 1024, xb, PFULL(n >= 0 && n < 6 ? n : 0, m1in, RS_IN), pfon);
          if (n >= 6) dma1ntc(lds0 + slot + n * 1024, xb + ROWB, PFULL(n >= 6 ? n - 6 : 0, m1in, RS_IN), pfon);
        }
        if (i >= 16) pre[i - 16] = __builtin_amdgcn_raw_buffer_load_b128(nrs, PFULL(i >= 16 ? i - 13 : 0, m1in, RS_IN), 0, 2);
        SB();
      }
    }
    UNROLL for (int ks = 0; ks < 6; ++ks) {
      S_STEP(0, ks);
      if (ks == 3) pre[2] = __builtin_amdgcn_raw_buffer_load_b128(nrs, PFULL(5, m1in, RS_IN), 0, 2);
      if (ks == 4) { rkb[0] = RKLD(0); rkb[1] = RKLD(1); }
      SB();
    }
    STAGE(2, 30);
    UNROLL for (int i = 0; i < 18; ++i) {
      U_STEP(1, i);
      if (i >= 1 && i <= 4) { P1(0, 2 * (i - 1)); P1(0, 2 * (i - 1) + 1); }
      if (i == 5) X1(0);
      if (i >= 6) P2H(0, i - 6);
      SB();
    }
#undef SFILL
#define SFILL { if (ks == 0) P2H(0, 12); if (ks == 1) P2H(0, 14); if (ks == 3) P3(0, 0); if (ks == 4) P3(0, 2); }
    UNROLL for (int ks = 0; ks < 6; ++ks) {
      S_STEP(1, ks);
      if (ks == 0) P2H(0, 13);
      if (ks == 1) P2H(0, 15);
      if (ks == 2) X2(0);
      if (ks == 3) P3(0, 1);
      if (ks == 4) P3(0, 3);
      SB();
    }
    {
      f32x16 v0, v1;
#define R2_FILL(g)                                                                                               \
  {                                                                                                              \
    if ((g) >= 1 && (g) <= 8) P1(1, (g) >= 1 && (g) <= 8 ? (g) - 1 : 0);                                         \
    if ((g) == 9) X1(1);                                                                                         \
    { const int H_ = (g) == 10 ? 0 : ((g) >= 14 && (g) <= 22 ? (g) - 13 : ((g) >= 26 && (g) <= 31 ? (g) - 16 : -1)); \
      if (H_ >= 0) P2H(1, H_ >= 0 ? H_ : 0); }                                                                   \
    if ((g) == 32) X2(1);                                                                                        \
    if ((g) == 33) P3(1, 0);                                                                                     \
    if ((g) == 34) P3(1, 1);                                                                                     \
    if ((g) == 11 || (g) == 23 || (g) == 35) { PIN(v0); vp[(g) / 12][0] = PACKS(v0, 0); vp[(g) / 12][1] = PACKS(v0, 1); } \
    if ((g) == 12 || (g) == 24) { PIN(v1); vp[(g) / 12 - 1][2] = PACKS(v1, 0); vp[(g) / 12 - 1][3] = PACKS(v1, 1); } \
  }
      UNROLL for (int j = 0; j < 18; ++j) {
        const int ks = j % 6, k = 36 + j;
        v0 = mfma32(x1[0][ks], wb[k & 3], ks == 0 ? zz : v0);
        SB();
        wb[(k + 3) & 3] = WLD(SW(k + 3));
        R2_FILL(2 * j);
        SB();
        v1 = mfma32(x1[1][ks], wb[k & 3], ks == 0 ? zz : v1);
        SB();
        R2_FILL(2 * j + 1);
        SB();
      }
#undef R2_FILL
      STAGE(0, 10);
      f32x16 oc[2];
      UNROLL for (int n = 0; n < 6; ++n) {
        const int q = n / 3, tt = n % 3;
        f32x16 o;
        UNROLL for (int g = 0; g < 4; ++g) {
          f32x4 r4 = mfma4(id0, id1, x1[q][2 * tt + (g >> 1)][2 * (g & 1)], x1[q][2 * tt + (g >> 1)][2 * (g & 1) + 1], z4);
          UNROLL for (int e = 0; e < 4; ++e) o[4 * g + e] = r4[e];
        }
        SB();
        if (n == 0) { PIN(v1); vp[2][2] = PACKS(v1, 0); }
        o = mfma32(vp[tt][0], pf[q][0], o);
        SB();
        if (n == 0) { PIN(v1); vp[2][3] = PACKS(v1, 1); }
        if (n == 1) P3(1, 2);
        if (n == 2) P3(1, 3);
        SB();
        o = mfma32(vp[tt][1], pf[q][1], o);
        SB();
        if (n > 0) { PIN(oc[(n + 1) & 1]); x1[(n - 1) / 3][2 * ((n - 1) % 3)] = PACKS(oc[(n + 1) & 1], 0); }
        SB();
        o = mfma32(vp[tt][2], pf[q][2], o);
        SB();
        if (n > 0) { PIN(oc[(n + 1) & 1]); x1[(n - 1) / 3][2 * ((n - 1) % 3) + 1] = PACKS(oc[(n + 1) & 1], 1); }
        SB();
        o = mfma32(vp[tt][3], pf[q][3], o);
        oc[n & 1] = o;
        SB();
      }
      wait_vm0();
      UNROLL for (int ks = 0; ks < 6; ++ks) xf[0][ks] = lds_ld(smem, XA(ks));
      MEMB();
      UNROLL for (int i = 0; i < 3; ++i) lds_st(smem, sl16 + i * 1024, pre[i]);
      MEMB();
      UNROLL for (int ks = 0; ks < 6; ++ks) xf[1][ks] = lds_ld(smem, t1adj + XA(ks));
      MEMB();
      x1[1][4] = PACKS(oc[1], 0);
      x1[1][5] = PACKS(oc[1], 1);
    }
    { int wy = win / 28, wx = win - wy * 28; ob = (unsigned)w * RS_IN + (unsigned)((wy * 8) * 224 + wx * 8) * 192u; }
    if (!more) {
      UNROLL for (int a = 0; a < 2; ++a) {
        UNROLL for (int ks = 0; ks < 6; ++ks) lds_st(smem, XA(ks), x1[a][ks]);
        MEMB();
        UNROLL for (int i = 0; i < 6; ++i) x1[a][i] = lds_ld(smem, sl16 + i * 1024);
        MEMB();
      }
      const __amdgpu_buffer_rsrc_t ors0 = __builtin_amdgcn_make_buffer_rsrc(out + (size_t)ob, 0, 0x41000, 0x00020000);
      const __amdgpu_buffer_rsrc_t ors1 = __builtin_amdgcn_make_buffer_rsrc(out + (size_t)(ob + ROWB), 0, 0x41000, 0x00020000);
      UNROLL for (int n = 0; n < 12; ++n) OSTORE(n);
      break;
    }
    win = nwin;
    ++iter;
  }
  *(unsigned*)(smem + CNT_OFF + 128 + wave * 4) = 0u;
}

extern "C" void kernel_launch(void* const* d_in, const int* in_sizes, int n_in,
                              void* d_out, int out_size, void* d_ws, size_t ws_size,
                              hipStream_t stream) {
  swin_kernel<<<NBLK + 8, 512, 0, stream>>>((const char*)d_in[0], (const char*)d_in[1], (const char*)d_in[2],
                                       (const char*)d_in[3], (const char*)d_in[4], (char*)d_out, (char*)d_ws);
}
```
